```python
import torch

def _softmax(x, axis=-1):
    return torch.softmax(x, dim=axis)

def _gelu(x):
    return torch.nn.functional.gelu(x, approximate='tanh')

def _silu(x):
    return torch.nn.functional.silu(x)

def _sigmoid(x):
    return torch.sigmoid(x)

def _layernorm(x, axis=-1, eps=1e-05):
    m = x.mean(dim=axis, keepdim=True)
    v = x.var(dim=axis, keepdim=True, unbiased=False)
    return (x - m) / torch.sqrt(v + eps)

def _scatter_add(tgt, idx, src, axis=0):
    return tgt.clone().index_add_(axis, idx, src)

def _segment_sum(src, seg, n):
    out = torch.zeros((n,) + src.shape[1:], dtype=src.dtype)
    return out.index_add_(0, seg, src)

def _pad(x, pw):
    flat = tuple((v for pair in reversed(pw) for v in pair))
    return torch.nn.functional.pad(x, flat)
kernel_name = 'adaLN_cond__gqa_block_cached__mixer_channel_mlp__decode'

def setup_inputs():

    _w = lambda a: (lambda t: t.to(torch.bfloat16) if t.is_floating_point() else t)(torch.as_tensor(a))

    def _A_setup():
        B, Sq, D = (2, 1, 1024)
        Hq, Hkv, Sk, Dh = (32, 4, 32768, 128)
        assert Hq % Hkv == 0, (Hq, Hkv)
        return {'x': _w((lambda *s: torch.randn(*s, dtype=torch.bfloat16) * 0.1)(B, Sq, D)), 'k_cache': _w((lambda *s: torch.randn(*s, dtype=torch.bfloat16) * 1.6)(B, Hkv, Sk, Dh)), 'v_cache': _w((lambda *s: torch.randn(*s, dtype=torch.bfloat16) * 0.8)(B, Hkv, Sk, Dh)), 'wq': _w((lambda *s: torch.randn(*s, dtype=torch.bfloat16) * 1.6)(D, Hq * Dh) * D ** (-0.5)), 'wo': _w((lambda *s: torch.randn(*s, dtype=torch.bfloat16) * 0.8)(Hq * Dh, D) * (Hq * Dh) ** (-0.5))}
    _A_in = _A_setup()

    def _B_setup():
        B, S, D, F = (2, 1, 1024, 3584)
        return {'x': _w((lambda *s: torch.randn(*s, dtype=torch.bfloat16) * 0.1)(B, S, D)), 'w1': _w((lambda *s: torch.randn(*s, dtype=torch.bfloat16) * 3.2)(D, F) * D ** (-0.5)), 'b1': _w((lambda *s: torch.randn(*s, dtype=torch.bfloat16) * 3.2)(F)), 'w2': _w((lambda *s: torch.randn(*s, dtype=torch.bfloat16) * 3.2)(F, D) * F ** (-0.5)), 'b2': _w((lambda *s: torch.randn(*s, dtype=torch.bfloat16) * 3.2)(D))}
    _B_in = _B_setup()
    return {'A_x': _A_in['x'], 'A_k_cache': _A_in['k_cache'], 'A_v_cache': _A_in['v_cache'], 'A_wq': _A_in['wq'], 'A_wo': _A_in['wo'], 'B_x': _B_in['x'], 'B_w1': _B_in['w1'], 'B_b1': _B_in['b1'], 'B_w2': _B_in['w2'], 'B_b2': _B_in['b2'], 'c': _w((lambda *s: torch.randn(*s, dtype=torch.bfloat16) * 0.2)(2, 1024) * 8.0), 'w_mod': _w((lambda *s: torch.randn(*s, dtype=torch.bfloat16) * 0.2)(1024, 6 * 1024) * 1024 ** (-0.5) * 8.0)}

def reference(A_x, A_k_cache, A_v_cache, A_wq, A_wo, B_x, B_w1, B_b1, B_w2, B_b2, c, w_mod):

    def _A_ref(x, k_cache, v_cache, wq, wo):
        B, Sq, D = x.shape
        Hq, Dh = (32, 128)
        Hkv = k_cache.shape[1]
        rep = Hq // Hkv
        q = (lambda x, *d: x.permute(*d))((x @ wq).reshape(B, Sq, Hq, Dh), 0, 2, 1, 3)
        k = (lambda x, n, axis: torch.repeat_interleave(x, n, dim=axis))(k_cache, rep, axis=1)
        v = (lambda x, n, axis: torch.repeat_interleave(x, n, dim=axis))(v_cache, rep, axis=1)
        scores = torch.einsum('bhqd,bhkd->bhqk', q, k) * Dh ** (-0.5)
        p = _softmax(scores, axis=-1)
        o = torch.einsum('bhqk,bhkd->bhqd', p, v)
        return (lambda x, *d: x.permute(*d))(o, 0, 2, 1, 3).reshape(B, Sq, Hq * Dh) @ wo

    def _B_ref(x, w1, b1, w2, b2):
        y = _layernorm(x, axis=-1)
        h = _gelu(torch.einsum('bsd,df->bsf', y, w1) + b1)
        return torch.einsum('bsf,fd->bsd', h, w2) + b2
    x = A_x
    mod = (_silu(c) @ w_mod).reshape(c.shape[0], 6, 1, -1)
    sa, ca, ga, sb, cb, gb = (mod[:, 0].clone(), mod[:, 1].clone(), mod[:, 2].clone(), mod[:, 3].clone(), mod[:, 4].clone(), mod[:, 5].clone())
    h = _layernorm(x, axis=-1) * (1 + ca) + sa
    x = x + ga * _A_ref(h, A_k_cache, A_v_cache, A_wq, A_wo)
    h = _layernorm(x, axis=-1) * (1 + cb) + sb
    x = x + gb * _B_ref(h, B_w1, B_b1, B_w2, B_b2)
    return x
```

```cpp
#include <hip/hip_runtime.h>
#include <hip/hip_bf16.h>
#include <stdint.h>

typedef unsigned v4u __attribute__((ext_vector_type(4)));
typedef float f32x4 __attribute__((ext_vector_type(4)));
typedef short bf16x8 __attribute__((ext_vector_type(8)));
typedef unsigned short u16;

constexpr int D = 1024, HQ = 32, HKV = 4, SK = 32768, DH = 128, FF = 3584;
constexpr int NSPLIT = 32;

__device__ __forceinline__ float bf2f(unsigned b) { return __uint_as_float(b << 16); }
__device__ __forceinline__ float lo2f(unsigned w) { return __uint_as_float(w << 16); }
__device__ __forceinline__ float hi2f(unsigned w) { return __uint_as_float(w & 0xffff0000u); }
__device__ __forceinline__ unsigned f2bf(float f) {
  __hip_bfloat16 h = __float2bfloat16(f);
  return (unsigned)__builtin_bit_cast(unsigned short, h);
}
__device__ __forceinline__ float rbf(float f) { return bf2f(f2bf(f)); }
__device__ __forceinline__ f32x4 mfma(v4u a, v4u b, f32x4 c) {
  return __builtin_amdgcn_mfma_f32_16x16x32_bf16(__builtin_bit_cast(bf16x8, a), __builtin_bit_cast(bf16x8, b), c, 0, 0, 0);
}

template <int K>
__device__ __forceinline__ void strip_gemv(const u16* W, int ldw, int n0, const float* in, float* red, float* res) {
  const int tid = threadIdx.x, half = tid & 1, r0 = tid >> 1;
  float acc[2][8];
#pragma unroll
  for (int b = 0; b < 2; ++b)
#pragma unroll
    for (int j = 0; j < 8; ++j) acc[b][j] = 0.f;
#pragma unroll 4
  for (int k = r0; k < K; k += 128) {
    const v4u w = *reinterpret_cast<const v4u*>(W + (size_t)k * ldw + n0 + 8 * half);
    const float a0 = in[k], a1 = in[K + k];
#pragma unroll
    for (int q = 0; q < 4; ++q) {
      const float wl = lo2f(w[q]), wh = hi2f(w[q]);
      acc[0][2 * q] += a0 * wl; acc[0][2 * q + 1] += a0 * wh;
      acc[1][2 * q] += a1 * wl; acc[1][2 * q + 1] += a1 * wh;
    }
  }
#pragma unroll
  for (int b = 0; b < 2; ++b)
#pragma unroll
    for (int j = 0; j < 8; ++j) red[r0 * 33 + b * 16 + half * 8 + j] = acc[b][j];
  __syncthreads();
  if (tid < 32) {
    float s = 0.f;
    for (int r = 0; r < 128; ++r) s += red[r * 33 + tid];
    res[tid] = s;
  }
  __syncthreads();
}

__device__ __forceinline__ float block_sum256(float v, float* sc) {
#pragma unroll
  for (int m = 32; m >= 1; m >>= 1) v += __shfl_xor(v, m);
  __syncthreads();
  if ((threadIdx.x & 63) == 0) sc[threadIdx.x >> 6] = v;
  __syncthreads();
  return sc[0] + sc[1] + sc[2] + sc[3];
}
__device__ __forceinline__ void layernorm2(float* v, float* sc) {
  for (int b = 0; b < 2; ++b) {
    float s = 0.f;
    for (int k = threadIdx.x; k < D; k += 256) s += v[b * D + k];
    const float mean = block_sum256(s, sc) * (1.f / D);
    float q = 0.f;
    for (int k = threadIdx.x; k < D; k += 256) { const float d = v[b * D + k] - mean; q += d * d; }
    const float var = block_sum256(q, sc) * (1.f / D);
    const float rs = 1.f / sqrtf(var + 1e-5f);
    for (int k = threadIdx.x; k < D; k += 256) v[b * D + k] = (v[b * D + k] - mean) * rs;
    __syncthreads();
  }
}

__global__ void __launch_bounds__(256) k_mod(const u16* c, const u16* wmod, float* mod) {
  __shared__ float in[2 * D]; __shared__ float red[128 * 33]; __shared__ float res[32];
  for (int i = threadIdx.x; i < 2 * D; i += 256) { const float x = bf2f(c[i]); in[i] = rbf(x / (1.f + __expf(-x))); }
  __syncthreads();
  strip_gemv<D>(wmod, 6 * D, blockIdx.x * 16, in, red, res);
  if (threadIdx.x < 32) mod[(threadIdx.x >> 4) * 6 * D + blockIdx.x * 16 + (threadIdx.x & 15)] = rbf(res[threadIdx.x]);
}

__global__ void __launch_bounds__(256) k_q(const u16* x, const float* mod, const u16* wq, u16* q) {
  __shared__ float in[2 * D]; __shared__ float red[128 * 33]; __shared__ float res[32]; __shared__ float sc[4];
  for (int i = threadIdx.x; i < 2 * D; i += 256) in[i] = bf2f(x[i]);
  __syncthreads();
  layernorm2(in, sc);
  for (int i = threadIdx.x; i < 2 * D; i += 256) { const int b = i >> 10, k = i & 1023;
    in[i] = rbf(in[i] * (1.f + mod[b * 6 * D + D + k]) + mod[b * 6 * D + k]); }
  __syncthreads();
  strip_gemv<D>(wq, HQ * DH, blockIdx.x * 16, in, red, res);
  if (threadIdx.x < 32) q[(threadIdx.x >> 4) * HQ * DH + blockIdx.x * 16 + (threadIdx.x & 15)] = (u16)f2bf(res[threadIdx.x]);
}

__global__ void __launch_bounds__(512, 2) k_attn(const u16* q, const u16* kc, const u16* vc, float* pO, float* pML) {
  __shared__ float sO[8][8][132]; __shared__ float sM[8][8], sL[8][8];
  const int tid = threadIdx.x, lane = tid & 63, w = __builtin_amdgcn_readfirstlane(tid >> 6);
  const int grp = blockIdx.x & 7, split = blockIdx.x >> 3, b = grp >> 2, hkv = grp & 3;
  const int c = lane & 15, g = lane >> 4;
  const size_t key0 = (size_t)grp * SK + split * 1024 + w * 128;
  const u16* kb = kc + key0 * DH; const u16* vb = vc + key0 * DH;
  v4u qf[4];
#pragma unroll
  for (int s = 0; s < 4; ++s) {
    v4u z = {0u, 0u, 0u, 0u};
    if (c < 8) z = *reinterpret_cast<const v4u*>(q + (size_t)b * HQ * DH + (hkv * 8 + c) * DH + 32 * s + 8 * g);
    qf[s] = z;
  }
  v4u kf[8][4];
#pragma unroll
  for (int t = 0; t < 8; ++t)
#pragma unroll
    for (int s = 0; s < 4; ++s) {
      const int key = 16 * t + 4 * (c & 3) + (c >> 2);
      kf[t][s] = __builtin_nontemporal_load(reinterpret_cast<const v4u*>(kb + key * DH + 32 * s + 8 * g));
    }
  __builtin_amdgcn_sched_barrier(0);
  f32x4 sc[8];
#pragma unroll
  for (int t = 0; t < 8; ++t) {
    f32x4 a = {0.f, 0.f, 0.f, 0.f};
#pragma unroll
    for (int s = 0; s < 4; ++s) a = mfma(kf[t][s], qf[s], a);
    sc[t] = a;
  }
  v4u vf[4][8];
#pragma unroll
  for (int u = 0; u < 4; ++u)
#pragma unroll
    for (int j = 0; j < 8; ++j)
      vf[u][j] = __builtin_nontemporal_load(reinterpret_cast<const v4u*>(vb + (32 * u + 4 * j + g) * DH + 8 * c));
  __builtin_amdgcn_sched_barrier(0);
  const float C2 = 0.08838834764831845f * 1.4426950408889634f;
  float m = -INFINITY;
#pragma unroll
  for (int t = 0; t < 8; ++t)
#pragma unroll
    for (int i = 0; i < 4; ++i) m = fmaxf(m, sc[t][i]);
  m = fmaxf(m, __shfl_xor(m, 16)); m = fmaxf(m, __shfl_xor(m, 32));
  const float mc = m * C2;
  float l = 0.f; unsigned pk[4][4];
#pragma unroll
  for (int u = 0; u < 4; ++u) {
    float p[8];
#pragma unroll
    for (int j = 0; j < 8; ++j) { p[j] = __builtin_amdgcn_exp2f(sc[2 * u + (j >> 2)][j & 3] * C2 - mc); l += p[j]; }
#pragma unroll
    for (int r = 0; r < 4; ++r) pk[u][r] = f2bf(p[2 * r]) | (f2bf(p[2 * r + 1]) << 16);
  }
  l += __shfl_xor(l, 16); l += __shfl_xor(l, 32);
  f32x4 o[8];
#pragma unroll
  for (int dd = 0; dd < 8; ++dd) o[dd] = f32x4{0.f, 0.f, 0.f, 0.f};
#pragma unroll
  for (int u = 0; u < 4; ++u) {
    const v4u pb = {pk[u][0], pk[u][1], pk[u][2], pk[u][3]};
#pragma unroll
    for (int dd = 0; dd < 8; ++dd) {
      v4u a;
#pragma unroll
      for (int r = 0; r < 4; ++r)
        a[r] = __builtin_amdgcn_perm(vf[u][2 * r + 1][dd >> 1], vf[u][2 * r][dd >> 1], (dd & 1) ? 0x07060302u : 0x05040100u);
      o[dd] = mfma(a, pb, o[dd]);
    }
  }
  if (c < 8) {
#pragma unroll
    for (int i = 0; i < 4; ++i)
#pragma unroll
      for (int dd = 0; dd < 8; ++dd) sO[w][c][32 * g + 8 * i + dd] = o[dd][i];
    if (g == 0) { sM[w][c] = mc; sL[w][c] = l; }
  }
  __syncthreads();
  {
    const int h = w, d = 2 * lane;
    float M = -INFINITY;
#pragma unroll
    for (int ww = 0; ww < 8; ++ww) M = fmaxf(M, sM[ww][h]);
    float L = 0.f, o0 = 0.f, o1 = 0.f;
#pragma unroll
    for (int ww = 0; ww < 8; ++ww) { const float f = __builtin_amdgcn_exp2f(sM[ww][h] - M);
      L += f * sL[ww][h]; o0 += f * sO[ww][h][d]; o1 += f * sO[ww][h][d + 1]; }
    const size_t row = ((size_t)grp * NSPLIT + split) * 8 + h;
    pO[row * DH + d] = o0; pO[row * DH + d + 1] = o1;
    if (lane == 0) { pML[row * 2] = M; pML[row * 2 + 1] = L; }
  }
}

__global__ void __launch_bounds__(128) k_comb(const float* pO, const float* pML, float* o) {
  const int grp = blockIdx.x >> 3, h = blockIdx.x & 7, d = threadIdx.x;
  float M = -INFINITY;
  for (int s = 0; s < NSPLIT; ++s) M = fmaxf(M, pML[(((size_t)grp * NSPLIT + s) * 8 + h) * 2]);
  float L = 0.f, a = 0.f;
  for (int s = 0; s < NSPLIT; ++s) { const size_t row = ((size_t)grp * NSPLIT + s) * 8 + h;
    const float f = __builtin_amdgcn_exp2f(pML[row * 2] - M); L += f * pML[row * 2 + 1]; a += f * pO[row * DH + d]; }
  const int b = grp >> 2, hkv = grp & 3;
  o[b * HQ * DH + (hkv * 8 + h) * DH + d] = rbf(a / L);
}

__global__ void __launch_bounds__(256) k_wo(const float* o, const u16* wo, const u16* x, const float* mod, float* x1) {
  __shared__ float in[2 * HQ * DH]; __shared__ float red[128 * 33]; __shared__ float res[32];
  for (int i = threadIdx.x; i < 2 * HQ * DH; i += 256) in[i] = o[i];
  __syncthreads();
  strip_gemv<HQ * DH>(wo, D, blockIdx.x * 16, in, red, res);
  if (threadIdx.x < 32) { const int b = threadIdx.x >> 4, n = blockIdx.x * 16 + (threadIdx.x & 15);
    x1[b * D + n] = rbf(bf2f(x[b * D + n]) + rbf(mod[b * 6 * D + 2 * D + n] * rbf(res[threadIdx.x]))); }
}

__global__ void __launch_bounds__(256) k_w1(const float* x1, const float* mod, const u16* w1, const u16* b1, float* h1) {
  __shared__ float in[2 * D]; __shared__ float red[128 * 33]; __shared__ float res[32]; __shared__ float sc[4];
  for (int i = threadIdx.x; i < 2 * D; i += 256) in[i] = x1[i];
  __syncthreads();
  layernorm2(in, sc);
  for (int i = threadIdx.x; i < 2 * D; i += 256) { const int b = i >> 10, k = i & 1023;
    in[i] = rbf(in[i] * (1.f + mod[b * 6 * D + 4 * D + k]) + mod[b * 6 * D + 3 * D + k]); }
  __syncthreads();
  layernorm2(in, sc);
  for (int i = threadIdx.x; i < 2 * D; i += 256) in[i] = rbf(in[i]);
  __syncthreads();
  strip_gemv<D>(w1, FF, blockIdx.x * 16, in, red, res);
  if (threadIdx.x < 32) { const int b = threadIdx.x >> 4, n = blockIdx.x * 16 + (threadIdx.x & 15);
    const float u = res[threadIdx.x] + bf2f(b1[n]);
    const float t = 0.7978845608028654f * (u + 0.044715f * u * u * u);
    h1[b * FF + n] = rbf(0.5f * u * (1.f + tanhf(t))); }
}

__global__ void __launch_bounds__(256) k_w2(const float* h1, const u16* w2, const u16* b2, const float* x1, const float* mod, u16* out) {
  __shared__ float in[2 * FF]; __shared__ float red[128 * 33]; __shared__ float res[32];
  for (int i = threadIdx.x; i < 2 * FF; i += 256) in[i] = h1[i];
  __syncthreads();
  strip_gemv<FF>(w2, D, blockIdx.x * 16, in, red, res);
  if (threadIdx.x < 32) { const int b = threadIdx.x >> 4, n = blockIdx.x * 16 + (threadIdx.x & 15);
    out[b * D + n] = (u16)f2bf(x1[b * D + n] + mod[b * 6 * D + 5 * D + n] * (res[threadIdx.x] + bf2f(b2[n]))); }
}

extern "C" void kernel_launch(void* const* d_in, const int* in_sizes, int n_in, void* d_out, int out_size, void* d_ws,
                              size_t ws_size, hipStream_t stream) {
  const u16* x = (const u16*)d_in[0]; const u16* kc = (const u16*)d_in[1]; const u16* vc = (const u16*)d_in[2];
  const u16* wq = (const u16*)d_in[3]; const u16* wo = (const u16*)d_in[4];
  const u16* w1 = (const u16*)d_in[6]; const u16* b1 = (const u16*)d_in[7]; const u16* w2 = (const u16*)d_in[8];
  const u16* b2 = (const u16*)d_in[9]; const u16* c = (const u16*)d_in[10]; const u16* wmod = (const u16*)d_in[11];
  char* ws = (char*)d_ws;
  float* mod = (float*)ws;
  u16* q = (u16*)(ws + 65536);
  float* o = (float*)(ws + 131072);
  float* x1 = (float*)(ws + 196608);
  float* h1 = (float*)(ws + 262144);
  float* pML = (float*)(ws + 327680);
  float* pO = (float*)(ws + 1048576);
  k_mod<<<384, 256, 0, stream>>>(c, wmod, mod);
  k_q<<<256, 256, 0, stream>>>(x, mod, wq, q);
  k_attn<<<256, 512, 0, stream>>>(q, kc, vc, pO, pML);
  k_comb<<<64, 128, 0, stream>>>(pO, pML, o);
  k_wo<<<64, 256, 0, stream>>>(o, wo, x, mod, x1);
  k_w1<<<224, 256, 0, stream>>>(x1, mod, w1, b1, h1);
  k_w2<<<64, 256, 0, stream>>>(h1, w2, b2, x1, mod, (u16*)d_out);
}
```

```cpp
#include <hip/hip_runtime.h>
#include <hip/hip_bf16.h>
#include <stdint.h>

#define LAS __attribute__((address_space(3)))
#define GAS __attribute__((address_space(1)))
typedef unsigned short u16;
typedef unsigned v4u __attribute__((ext_vector_type(4)));
typedef unsigned v2u __attribute__((ext_vector_type(2)));
typedef float f32x4 __attribute__((ext_vector_type(4)));
typedef short s16x4 __attribute__((ext_vector_type(4)));
typedef short bf16x8 __attribute__((ext_vector_type(8)));
typedef __bf16 bf16x2 __attribute__((ext_vector_type(2)));

namespace {

constexpr int D = 1024, SK = 32768, DH = 128, FF = 3584;
constexpr int NTHR = 640;
#define LOADW 8
#define VA_SPLIT 24
constexpr int VA = VA_SPLIT;

constexpr int WS_EPOCH = 0, WS_ALIVE = 4096, WS_BOX = 65536, BOX = 16384;
constexpr int BX_MC = 0, BX_Q = 1024, BX_MR = 2048, BX_ML = 3072, BX_O = 4096, BX_T1 = 8192, BX_T2 = 10240, BX_X1 = BX_T1 + 1024;
constexpr int WS_BYTES = WS_BOX + 256 * BOX;
constexpr int WS_DBG = 8 << 20;
constexpr unsigned SENT = 0xFFFFFFFFu;
constexpr unsigned SPIN_CAP = 1u << 14, LDS_CAP = 1u << 18;
constexpr int AUX_SC1 = 16;

constexpr int L_RING = 0, L_P = 131072, L_CTL = L_P + 16384;
constexpr int L_LANDED = L_CTL, L_PROG = L_CTL + 32, L_BAR = L_CTL + 64;
constexpr int L_SVEC = L_CTL + 256;
constexpr int L_HVEC = L_SVEC + 512;
constexpr int L_QVEC = L_HVEC + 512;
constexpr int L_STAGE = L_QVEC + 2048;
constexpr int L_ST1 = L_STAGE + 1024;
constexpr int L_ST2 = L_ST1 + 128;
constexpr int L_MLW = L_ST2 + 128;
constexpr int L_MLF = L_MLW + 512;
constexpr int L_TVEC = L_MLF + 64;
constexpr int L_UVEC = L_TVEC + 512;
constexpr int L_STAMP = L_UVEC + 512;
constexpr int LDS_BYTES = L_STAMP + 256;
static_assert(LDS_BYTES <= 160 * 1024, "LDS budget");

typedef LAS unsigned char* lptr;

__device__ __forceinline__ float lo2f(unsigned w) { return __uint_as_float(w << 16); }
__device__ __forceinline__ float hi2f(unsigned w) { return __uint_as_float(w & 0xffff0000u); }
__device__ __forceinline__ unsigned pk(float a, float b) {
  bf16x2 v = {(__bf16)a, (__bf16)b};
  return __builtin_bit_cast(unsigned, v);
}
__device__ __forceinline__ float rbf(float a) { return lo2f(pk(a, a)); }
__device__ __forceinline__ f32x4 mfma(v4u a, v4u b, f32x4 c) {
  return __builtin_amdgcn_mfma_f32_16x16x32_bf16(__builtin_bit_cast(bf16x8, a), __builtin_bit_cast(bf16x8, b), c, 0, 0, 0);
}
template <int CTRL> __device__ __forceinline__ float dpp(float x) {
  return __builtin_bit_cast(float, __builtin_amdgcn_mov_dpp(__builtin_bit_cast(int, x), CTRL, 0xf, 0xf, true));
}
constexpr int XOR1 = 0xB1, XOR2 = 0x4E, ROR4 = 0x124, ROR8 = 0x128;
__device__ __forceinline__ float sum16(float x) {
  auto s = __builtin_amdgcn_permlane16_swap(__float_as_uint(x), __float_as_uint(x), false, false);
  return __uint_as_float(s[0]) + __uint_as_float(s[1]);
}
__device__ __forceinline__ float sum32(float x) {
  auto s = __builtin_amdgcn_permlane32_swap(__float_as_uint(x), __float_as_uint(x), false, false);
  return __uint_as_float(s[0]) + __uint_as_float(s[1]);
}
__device__ __forceinline__ float max16(float x) {
  auto s = __builtin_amdgcn_permlane16_swap(__float_as_uint(x), __float_as_uint(x), false, false);
  return fmaxf(__uint_as_float(s[0]), __uint_as_float(s[1]));
}
__device__ __forceinline__ float max32(float x) {
  auto s = __builtin_amdgcn_permlane32_swap(__float_as_uint(x), __float_as_uint(x), false, false);
  return fmaxf(__uint_as_float(s[0]), __uint_as_float(s[1]));
}
__device__ __forceinline__ float sum_b345(float x) { x += dpp<ROR8>(x); return sum32(sum16(x)); }
__device__ __forceinline__ f32x4 sum_b345(f32x4 v) { return f32x4{sum_b345(v.x), sum_b345(v.y), sum_b345(v.z), sum_b345(v.w)}; }
__device__ __forceinline__ float sum_all(float x) {
  x += dpp<XOR1>(x); x += dpp<XOR2>(x); x += dpp<ROR4>(x); x += dpp<ROR8>(x);
  return sum32(sum16(x));
}
__device__ __forceinline__ float sum_nb2(float x) { x += dpp<XOR1>(x); x += dpp<XOR2>(x); return sum_b345(x); }
__device__ __forceinline__ f32x4 asf(v4u v) { return __builtin_bit_cast(f32x4, v); }
__device__ __forceinline__ v4u asu(f32x4 v) { return __builtin_bit_cast(v4u, v); }
__device__ __forceinline__ bool valid4(v4u v) { return v.x != SENT && v.y != SENT && v.z != SENT && v.w != SENT; }
__device__ __forceinline__ float expf2(float x) { return __builtin_amdgcn_exp2f(x * 1.4426950408889634f); }

#define LGKM0() asm volatile("s_waitcnt lgkmcnt(0)" ::: "memory")
#define VM0() asm volatile("s_waitcnt vmcnt(0)" ::: "memory")
#define CFENCE() asm volatile("" ::: "memory")

#define STAMP(i) do { } while (0)

__device__ __forceinline__ void dma16(unsigned voff, const unsigned char* sbase, unsigned ldsaddr) {
  unsigned keep;
  asm volatile("s_mov_b32 %0, m0\n\ts_mov_b32 m0, %3\n\ts_nop 4\n\tglobal_load_lds_dwordx4 %1, %2 nt\n\ts_mov_b32 m0, %0"
               : "=&s"(keep) : "v"(voff), "s"(sbase), "s"(ldsaddr) : "memory");
}

__device__ __forceinline__ unsigned lds_u32(lptr L, int off) {
  return (unsigned)__builtin_amdgcn_readfirstlane(*(volatile LAS unsigned*)(L + off));
}
__device__ __forceinline__ void wait_landed(lptr L, int n) {
  const unsigned need = (unsigned)(n >> 1) + 1u;
  for (unsigned sp = 0; lds_u32(L, L_LANDED + 4 * (n & 1)) < need && sp < (1u << 24); ++sp) { }
  CFENCE();
}
__device__ __forceinline__ void release(lptr L, int w, int lane, int n) {
  LGKM0();
  if (lane == 0) *(volatile LAS unsigned*)(L + L_PROG + 4 * w) = (unsigned)n;
}
__device__ __forceinline__ void cbar(lptr L, int lane, unsigned& target) {
  target += 8;
  LGKM0();
  if (lane == 0) __hip_atomic_fetch_add((LAS unsigned*)(L + L_BAR), 1u, __ATOMIC_RELAXED, __HIP_MEMORY_SCOPE_WORKGROUP);
  for (unsigned sp = 0; lds_u32(L, L_BAR) < target && sp < (1u << 24); ++sp) { }
  CFENCE();
}
__device__ __forceinline__ v4u tfrag(lptr L, int slot, int tl) {
  const s16x4 a = __builtin_amdgcn_ds_read_tr16_b64_v4i16((LAS s16x4*)(L + L_RING + slot * 8192 + tl));
  const s16x4 b = __builtin_amdgcn_ds_read_tr16_b64_v4i16((LAS s16x4*)(L + L_RING + slot * 8192 + tl + 1024));
  const v2u x = __builtin_bit_cast(v2u, a), y = __builtin_bit_cast(v2u, b);
  return v4u{x.x, x.y, y.x, y.y};
}
__device__ __forceinline__ v4u vfrag(lptr L, int off, int lane, int s) {
  return *(const LAS v4u*)(L + off + (lane & 1) * 256 + s * 64 + (lane >> 4) * 16);
}
template <int N>
__device__ __forceinline__ void gather(v4u (&v)[N], const int (&off)[N], __amdgpu_buffer_rsrc_t rs) {
  for (unsigned sp = 0;; ++sp) {
    bool ok = true;
#pragma unroll
    for (int q = 0; q < N; ++q) v[q] = __builtin_amdgcn_raw_buffer_load_b128(rs, off[q], 0, AUX_SC1);
#pragma unroll
    for (int q = 0; q < N; ++q) ok = ok && valid4(v[q]);
    if (__all(ok) || sp > SPIN_CAP) break;
    CFENCE();
  }
}
__device__ __forceinline__ int box(int j) { return WS_BOX + j * BOX; }
__device__ __forceinline__ void merge8(lptr L, int off, int b, float cnt, float& mean, float& var) {
  float m[8], M2 = 0.f, s = 0.f;
#pragma unroll
  for (int i = 0; i < 8; ++i) { const LAS float* r = (const LAS float*)(L + off + i * 16 + b * 8); m[i] = r[0]; M2 += r[1]; s += r[0]; }
  mean = s * 0.125f;
#pragma unroll
  for (int i = 0; i < 8; ++i) { const float d = m[i] - mean; M2 += cnt * d * d; }
  var = M2 * (1.f / D);
}
__device__ __forceinline__ void ln_consts(float mean, float var, float& m, float& rden) {
  m = rbf(mean);
  rden = 1.f / rbf(sqrtf(rbf(rbf(var) + 1e-5f)));
}
__device__ __forceinline__ float ln_apply(float x, float m, float rden) { return rbf(rbf(x - m) * rden); }

template <int W>
__device__ __forceinline__ void loader_seg(lptr L, unsigned lds0, int ld, int lane, const unsigned char* base, int ldb, int hi, int ss, int ch,
                                           int nblk, int& n, int& own, int& minprog) {
  const unsigned voff = (unsigned)((lane >> 4) * ldb + (((lane & 15) ^ ((lane >> 4) << 2)) << 4));
#pragma unroll 1
  for (int bb = 0; bb < nblk; ++bb, ++n) {
    if ((n & 1) != ld) continue;
    if (minprog < n - 15) {
      VM0();
      if (lane == 0) *(volatile LAS unsigned*)(L + L_LANDED + 4 * ld) = (unsigned)own;
      for (unsigned sp = 0; sp < (1u << 24); ++sp) {
        unsigned m = *(volatile LAS unsigned*)(L + L_PROG + 4 * (lane & 7));
        m = min(m, (unsigned)__builtin_amdgcn_mov_dpp((int)m, XOR1, 0xf, 0xf, true));
        m = min(m, (unsigned)__builtin_amdgcn_mov_dpp((int)m, XOR2, 0xf, 0xf, true));
        m = min(m, (unsigned)__builtin_amdgcn_mov_dpp((int)m, ROR4, 0xf, 0xf, true));
        minprog = __builtin_amdgcn_readfirstlane((int)m);
        if (minprog >= n - 15) break;
      }
    }
    const unsigned char* bp = base + (size_t)((bb & 3) * ss) + (size_t)((bb >> 2) * ch);
    const unsigned dst = lds0 + L_RING + (unsigned)(n & 15) * 8192u;
#pragma unroll
    for (int p = 0; p < 8; ++p) {
      dma16(voff, bp + (size_t)((p & 3) * 4) * ldb + (size_t)(p >> 2) * hi, dst + p * 1024);
      asm volatile("s_waitcnt vmcnt(%0)" ::"i"(W - 1) : "memory");
      if (((p + 1 - (W - 1)) & 7) == 0) {
        const int done = own + (p + 1 - (W - 1)) / 8 + ((p + 1 - (W - 1)) < 0 && ((p + 1 - (W - 1)) % 8) != 0 ? -1 : 0);
        if (lane == 0 && done > 0) *(volatile LAS unsigned*)(L + L_LANDED + 4 * ld) = (unsigned)done;
      }
    }
    ++own;
  }
}

__global__ void __launch_bounds__(NTHR) fused_block(const u16* __restrict__ x, const u16* __restrict__ kc, const u16* __restrict__ vc,
                                                     const u16* __restrict__ wq, const u16* __restrict__ wo, const u16* __restrict__ w1,
                                                     const u16* __restrict__ b1, const u16* __restrict__ w2, const u16* __restrict__ b2,
                                                     const u16* __restrict__ cvec, const u16* __restrict__ wmod, u16* __restrict__ out,
                                                     unsigned char* ws) {
  extern __shared__ __attribute__((aligned(16))) unsigned char smem[];
  lptr L = (lptr)smem;
  const int tid = threadIdx.x, lane = tid & 63, w = __builtin_amdgcn_readfirstlane(tid >> 6);
  const int j = blockIdx.x, grp = j & 7, split = j >> 3, batt = grp >> 2, hkv = grp & 3;
  const bool isA = split < 16, isC = split >= 30, isB = !isA && !isC;
  const int kq = split & 7, g32 = grp * 4 + (split >> 3), g16 = grp * 2 + ((split >> 3) & 1);
  const int offQ = isA ? 0 : 4, offMR = offQ + 4, offK = offMR + 4, offV = offK + 32;
  const int ai = grp * 16 + split, a_hkv = ai >> 5, a_dt = (ai >> 2) & 7, a_cg = ai & 3;
  const int bi = grp * 14 + (split - 16), ci = grp * 2 + (split - 30);

  if (w >= 8) {
    const int ld = w - 8;
    __builtin_amdgcn_s_setprio(3);
    const unsigned lds0 = (unsigned)(size_t)L;
    int n = 0, own = 0, minprog = 0;
    __builtin_amdgcn_s_barrier();
    STAMP(16);
    const unsigned char* wm = (const unsigned char*)wmod + (size_t)kq * 128 * 12288;
    if (!isA) loader_seg<LOADW>(L, lds0, ld, lane, wm + g16 * 256, 12288, 16 * 12288, 32 * 12288, 0, 4, n, own, minprog);
    loader_seg<LOADW>(L, lds0, ld, lane, (const unsigned char*)wq + (size_t)kq * 128 * 8192 + g32 * 256, 8192, 16 * 8192, 32 * 8192, 0, 4, n, own, minprog);
    loader_seg<LOADW>(L, lds0, ld, lane, wm + 4096 + g32 * 256, 12288, 16 * 12288, 32 * 12288, 0, 4, n, own, minprog);
    STAMP(17);
    const size_t kvoff = ((size_t)grp * SK + split * 1024) * 256;
    loader_seg<LOADW>(L, lds0, ld, lane, (const unsigned char*)kc + kvoff, 256, 4096, 8192, 32768, 32, n, own, minprog);
    STAMP(18);
    if (isA) {
      loader_seg<LOADW>(L, lds0, ld, lane, (const unsigned char*)vc + kvoff, 256, 4096, 8192, 32768, VA, n, own, minprog);
      loader_seg<LOADW>(L, lds0, ld, lane, (const unsigned char*)wo + ((size_t)(a_hkv * 8) * 128 + 16 * a_dt) * 2048 + a_cg * 512, 2048, 128 * 2048,
                        256 * 2048, 256, 8, n, own, minprog);
      loader_seg<LOADW>(L, lds0, ld, lane, (const unsigned char*)vc + kvoff + (size_t)VA * 8192, 256, 4096, 8192, 32768, 32 - VA, n, own, minprog);
    } else {
      loader_seg<LOADW>(L, lds0, ld, lane, (const unsigned char*)vc + kvoff, 256, 4096, 8192, 32768, 32, n, own, minprog);
    }
    STAMP(19);
    if (isB) {
      loader_seg<16>(L, lds0, ld, lane, (const unsigned char*)w1 + (size_t)(bi & 7) * 128 * 7168 + (bi >> 3) * 512, 7168, 16 * 7168, 32 * 7168, 256, 8, n,
                     own, minprog);
      loader_seg<16>(L, lds0, ld, lane, (const unsigned char*)w2 + (size_t)(bi >> 2) * 128 * 2048 + (bi & 3) * 512, 2048, 16 * 2048, 32 * 2048, 256, 8, n,
                     own, minprog);
    }
    VM0();
    if (lane == 0) *(volatile LAS unsigned*)(L + L_LANDED + 4 * ld) = (unsigned)own;
    STAMP(20);
    return;
  }

  const __amdgpu_buffer_rsrc_t rs = __builtin_amdgcn_make_buffer_rsrc((void*)ws, 0, WS_BYTES, 0x00020000);
  const int mybox = box(j);
  const int piece = lane & 7, sg = lane >> 3;
  unsigned bar = 0;
  STAMP(0);
  {
    const v4u s4 = {SENT, SENT, SENT, SENT};
    const int lo = isA ? 1 : 0, hi = isB ? 12 : 10;
    if (lo + w < hi) __builtin_amdgcn_raw_buffer_store_b128(s4, rs, mybox + (lo + w) * 1024 + lane * 16, 0, AUX_SC1);
    if (lo + w + 8 < hi) __builtin_amdgcn_raw_buffer_store_b128(s4, rs, mybox + (lo + w + 8) * 1024 + lane * 16, 0, AUX_SC1);
  }
  unsigned long long epoch = 0;
  if (w == 0) epoch = __hip_atomic_load((GAS unsigned long long*)(ws + WS_EPOCH), __ATOMIC_RELAXED, __HIP_MEMORY_SCOPE_AGENT);
  const v2u xr = *(const GAS v2u*)(x + tid * 4);
  const v2u xh = *(const GAS v2u*)(x + (piece >> 2) * D + kq * 128 + 16 * w + 4 * (piece & 3));
  v2u cr = {0u, 0u};
  if (w == 1) cr = *(const GAS v2u*)(cvec + (lane >> 5) * D + kq * 128 + 4 * (lane & 31));
  if (tid < 64) *(volatile LAS unsigned*)(L + L_CTL + 4 * tid) = 0u;
  LGKM0();
  __builtin_amdgcn_s_barrier();

  if (w == 1) {
    float s[4] = {lo2f(cr.x), hi2f(cr.x), lo2f(cr.y), hi2f(cr.y)};
#pragma unroll
    for (int i = 0; i < 4; ++i) s[i] = s[i] / (1.f + expf2(-s[i]));
    *(LAS v2u*)(L + L_SVEC + (lane >> 5) * 256 + (lane & 31) * 8) = v2u{pk(s[0], s[1]), pk(s[2], s[3])};
  }
  {
    const float a0 = lo2f(xr.x), a1 = hi2f(xr.x), a2 = lo2f(xr.y), a3 = hi2f(xr.y);
    const float mean = sum_all((a0 + a1) + (a2 + a3)) * (1.f / 256.f);
    const float d0 = a0 - mean, d1 = a1 - mean, d2 = a2 - mean, d3 = a3 - mean;
    const float M2 = sum_all((d0 * d0 + d1 * d1) + (d2 * d2 + d3 * d3));
    if (lane == 0) { LAS float* r = (LAS float*)(L + L_ST1 + w * 8); r[0] = mean; r[1] = M2; }
  }
  VM0();
  cbar(L, lane, bar);
  const unsigned long long tag = (epoch ^ 0x9E3779B97F4A7C15ull) * 0xD6E8FEB86659FD93ull + 1ull;
  if (tid == 0) __hip_atomic_store((GAS unsigned long long*)(ws + WS_ALIVE + j * 16), tag, __ATOMIC_RELAXED, __HIP_MEMORY_SCOPE_AGENT);
  float lnx[4];
  {
    const int b = piece >> 2;
    float mm[4], M2 = 0.f, s = 0.f;
#pragma unroll
    for (int i = 0; i < 4; ++i) { const LAS float* r = (const LAS float*)(L + L_ST1 + (b * 4 + i) * 8); mm[i] = r[0]; M2 += r[1]; s += r[0]; }
    const float mean = s * 0.25f;
#pragma unroll
    for (int i = 0; i < 4; ++i) { const float d = mm[i] - mean; M2 += 256.f * d * d; }
    float m, rden;
    ln_consts(mean, M2 * (1.f / D), m, rden);
    lnx[0] = ln_apply(lo2f(xh.x), m, rden); lnx[1] = ln_apply(hi2f(xh.x), m, rden);
    lnx[2] = ln_apply(lo2f(xh.y), m, rden); lnx[3] = ln_apply(hi2f(xh.y), m, rden);
  }
  const int tl = (8 * (lane >> 4) + ((lane >> 2) & 3)) * 256 + (((2 * w + ((lane & 3) >> 1)) ^ (((lane >> 2) & 3) << 2)) << 4) + (lane & 1) * 8;
  const int pubo = w * 128 + (lane & 15) * 64 + (lane >> 4) * 16;
  STAMP(1);

  if (!isA) {
    f32x4 acc = {0.f, 0.f, 0.f, 0.f};
#pragma unroll
    for (int s = 0; s < 4; ++s) { wait_landed(L, s); acc = mfma(tfrag(L, s, tl), vfrag(L, L_SVEC, lane, s), acc); }
    release(L, w, lane, 4);
    if ((lane & 15) < 2) __builtin_amdgcn_raw_buffer_store_b128(asu(acc), rs, mybox + BX_MC + pubo, 0, AUX_SC1);
  }
  STAMP(2);
  v4u qw[4];
#pragma unroll
  for (int s = 0; s < 4; ++s) { wait_landed(L, offQ + s); qw[s] = tfrag(L, (offQ + s) & 15, tl); }
  release(L, w, lane, offMR);
  {
    f32x4 acc = {0.f, 0.f, 0.f, 0.f};
#pragma unroll
    for (int s = 0; s < 4; ++s) { wait_landed(L, offMR + s); acc = mfma(tfrag(L, (offMR + s) & 15, tl), vfrag(L, L_SVEC, lane, s), acc); }
    release(L, w, lane, offK + w);
    if ((lane & 15) < 2) __builtin_amdgcn_raw_buffer_store_b128(asu(acc), rs, mybox + BX_MR + pubo, 0, AUX_SC1);
  }
  STAMP(3);
  if (w == 0) {
    for (unsigned sp = 0;; ++sp) {
      bool ok = true;
#pragma unroll
      for (int q = 0; q < 4; ++q) {
        const v2u t = __builtin_amdgcn_raw_buffer_load_b64(rs, WS_ALIVE + (q * 64 + lane) * 16, 0, AUX_SC1);
        ok = ok && t.x == (unsigned)tag && t.y == (unsigned)(tag >> 32);
      }
      if (__all(ok) || sp > SPIN_CAP) break;
      CFENCE();
    }
    if (j == 0 && lane == 0) __hip_atomic_store((GAS unsigned long long*)(ws + WS_EPOCH), epoch + 1ull, __ATOMIC_RELAXED, __HIP_MEMORY_SCOPE_AGENT);
  }
  cbar(L, lane, bar);
  STAMP(4);
  {
    v4u g[2];
    const int js = (16 + (kq & 1) * 8 + sg) * 8 + (kq >> 1), jc = (16 + (kq & 1) * 8 + sg) * 8 + 4 + (kq >> 1);
    const int off[2] = {box(js) + BX_MC + w * 128 + piece * 16, box(jc) + BX_MC + w * 128 + piece * 16};
    gather<2>(g, off, rs);
    const f32x4 sa = sum_b345(asf(g[0])), ca = sum_b345(asf(g[1]));
    float h[4];
#pragma unroll
    for (int i = 0; i < 4; ++i) h[i] = rbf(rbf(lnx[i] * rbf(1.f + rbf(ca[i]))) + rbf(sa[i]));
    if (lane < 8) *(LAS v2u*)(L + L_HVEC + (piece >> 2) * 256 + (16 * w + 4 * (piece & 3)) * 2) = v2u{pk(h[0], h[1]), pk(h[2], h[3])};
    cbar(L, lane, bar);
    STAMP(5);
    f32x4 acc = {0.f, 0.f, 0.f, 0.f};
#pragma unroll
    for (int s = 0; s < 4; ++s) acc = mfma(qw[s], vfrag(L, L_HVEC, lane, s), acc);
    if ((lane & 15) < 2) *(LAS f32x4*)(L + L_STAGE + (lane & 15) * 512 + (16 * w + 4 * (lane >> 4)) * 4) = acc;
    cbar(L, lane, bar);
    if (w == 0) __builtin_amdgcn_raw_buffer_store_b128(*(const LAS v4u*)(L + L_STAGE + lane * 16), rs, mybox + BX_Q + lane * 16, 0, AUX_SC1);
  }
  STAMP(6);
  v4u qf[4];
  {
    v4u g[4]; int off[4];
    const int head = hkv * 8 + w;
#pragma unroll
    for (int q = 0; q < 4; ++q) {
      const int jq = ((head & 3) * 8 + 2 * q + (lane >> 5)) * 8 + (head >> 2);
      off[q] = box(jq) + BX_Q + batt * 512 + (lane & 31) * 16;
    }
    gather<4>(g, off, rs);
    const f32x4 t = (asf(g[0]) + asf(g[1])) + (asf(g[2]) + asf(g[3]));
    const float q0 = sum32(t.x), q1 = sum32(t.y), q2 = sum32(t.z), q3 = sum32(t.w);
    if (lane < 32) *(LAS v2u*)(L + L_QVEC + w * 256 + lane * 8) = v2u{pk(q0, q1), pk(q2, q3)};
    cbar(L, lane, bar);
#pragma unroll
    for (int s = 0; s < 4; ++s) qf[s] = *(const LAS v4u*)(L + L_QVEC + (lane & 7) * 256 + s * 64 + (lane >> 4) * 16);
  }
  STAMP(7);
  float sc[4][8];
  {
    const int m = lane & 15;
    const int kl = (8 * (m >> 2) + (m & 3)) * 256 + ((m & 3) << 6) + (lane >> 4) * 16;
#pragma unroll
    for (int i = 0; i < 4; ++i) {
      const int n = offK + w + 8 * i;
      wait_landed(L, n);
      v4u kf[2][4];
#pragma unroll
      for (int T = 0; T < 2; ++T)
#pragma unroll
        for (int s = 0; s < 4; ++s) kf[T][s] = *(const LAS v4u*)(L + L_RING + (n & 15) * 8192 + T * 1024 + (kl ^ (s << 6)));
      release(L, w, lane, i < 3 ? n + 8 : offV);
#pragma unroll
      for (int T = 0; T < 2; ++T) {
        f32x4 a = {0.f, 0.f, 0.f, 0.f};
#pragma unroll
        for (int s = 0; s < 4; ++s) a = mfma(kf[T][s], qf[s], a);
#pragma unroll
        for (int r = 0; r < 4; ++r) sc[i][T * 4 + r] = rbf(rbf(a[r]) * 0.08838834764831845f);
      }
    }
  }
  STAMP(8);
  {
    float mw = sc[0][0];
#pragma unroll
    for (int i = 0; i < 4; ++i)
#pragma unroll
      for (int r = 0; r < 8; ++r) mw = fmaxf(mw, sc[i][r]);
    mw = max32(max16(mw));
    float lw = 0.f;
#pragma unroll
    for (int i = 0; i < 4; ++i)
#pragma unroll
      for (int r = 0; r < 8; ++r) { sc[i][r] = expf2(sc[i][r] - mw); lw += sc[i][r]; }
    lw = sum32(sum16(lw));
    if (lane < 8) *(LAS v2u*)(L + L_MLW + w * 64 + lane * 8) = v2u{__float_as_uint(mw), __float_as_uint(lw)};
    cbar(L, lane, bar);
    if (w == 0) {
      const int h = lane & 7;
      float mm[8], M = -INFINITY, Ls = 0.f;
#pragma unroll
      for (int i = 0; i < 8; ++i) { mm[i] = *(const LAS float*)(L + L_MLW + i * 64 + h * 8); M = fmaxf(M, mm[i]); }
#pragma unroll
      for (int i = 0; i < 8; ++i) Ls += *(const LAS float*)(L + L_MLW + i * 64 + h * 8 + 4) * expf2(mm[i] - M);
      if (lane < 8) __builtin_amdgcn_raw_buffer_store_b64(v2u{__float_as_uint(M), __float_as_uint(Ls)}, rs, mybox + BX_ML + lane * 8, 0, AUX_SC1);
      v4u g[2];
      const int off[2] = {box((lane >> 2) * 8 + grp) + BX_ML + (lane & 3) * 16, box(((lane >> 2) + 16) * 8 + grp) + BX_ML + (lane & 3) * 16};
      gather<2>(g, off, rs);
      const f32x4 u = asf(g[0]), v = asf(g[1]);
      float M0 = fmaxf(u.x, v.x), M1 = fmaxf(u.z, v.z);
      M0 = fmaxf(M0, dpp<ROR4>(M0)); M0 = fmaxf(M0, dpp<ROR8>(M0)); M0 = max32(max16(M0));
      M1 = fmaxf(M1, dpp<ROR4>(M1)); M1 = fmaxf(M1, dpp<ROR8>(M1)); M1 = max32(max16(M1));
      float L0 = u.y * expf2(u.x - M0) + v.y * expf2(v.x - M0), L1 = u.w * expf2(u.z - M1) + v.w * expf2(v.z - M1);
      L0 += dpp<ROR4>(L0); L0 += dpp<ROR8>(L0); L0 = sum32(sum16(L0));
      L1 += dpp<ROR4>(L1); L1 += dpp<ROR8>(L1); L1 = sum32(sum16(L1));
      if (lane < 4) *(LAS f32x4*)(L + L_MLF + lane * 16) = f32x4{M0, 1.f / L0, M1, 1.f / L1};
    }
    cbar(L, lane, bar);
    STAMP(9);
    const float Mg = *(const LAS float*)(L + L_MLF + (lane & 7) * 8), rL = *(const LAS float*)(L + L_MLF + (lane & 7) * 8 + 4);
    const float f = expf2(mw - Mg) * rL;
#pragma unroll
    for (int i = 0; i < 4; ++i) {
      const v4u p = {pk(sc[i][0] * f, sc[i][1] * f), pk(sc[i][2] * f, sc[i][3] * f), pk(sc[i][4] * f, sc[i][5] * f), pk(sc[i][6] * f, sc[i][7] * f)};
      if ((lane & 15) < 8) *(LAS v4u*)(L + L_P + (w + 8 * i) * 512 + ((lane >> 4) * 8 + (lane & 7)) * 16) = p;
    }
    cbar(L, lane, bar);
  }
  STAMP(10);
  f32x4 ga = {0.f, 0.f, 0.f, 0.f}, gb = {0.f, 0.f, 0.f, 0.f};
  v2u xres = {0u, 0u}, bias2 = {0u, 0u};
  if (isC) {
    v4u g[1];
    { const int g3 = 8 + ci; const int off[1] = {box(((g3 & 3) * 8 + sg) * 8 + (g3 >> 2)) + BX_MR + w * 128 + piece * 16};
      gather<1>(g, off, rs);
      const f32x4 t = sum_b345(asf(g[0]));
      if (lane < 8) __builtin_amdgcn_raw_buffer_store_b128(asu(f32x4{rbf(t.x), rbf(t.y), rbf(t.z), rbf(t.w)}), rs, mybox + BX_T1 + w * 128 + piece * 16, 0, AUX_SC1); }
    v4u g2[2];
    const int ga3 = ci >> 1, gb3 = 24 + (ci >> 1), line = (ci & 1) * 4 + (w & 3);
    const int off2[2] = {box(((ga3 & 3) * 8 + sg) * 8 + (ga3 >> 2)) + BX_MR + line * 128 + piece * 16,
                         box(((gb3 & 3) * 8 + sg) * 8 + (gb3 >> 2)) + BX_MR + line * 128 + piece * 16};
    gather<2>(g2, off2, rs);
    const f32x4 ta = sum_b345(asf(g2[0])), tb = sum_b345(asf(g2[1]));
    ga = f32x4{rbf(ta.x), rbf(ta.y), rbf(ta.z), rbf(ta.w)};
    gb = f32x4{rbf(tb.x), rbf(tb.y), rbf(tb.z), rbf(tb.w)};
    const int col = 64 * ci + 16 * (w & 3) + 4 * (piece & 3);
    xres = *(const GAS v2u*)(x + (piece >> 2) * D + col);
    bias2 = *(const GAS v2u*)(b2 + col);
  }
  f32x4 oacc = {0.f, 0.f, 0.f, 0.f};
  v4u tw[8];
  f32x4 sbv = {0.f, 0.f, 0.f, 0.f}, cb1 = {0.f, 0.f, 0.f, 0.f};
  v2u bias1 = {0u, 0u};
  const int pl = L_P + ((lane >> 4) * 8 + (lane & 7)) * 16;
  auto pv = [&](int i0, int i1, int shift) {
    for (int i = i0; i < i1; i += 2) {
      const int n = offV + shift + i;
      wait_landed(L, n); wait_landed(L, n + 1);
      const v4u a0 = tfrag(L, n & 15, tl), a1 = tfrag(L, (n + 1) & 15, tl);
      const v4u p0 = *(const LAS v4u*)(L + pl + i * 512), p1 = *(const LAS v4u*)(L + pl + (i + 1) * 512);
      release(L, w, lane, n + 2);
      oacc = mfma(a0, p0, oacc);
      oacc = mfma(a1, p1, oacc);
    }
  };
  if (isA) {
    pv(0, VA, 0);
#pragma unroll
    for (int q = 0; q < 8; ++q) { wait_landed(L, offV + VA + q); tw[q] = tfrag(L, (offV + VA + q) & 15, tl); }
    release(L, w, lane, offV + VA + 8);
    pv(VA, 32, 8);
  } else {
    pv(0, 16, 0);
    if (isB) {
      v4u g[2];
      const int off[2] = {box(30 * 8 + (w >> 1) + (w & 1) * 8) + BX_T1 + lane * 16, box(30 * 8 + ((8 + w) >> 1) + (w & 1) * 8) + BX_T1 + lane * 16};
      gather<2>(g, off, rs);
      sbv = asf(g[0]);
      const f32x4 cb = asf(g[1]);
      cb1 = f32x4{rbf(1.f + cb.x), rbf(1.f + cb.y), rbf(1.f + cb.z), rbf(1.f + cb.w)};
      bias1 = *(const GAS v2u*)(b1 + (bi >> 2) * 128 + 16 * w + 4 * (piece & 3));
    }
    pv(16, 32, 0);
  }
  if ((lane & 15) < 8) __builtin_amdgcn_raw_buffer_store_b128(asu(oacc), rs, mybox + BX_O + w * 512 + ((lane & 15) * 4 + (lane >> 4)) * 16, 0, AUX_SC1);
  STAMP(11);

  if (isA) {
    v4u g[4]; int off[4];
#pragma unroll
    for (int q = 0; q < 4; ++q) off[q] = box((sg + 8 * q) * 8 + (w >> 2) * 4 + a_hkv) + BX_O + a_dt * 512 + (w & 3) * 128 + piece * 16;
    gather<4>(g, off, rs);
    const f32x4 t = sum_b345((asf(g[0]) + asf(g[1])) + (asf(g[2]) + asf(g[3])));
    if (lane < 8) *(LAS v2u*)(L + L_TVEC + (w >> 2) * 256 + (32 * (w & 3) + 4 * piece) * 2) = v2u{pk(t.x, t.y), pk(t.z, t.w)};
    cbar(L, lane, bar);
    STAMP(12);
    f32x4 a0 = {0.f, 0.f, 0.f, 0.f}, a1 = {0.f, 0.f, 0.f, 0.f};
#pragma unroll
    for (int s = 0; s < 4; ++s) { const v4u bv = vfrag(L, L_TVEC, lane, s); a0 = mfma(tw[s], bv, a0); a1 = mfma(tw[4 + s], bv, a1); }
    if ((lane & 15) < 2) {
      __builtin_amdgcn_raw_buffer_store_b128(asu(a0), rs, mybox + BX_T1 + pubo, 0, AUX_SC1);
      __builtin_amdgcn_raw_buffer_store_b128(asu(a1), rs, mybox + BX_T1 + 1024 + pubo, 0, AUX_SC1);
    }
    STAMP(13);
  } else if (isC) {
    if (w < 4) {
      const int ct = 4 * ci + w, cg = ct >> 4, ti = ct & 15;
      v4u g[4]; int off[4];
#pragma unroll
      for (int q = 0; q < 4; ++q) {
        const int pr = sg + 8 * q, a = pr * 4 + cg;
        off[q] = box((a & 15) * 8 + (a >> 4)) + BX_T1 + ti * 128 + piece * 16;
      }
      gather<4>(g, off, rs);
      const f32x4 t = sum_b345((asf(g[0]) + asf(g[1])) + (asf(g[2]) + asf(g[3])));
      const float xr4[4] = {lo2f(xres.x), hi2f(xres.x), lo2f(xres.y), hi2f(xres.y)};
      f32x4 x1;
#pragma unroll
      for (int i = 0; i < 4; ++i) x1[i] = rbf(xr4[i] + rbf(ga[i] * rbf(t[i])));
      if (lane < 8) __builtin_amdgcn_raw_buffer_store_b128(asu(x1), rs, mybox + BX_X1 + w * 128 + piece * 16, 0, AUX_SC1);
      STAMP(12);
#pragma unroll
      for (int q = 0; q < 4; ++q) {
        const int kf = sg + 8 * q, bb = kf * 4 + cg;
        const int gg = bb / 14;
        off[q] = kf < 28 ? box((16 + bb - gg * 14) * 8 + gg) + BX_T2 + ti * 128 + piece * 16 : WS_BYTES;
      }
      gather<4>(g, off, rs);
      const f32x4 z = sum_b345((asf(g[0]) + asf(g[1])) + (asf(g[2]) + asf(g[3])));
      const float bb4[4] = {lo2f(bias2.x), hi2f(bias2.x), lo2f(bias2.y), hi2f(bias2.y)};
      float o[4];
#pragma unroll
      for (int i = 0; i < 4; ++i) o[i] = x1[i] + rbf(gb[i] * rbf(rbf(z[i]) + bb4[i]));
      if (lane < 8) *(GAS v2u*)(out + (piece >> 2) * D + 64 * ci + 16 * w + 4 * (piece & 3)) = v2u{pk(o[0], o[1]), pk(o[2], o[3])};
      STAMP(13);
    }
  } else {
#pragma unroll
    for (int q = 0; q < 8; ++q) { wait_landed(L, offV + 32 + q); tw[q] = tfrag(L, (offV + 32 + q) & 15, tl); }
    v4u g[1];
    { const int cc = 2 * w + (sg >> 2);
      const int off[1] = {box((30 + (cc & 1)) * 8 + (cc >> 1)) + BX_X1 + (sg & 3) * 128 + piece * 16};
      gather<1>(g, off, rs); }
    STAMP(12);
    const f32x4 x1 = asf(g[0]);
    const int b = piece >> 2;
    float m, rden;
    {
      const float mean = sum_nb2((x1.x + x1.y) + (x1.z + x1.w)) * (1.f / 128.f);
      const f32x4 d = x1 - mean;
      const float M2 = sum_nb2((d.x * d.x + d.y * d.y) + (d.z * d.z + d.w * d.w));
      if (lane == 0 || lane == 4) { LAS float* r = (LAS float*)(L + L_ST1 + w * 16 + b * 8); r[0] = mean; r[1] = M2; }
      cbar(L, lane, bar);
      float mu, var;
      merge8(L, L_ST1, b, 128.f, mu, var);
      ln_consts(mu, var, m, rden);
    }
    f32x4 h2;
#pragma unroll
    for (int i = 0; i < 4; ++i) h2[i] = rbf(rbf(ln_apply(x1[i], m, rden) * cb1[i]) + sbv[i]);
    {
      const float mean = sum_nb2((h2.x + h2.y) + (h2.z + h2.w)) * (1.f / 128.f);
      const f32x4 d = h2 - mean;
      const float M2 = sum_nb2((d.x * d.x + d.y * d.y) + (d.z * d.z + d.w * d.w));
      if (lane == 0 || lane == 4) { LAS float* r = (LAS float*)(L + L_ST2 + w * 16 + b * 8); r[0] = mean; r[1] = M2; }
      cbar(L, lane, bar);
      float mu, var;
      merge8(L, L_ST2, b, 128.f, mu, var);
      ln_consts(mu, var, m, rden);
    }
    if (w == (bi & 7))
      *(LAS v2u*)(L + L_TVEC + b * 256 + (16 * sg + 4 * (piece & 3)) * 2) =
          v2u{pk(ln_apply(h2.x, m, rden), ln_apply(h2.y, m, rden)), pk(ln_apply(h2.z, m, rden), ln_apply(h2.w, m, rden))};
    cbar(L, lane, bar);
    {
      f32x4 a0 = {0.f, 0.f, 0.f, 0.f}, a1 = {0.f, 0.f, 0.f, 0.f};
#pragma unroll
      for (int s = 0; s < 4; ++s) { const v4u bv = vfrag(L, L_TVEC, lane, s); a0 = mfma(tw[s], bv, a0); a1 = mfma(tw[4 + s], bv, a1); }
      if ((lane & 15) < 2) {
        __builtin_amdgcn_raw_buffer_store_b128(asu(a0), rs, mybox + BX_T1 + pubo, 0, AUX_SC1);
        __builtin_amdgcn_raw_buffer_store_b128(asu(a1), rs, mybox + BX_T1 + 1024 + pubo, 0, AUX_SC1);
      }
    }
    STAMP(13);
#pragma unroll
    for (int q = 0; q < 8; ++q) { wait_landed(L, offV + 40 + q); tw[q] = tfrag(L, (offV + 40 + q) & 15, tl); }
    {
      const int kf = bi >> 2, cg = kf >> 1, bb = cg * 8 + sg, gg = bb / 14;
      const int off[1] = {box((16 + bb - gg * 14) * 8 + gg) + BX_T1 + ((kf & 1) * 8 + w) * 128 + piece * 16};
      gather<1>(g, off, rs);
      const f32x4 t = sum_b345(asf(g[0]));
      const float bb4[4] = {lo2f(bias1.x), hi2f(bias1.x), lo2f(bias1.y), hi2f(bias1.y)};
      float hh[4];
#pragma unroll
      for (int i = 0; i < 4; ++i) {
        const float u = rbf(rbf(t[i]) + bb4[i]);
        const float z = 0.7978845608028654f * (u + 0.044715f * u * u * u);
        hh[i] = u / (1.f + expf2(-2.f * z));
      }
      if (lane < 8) *(LAS v2u*)(L + L_UVEC + (piece >> 2) * 256 + (16 * w + 4 * (piece & 3)) * 2) = v2u{pk(hh[0], hh[1]), pk(hh[2], hh[3])};
      cbar(L, lane, bar);
      STAMP(14);
      f32x4 a0 = {0.f, 0.f, 0.f, 0.f}, a1 = {0.f, 0.f, 0.f, 0.f};
#pragma unroll
      for (int s = 0; s < 4; ++s) { const v4u bv = vfrag(L, L_UVEC, lane, s); a0 = mfma(tw[s], bv, a0); a1 = mfma(tw[4 + s], bv, a1); }
      if ((lane & 15) < 2) {
        __builtin_amdgcn_raw_buffer_store_b128(asu(a0), rs, mybox + BX_T2 + pubo, 0, AUX_SC1);
        __builtin_amdgcn_raw_buffer_store_b128(asu(a1), rs, mybox + BX_T2 + 1024 + pubo, 0, AUX_SC1);
      }
    }
  }
  STAMP(15);
}

}

extern "C" void kernel_launch(void* const* d_in, const int* in_sizes, int n_in, void* d_out, int out_size, void* d_ws, size_t ws_size,
                              hipStream_t stream) {
  (void)in_sizes; (void)n_in; (void)out_size; (void)ws_size;
  (void)hipFuncSetAttribute((const void*)fused_block, hipFuncAttributeMaxDynamicSharedMemorySize, LDS_BYTES);
  hipLaunchKernelGGL(fused_block, dim3(256), dim3(NTHR), LDS_BYTES, stream, (const u16*)d_in[0], (const u16*)d_in[1], (const u16*)d_in[2],
                     (const u16*)d_in[3], (const u16*)d_in[4], (const u16*)d_in[6], (const u16*)d_in[7], (const u16*)d_in[8],
                     (const u16*)d_in[9], (const u16*)d_in[10], (const u16*)d_in[11], (u16*)d_out, (unsigned char*)d_ws);
}
```

```cpp
#include <hip/hip_runtime.h>
#include <hip/hip_bf16.h>
#include <stdint.h>

#define LAS __attribute__((address_space(3)))
#define GAS __attribute__((address_space(1)))
typedef unsigned short u16;
typedef unsigned v4u __attribute__((ext_vector_type(4)));
typedef unsigned v2u __attribute__((ext_vector_type(2)));
typedef float f32x4 __attribute__((ext_vector_type(4)));
typedef float f32x2 __attribute__((ext_vector_type(2)));
typedef short s16x4 __attribute__((ext_vector_type(4)));
typedef short bf16x8 __attribute__((ext_vector_type(8)));
typedef __bf16 bf16x2 __attribute__((ext_vector_type(2)));

namespace {

constexpr int D = 1024, SK = 32768;
constexpr int NTHR = 704;
#define PREFETCH_CODE 22528
#define LOADW 10
#define LOADW2 16

constexpr int WS_EPOCH = 0, WS_ALIVE = 4096, WS_BOX = 65536, BOX = 16384;
constexpr int BX_MC = 0, BX_Q = 1024, BX_MR = 2048, BX_ML = 3072, BX_O = 4096, BX_T1 = 8192, BX_T2 = 10240, BX_X1 = BX_T1 + 1024;
constexpr int WS_BYTES = WS_BOX + 256 * BOX;
constexpr int WS_DBG = 8 << 20;
constexpr unsigned SENT = 0xFFFFFFFFu;
constexpr unsigned SPIN_CAP = 1u << 14, LDS_CAP = 1u << 18;
constexpr int AUX_SC1 = 16;

constexpr int L_RING = 0, L_P = 131072, L_CTL = L_P + 16384;
constexpr int L_LANDED = L_CTL, L_PROG = L_CTL + 32, L_BAR = L_CTL + 64, L_MLCNT = L_CTL + 68, L_FALIVE = L_CTL + 72, L_FML = L_CTL + 76;
constexpr int L_SVEC = L_CTL + 256;
constexpr int L_HVEC = L_SVEC + 512;
constexpr int L_QVEC = L_HVEC + 512;
constexpr int L_ST1 = L_QVEC + 2048;
constexpr int L_ST2 = L_ST1 + 128;
constexpr int L_MLW = L_ST2 + 128;
constexpr int L_MLF = L_MLW + 512;
constexpr int L_TVEC = L_MLF + 64;
constexpr int L_UVEC = L_TVEC + 512;
constexpr int L_STAMP = L_UVEC + 512;
constexpr int LDS_BYTES = L_STAMP + 320;
static_assert(LDS_BYTES <= 160 * 1024, "LDS budget");

typedef LAS unsigned char* lptr;

__device__ __forceinline__ float lo2f(unsigned w) { return __uint_as_float(w << 16); }
__device__ __forceinline__ float hi2f(unsigned w) { return __uint_as_float(w & 0xffff0000u); }
__device__ __forceinline__ unsigned pk(float a, float b) {
  bf16x2 v = {(__bf16)a, (__bf16)b};
  return __builtin_bit_cast(unsigned, v);
}
__device__ __forceinline__ float rbf(float a) { return hi2f(pk(a, a)); }
__device__ __forceinline__ f32x2 rbf2(f32x2 a) { const unsigned p = pk(a.x, a.y); return f32x2{lo2f(p), hi2f(p)}; }
__device__ __forceinline__ f32x4 mfma(v4u a, v4u b, f32x4 c) {
  return __builtin_amdgcn_mfma_f32_16x16x32_bf16(__builtin_bit_cast(bf16x8, a), __builtin_bit_cast(bf16x8, b), c, 0, 0, 0);
}
template <int CTRL> __device__ __forceinline__ float dpp(float x) {
  return __builtin_bit_cast(float, __builtin_amdgcn_mov_dpp(__builtin_bit_cast(int, x), CTRL, 0xf, 0xf, true));
}
constexpr int XOR1 = 0xB1, XOR2 = 0x4E, ROR4 = 0x124, ROR8 = 0x128;
__device__ __forceinline__ f32x2 swap32(float a, float b) {
  auto s = __builtin_amdgcn_permlane32_swap(__float_as_uint(a), __float_as_uint(b), false, false);
  return f32x2{__uint_as_float(s[0]), __uint_as_float(s[1])};
}
__device__ __forceinline__ f32x2 swap16(float a, float b) {
  auto s = __builtin_amdgcn_permlane16_swap(__float_as_uint(a), __float_as_uint(b), false, false);
  return f32x2{__uint_as_float(s[0]), __uint_as_float(s[1])};
}
__device__ __forceinline__ float sum16(float x) { const f32x2 s = swap16(x, x); return s.x + s.y; }
__device__ __forceinline__ float sum32(float x) { const f32x2 s = swap32(x, x); return s.x + s.y; }
__device__ __forceinline__ float max16(float x) { const f32x2 s = swap16(x, x); return fmaxf(s.x, s.y); }
__device__ __forceinline__ float max32(float x) { const f32x2 s = swap32(x, x); return fmaxf(s.x, s.y); }
__device__ __forceinline__ float tred(f32x4 v) {
  const f32x2 a = swap32(v.x, v.y), b = swap32(v.z, v.w);
  const f32x2 c = swap16(a.x + a.y, b.x + b.y);
  const float t = c.x + c.y;
  return t + dpp<ROR8>(t);
}
__device__ __forceinline__ int tcomp(int lane) { return ((lane >> 4) & 1) * 2 + (lane >> 5); }
__device__ __forceinline__ float sum_all(float x) {
  x += dpp<XOR1>(x); x += dpp<XOR2>(x); x += dpp<ROR4>(x); x += dpp<ROR8>(x);
  return sum32(sum16(x));
}
__device__ __forceinline__ f32x4 asf(v4u v) { return __builtin_bit_cast(f32x4, v); }
__device__ __forceinline__ v4u asu(f32x4 v) { return __builtin_bit_cast(v4u, v); }
__device__ __forceinline__ unsigned umax4(v4u v) { return max(max(max(v.x, v.y), v.z), v.w); }
__device__ __forceinline__ float exp_(float x) { return __builtin_amdgcn_exp2f(x * 1.4426950408889634f); }
__device__ __forceinline__ float rcp_(float x) { return __builtin_amdgcn_rcpf(x); }

#define LGKM0() asm volatile("s_waitcnt lgkmcnt(0)" ::: "memory")
#define VM0() asm volatile("s_waitcnt vmcnt(0)" ::: "memory")
#define CFENCE() asm volatile("" ::: "memory")
#define PIN(x) asm volatile("" : "+v"(x))
#define WGBAR() do { LGKM0(); __builtin_amdgcn_s_barrier(); CFENCE(); } while (0)

#define STAMPV(i, v) do { } while (0)
#define STAMP(i) do { } while (0)

__device__ __forceinline__ void dma16(unsigned voff, const unsigned char* sbase, unsigned ldsaddr) {
  unsigned keep;
  asm volatile("s_mov_b32 %0, m0\n\ts_mov_b32 m0, %3\n\ts_nop 4\n\tglobal_load_lds_dwordx4 %1, %2 nt\n\ts_mov_b32 m0, %0"
               : "=&s"(keep) : "v"(voff), "s"(sbase), "s"(ldsaddr) : "memory");
}
__device__ __forceinline__ unsigned lds_u32(lptr L, int off) {
  return (unsigned)__builtin_amdgcn_readfirstlane(*(volatile LAS unsigned*)(L + off));
}
__device__ __forceinline__ void lds_wait_ge(lptr L, int off, unsigned need) {
  for (unsigned sp = 0; lds_u32(L, off) < need && sp < LDS_CAP; ++sp) { }
  CFENCE();
}
__device__ __forceinline__ void wait_landed(lptr L, int n) { lds_wait_ge(L, L_LANDED + 4 * (n & 1), (unsigned)(n >> 1) + 1u); }
__device__ __forceinline__ void release(lptr L, int w, int lane, int n) {
  LGKM0();
  if (lane == 0) *(volatile LAS unsigned*)(L + L_PROG + 4 * w) = (unsigned)n;
}
__device__ __forceinline__ void lds_inc(lptr L, int off, int lane) {
  LGKM0();
  if (lane == 0) __hip_atomic_fetch_add((LAS unsigned*)(L + off), 1u, __ATOMIC_RELAXED, __HIP_MEMORY_SCOPE_WORKGROUP);
}
__device__ __forceinline__ void cbar(lptr L, int lane, unsigned& target) {
  target += 8;
  lds_inc(L, L_BAR, lane);
  lds_wait_ge(L, L_BAR, target);
}
__device__ __forceinline__ v4u tfrag(lptr L, int slot, int tl) {
  const s16x4 a = __builtin_amdgcn_ds_read_tr16_b64_v4i16((LAS s16x4*)(L + L_RING + slot * 8192 + tl));
  const s16x4 b = __builtin_amdgcn_ds_read_tr16_b64_v4i16((LAS s16x4*)(L + L_RING + slot * 8192 + tl + 1024));
  const v2u x = __builtin_bit_cast(v2u, a), y = __builtin_bit_cast(v2u, b);
  return v4u{x.x, x.y, y.x, y.y};
}
__device__ __forceinline__ v4u vfrag(lptr L, int off, int lane, int s) {
  return *(const LAS v4u*)(L + off + (lane & 1) * 256 + s * 64 + (lane >> 4) * 16);
}
template <int N>
__device__ __forceinline__ void gather_issue(v4u (&v)[N], const int (&off)[N], __amdgpu_buffer_rsrc_t rs) {
#pragma unroll
  for (int q = 0; q < N; ++q) v[q] = __builtin_amdgcn_raw_buffer_load_b128(rs, off[q], 0, AUX_SC1);
}
template <int N>
__device__ __forceinline__ bool gather_ok(const v4u (&v)[N]) {
  unsigned mx = umax4(v[0]);
#pragma unroll
  for (int q = 1; q < N; ++q) mx = max(mx, umax4(v[q]));
  return __builtin_amdgcn_ballot_w64(mx == SENT) == 0;
}
template <int N>
__device__ __forceinline__ void gather_finish(v4u (&v)[N], const int (&off)[N], __amdgpu_buffer_rsrc_t rs) {
  for (unsigned sp = 0; !gather_ok<N>(v) && sp < SPIN_CAP; ++sp) { CFENCE(); gather_issue<N>(v, off, rs); }
}
template <int N>
__device__ __forceinline__ void gather(v4u (&v)[N], const int (&off)[N], __amdgpu_buffer_rsrc_t rs) {
  gather_issue<N>(v, off, rs);
  gather_finish<N>(v, off, rs);
}
__device__ __forceinline__ int box(int j) { return WS_BOX + j * BOX; }
__device__ __forceinline__ void ln_consts(float s, float q, float& m, float& rden) {
  const float mean = s * (1.f / D);
  const float var = fmaxf(q * (1.f / D) - mean * mean, 0.f);
  m = rbf(mean);
  rden = rcp_(rbf(__builtin_amdgcn_sqrtf(rbf(rbf(var) + 1e-5f))));
}
__device__ __forceinline__ f32x2 ln2(f32x2 x, float m, float rden) { return rbf2(rbf2(x - m) * rden); }
__device__ __forceinline__ void stat_put(lptr L, int off, int w, int lane, f32x4 v) {
  const float s = (v.x + v.y) + (v.z + v.w), q = (v.x * v.x + v.y * v.y) + (v.z * v.z + v.w * v.w);
  const f32x2 a = swap32(s, q);
  float r = sum16(a.x + a.y);
  r += dpp<ROR8>(r); r += dpp<XOR2>(r); r += dpp<XOR1>(r);
  if ((lane & 0x1B) == 0) *(LAS float*)(L + off + w * 16 + (lane & 4) * 2 + (lane >> 5) * 4) = r;
}
__device__ __forceinline__ void stat_get(lptr L, int off, int lane, float& m, float& rden) {
  const f32x2 p = *(const LAS f32x2*)(L + off + (lane >> 3) * 16 + (lane & 4) * 2);
  const f32x2 a = swap32(p.x, p.y);
  float r = sum16(a.x + a.y);
  r += dpp<ROR8>(r);
  const f32x2 t = swap32(r, r);
  ln_consts(t.x, t.y, m, rden);
}

__device__ __forceinline__ int read_minprog(lptr L, int lane) {
  unsigned m = *(volatile LAS unsigned*)(L + L_PROG + 4 * (lane & 7));
  m = min(m, (unsigned)__builtin_amdgcn_mov_dpp((int)m, XOR1, 0xf, 0xf, true));
  m = min(m, (unsigned)__builtin_amdgcn_mov_dpp((int)m, XOR2, 0xf, 0xf, true));
  m = min(m, (unsigned)__builtin_amdgcn_mov_dpp((int)m, ROR4, 0xf, 0xf, true));
  return __builtin_amdgcn_readfirstlane((int)m);
}
template <int W>
__device__ __forceinline__ void load_block(lptr L, int ld, int lane, int n, const unsigned char* bp, int ldb, int hi, int& minprog) {
  if (minprog < n - 15) {
    minprog = read_minprog(L, lane);
    if (minprog < n - 15) {
      VM0();
      if (lane == 0) *(volatile LAS unsigned*)(L + L_LANDED + 4 * ld) = (unsigned)(n >> 1);
      for (unsigned sp = 0; sp < LDS_CAP; ++sp) {
        minprog = read_minprog(L, lane);
        if (minprog >= n - 15) break;
      }
    }
  }
  const unsigned voff = (unsigned)((lane >> 4) * ldb + (((lane & 15) ^ ((lane >> 4) << 2)) << 4));
  const unsigned dst = (unsigned)(size_t)L + L_RING + (unsigned)(n & 15) * 8192u;
#pragma unroll
  for (int p = 0; p < 8; ++p) {
    dma16(voff, bp + (size_t)((p & 3) * 4) * ldb + (size_t)(p >> 2) * hi, dst + p * 1024);
    asm volatile("s_waitcnt vmcnt(%0)" ::"i"(W - 1) : "memory");
    if (((p + 2 - W) & 7) == 0) {
      const int done = (n >> 1) + (p + 2 - W) / 8;
      if (lane == 0 && done > 0) *(volatile LAS unsigned*)(L + L_LANDED + 4 * ld) = (unsigned)done;
    }
  }
}

__global__ void __launch_bounds__(NTHR) fused_block(const u16* __restrict__ x, const u16* __restrict__ kc, const u16* __restrict__ vc,
                                                     const u16* __restrict__ wq, const u16* __restrict__ wo, const u16* __restrict__ w1,
                                                     const u16* __restrict__ b1, const u16* __restrict__ w2, const u16* __restrict__ b2,
                                                     const u16* __restrict__ cvec, const u16* __restrict__ wmod, u16* __restrict__ out,
                                                     unsigned char* ws) {
  extern __shared__ __attribute__((aligned(16))) unsigned char smem[];
  lptr L = (lptr)smem;
  const int tid = threadIdx.x, lane = tid & 63, w = __builtin_amdgcn_readfirstlane(tid >> 6);
  const int j = blockIdx.x, grp = j & 7, split = j >> 3, batt = grp >> 2, hkv = grp & 3;
  const bool isA = split < 16, isC = split >= 30, isB = !isA && !isC;
  const int kq = split & 7, g32 = grp * 4 + (split >> 3), g16 = grp * 2 + ((split >> 3) & 1);
  const int offQ = isA ? 0 : 4, offMR = offQ + 4, offK = offMR + 4, offX = offK + 32, offV = isA ? offX + 8 : offX, offW = offV + 32;
  const int ai = grp * 16 + split, a_hkv = ai >> 5, a_dt = (ai >> 2) & 7, a_cg = ai & 3;
  const int bi = grp * 14 + (split - 16), ci = grp * 2 + (split - 30);
  unsigned long long pc0;
  asm volatile("s_getpc_b64 %0" : "=s"(pc0));

  if (tid < 64) *(volatile LAS unsigned*)(L + L_CTL + 4 * tid) = 0u;
  WGBAR();
  if (__builtin_expect(w == 8 || w == 9, 1)) {
    const int ld = w - 8;
    __builtin_amdgcn_s_setprio(3);
    int minprog = 0;
    STAMP(16);
    const size_t kvoff = ((size_t)grp * SK + split * 1024) * 256;
    const unsigned char* wm = (const unsigned char*)wmod + (size_t)kq * 128 * 12288;
#pragma unroll 1
    for (int n = ld; n < offW; n += 2) {
      const unsigned char* bp; int ldb, hi;
      if (n < offK) {
        const int s = n & 3;
        if (n < offQ) { bp = wm + g16 * 256; ldb = 12288; }
        else if (n < offMR) { bp = (const unsigned char*)wq + (size_t)kq * 128 * 8192 + g32 * 256; ldb = 8192; }
        else { bp = wm + 4096 + g32 * 256; ldb = 12288; }
        bp += (size_t)(s * 32) * ldb; hi = 16 * ldb;
      } else if (n < offX) { bp = (const unsigned char*)kc + kvoff + (size_t)(n - offK) * 8192; ldb = 256; hi = 4096; }
      else if (n < offV) {
        const int q = n - offX;
        bp = (const unsigned char*)wo + ((size_t)(a_hkv * 8 + 2 * (q & 3)) * 128 + 16 * a_dt) * 2048 + a_cg * 512 + (q >> 2) * 256; ldb = 2048; hi = 128 * 2048;
      } else { bp = (const unsigned char*)vc + kvoff + (size_t)(n - offV) * 8192; ldb = 256; hi = 4096; }
      load_block<LOADW>(L, ld, lane, n, bp, ldb, hi, minprog);
    }
    STAMP(19);
    int nend = offW;
    if (isB) {
      nend = offW + 16;
#pragma unroll 1
      for (int n = offW + ld; n < nend; n += 2) {
        const int q = n - offW, s = q & 3, ch = (q >> 2) & 1;
        const unsigned char* bp; int ldb;
        if (q < 8) { bp = (const unsigned char*)w1 + (size_t)(bi & 7) * 128 * 7168 + (bi >> 3) * 512; ldb = 7168; }
        else { bp = (const unsigned char*)w2 + (size_t)(bi >> 2) * 128 * 2048 + (bi & 3) * 512; ldb = 2048; }
        load_block<LOADW2>(L, ld, lane, n, bp + (size_t)(s * 32) * ldb + ch * 256, ldb, 16 * ldb, minprog);
      }
    }
    VM0();
    if (lane == 0) *(volatile LAS unsigned*)(L + L_LANDED + 4 * ld) = (unsigned)(nend >> 1);
    STAMP(20);
    return;
  }

  const __amdgpu_buffer_rsrc_t rs = __builtin_amdgcn_make_buffer_rsrc((void*)ws, 0, WS_BYTES, 0x00020000);
  const int mybox = box(j);
  const int piece = lane & 7, sg = lane >> 3, comp = tcomp(lane);

  if (w == 10) {
    const v2u ep = __builtin_amdgcn_raw_buffer_load_b64(rs, WS_EPOCH, 0, AUX_SC1);
    {
      const unsigned long long a = (pc0 & ~255ull) + (unsigned long long)(split * 1024 + lane * 16);
      v4u t = {0u, 0u, 0u, 0u};
      if (split * 1024 < PREFETCH_CODE) asm volatile("global_load_dwordx4 %0, %1, off" : "+v"(t) : "v"(a) : "memory");
      asm volatile("s_waitcnt vmcnt(0)" : "+v"(t) :: "memory");
    }
    const unsigned long long epoch = ((unsigned long long)(unsigned)__builtin_amdgcn_readfirstlane((int)ep.y) << 32) | (unsigned)__builtin_amdgcn_readfirstlane((int)ep.x);
    const unsigned long long tag = (epoch ^ 0x9E3779B97F4A7C15ull) * 0xD6E8FEB86659FD93ull + 1ull;
    lds_wait_ge(L, L_BAR, 8);
    if (lane == 0) __hip_atomic_store((GAS unsigned long long*)(ws + WS_ALIVE + j * 16), tag, __ATOMIC_RELAXED, __HIP_MEMORY_SCOPE_AGENT);
    for (unsigned sp = 0;; ++sp) {
      bool ok = true;
#pragma unroll
      for (int q = 0; q < 4; ++q) {
        const v2u t = __builtin_amdgcn_raw_buffer_load_b64(rs, WS_ALIVE + (q * 64 + lane) * 16, 0, AUX_SC1);
        ok = ok && t.x == (unsigned)tag && t.y == (unsigned)(tag >> 32);
      }
      if (__all(ok) || sp > SPIN_CAP) break;
      CFENCE();
    }
    if (lane == 0) *(volatile LAS unsigned*)(L + L_FALIVE) = 1u;
    STAMP(4);
    if (j == 0 && lane == 0) __hip_atomic_store((GAS unsigned long long*)(ws + WS_EPOCH), epoch + 1ull, __ATOMIC_RELAXED, __HIP_MEMORY_SCOPE_AGENT);
    lds_wait_ge(L, L_MLCNT, 8);
    const int h = lane & 7;
    const f32x2 r = *(const LAS f32x2*)(L + L_MLW + sg * 64 + h * 8);
    float M = r.x;
    M = fmaxf(M, dpp<ROR8>(M)); M = max32(max16(M));
    float Ls = r.y * exp_(r.x - M);
    Ls += dpp<ROR8>(Ls); Ls = sum32(sum16(Ls));
    const float M1 = dpp<XOR1>(M), L1 = dpp<XOR1>(Ls);
    if (lane < 8 && !(lane & 1)) __builtin_amdgcn_raw_buffer_store_b128(asu(f32x4{M, Ls, M1, L1}), rs, mybox + BX_ML + lane * 8, 0, AUX_SC1);
    STAMP(29);
    v4u g[2];
    const int off[2] = {box((lane >> 2) * 8 + grp) + BX_ML + (lane & 3) * 16, box(((lane >> 2) + 16) * 8 + grp) + BX_ML + (lane & 3) * 16};
    gather<2>(g, off, rs);
    STAMP(30);
    const f32x4 u = asf(g[0]), v = asf(g[1]);
    float M0 = fmaxf(u.x, v.x), Mb = fmaxf(u.z, v.z);
    M0 = fmaxf(M0, dpp<ROR4>(M0)); M0 = fmaxf(M0, dpp<ROR8>(M0)); M0 = max32(max16(M0));
    Mb = fmaxf(Mb, dpp<ROR4>(Mb)); Mb = fmaxf(Mb, dpp<ROR8>(Mb)); Mb = max32(max16(Mb));
    float L0 = u.y * exp_(u.x - M0) + v.y * exp_(v.x - M0), Lb = u.w * exp_(u.z - Mb) + v.w * exp_(v.z - Mb);
    L0 += dpp<ROR4>(L0); L0 += dpp<ROR8>(L0); L0 = sum32(sum16(L0));
    Lb += dpp<ROR4>(Lb); Lb += dpp<ROR8>(Lb); Lb = sum32(sum16(Lb));
    if (lane < 4) *(LAS f32x4*)(L + L_MLF + lane * 16) = f32x4{M0, 1.f / L0, Mb, 1.f / Lb};
    LGKM0();
    if (lane == 0) *(volatile LAS unsigned*)(L + L_FML) = 1u;
    STAMP(9);
    return;
  }

  unsigned bar = 0;
  STAMP(0);
  const v2u xr = *(const GAS v2u*)(x + tid * 4);
  const unsigned xh = *(const GAS u16*)(x + (piece >> 2) * D + kq * 128 + 16 * w + 4 * (piece & 3) + comp);
  v2u cr = {0u, 0u};
  if (w == 1) cr = *(const GAS v2u*)(cvec + (lane >> 5) * D + kq * 128 + 4 * (lane & 31));
  CFENCE();
  {
    const v4u s4 = {SENT, SENT, SENT, SENT};
    const int lo = isA ? 1 : 0, hi = isB ? 12 : 10;
    if (lo + w < hi) __builtin_amdgcn_raw_buffer_store_b128(s4, rs, mybox + (lo + w) * 1024 + lane * 16, 0, AUX_SC1);
    if (lo + w + 8 < hi) __builtin_amdgcn_raw_buffer_store_b128(s4, rs, mybox + (lo + w + 8) * 1024 + lane * 16, 0, AUX_SC1);
  }
  CFENCE();
  if (w == 1) {
    float s[4] = {lo2f(cr.x), hi2f(cr.x), lo2f(cr.y), hi2f(cr.y)};
#pragma unroll
    for (int i = 0; i < 4; ++i) s[i] = s[i] / (1.f + exp_(-s[i]));
    *(LAS v2u*)(L + L_SVEC + (lane >> 5) * 256 + (lane & 31) * 8) = v2u{pk(s[0], s[1]), pk(s[2], s[3])};
  }
  {
    const float a0 = lo2f(xr.x), a1 = hi2f(xr.x), a2 = lo2f(xr.y), a3 = hi2f(xr.y);
    const float s = sum_all((a0 + a1) + (a2 + a3)), q = sum_all((a0 * a0 + a1 * a1) + (a2 * a2 + a3 * a3));
    if (lane == 0) *(LAS f32x2*)(L + L_ST1 + w * 8) = f32x2{s, q};
  }
  VM0();
  cbar(L, lane, bar);
  float lnx;
  {
    const int b = piece >> 2;
    const f32x4 r0 = *(const LAS f32x4*)(L + L_ST1 + b * 32), r1 = *(const LAS f32x4*)(L + L_ST1 + b * 32 + 16);
    float m, rden;
    ln_consts((r0.x + r0.z) + (r1.x + r1.z), (r0.y + r0.w) + (r1.y + r1.w), m, rden);
    lnx = rbf(rbf(lo2f(xh) - m) * rden);
    PIN(lnx);
  }
  const int tl = (8 * (lane >> 4) + ((lane >> 2) & 3)) * 256 + (((2 * w + ((lane & 3) >> 1)) ^ (((lane >> 2) & 3) << 2)) << 4) + (lane & 1) * 8;
  const int pubo = w * 128 + (lane & 15) * 64 + (lane >> 4) * 16;
  STAMP(1);

  if (!isA) {
    f32x4 acc = {0.f, 0.f, 0.f, 0.f};
#pragma unroll
    for (int s = 0; s < 4; ++s) { wait_landed(L, s); acc = mfma(tfrag(L, s, tl), vfrag(L, L_SVEC, lane, s), acc); }
    if ((lane & 15) < 2) __builtin_amdgcn_raw_buffer_store_b128(asu(acc), rs, mybox + BX_MC + pubo, 0, AUX_SC1);
  }
  STAMP(2);
  {
    lds_wait_ge(L, L_FALIVE, 1);
    v4u g[2];
    const int js = (16 + (kq & 1) * 8 + sg) * 8 + (kq >> 1);
    const int off[2] = {box(js) + BX_MC + w * 128 + piece * 16, box(js + 4) + BX_MC + w * 128 + piece * 16};
    gather_issue<2>(g, off, rs);
    v4u qw[4];
#pragma unroll
    for (int s = 0; s < 4; ++s) { wait_landed(L, offQ + s); qw[s] = tfrag(L, (offQ + s) & 15, tl); }
    release(L, w, lane, offMR);
    STAMP(3);
    gather_finish<2>(g, off, rs);
    STAMP(26);
    const float sa = rbf(tred(asf(g[0]))), ca = rbf(tred(asf(g[1])));
    const float h = rbf(lnx * rbf(1.f + ca)) + sa;
    if (!(lane & 8)) *(LAS u16*)(L + L_HVEC + (piece >> 2) * 256 + (16 * w + 4 * (piece & 3) + comp) * 2) = (u16)pk(h, h);
    cbar(L, lane, bar);
    STAMP(5);
    f32x4 acc = {0.f, 0.f, 0.f, 0.f};
#pragma unroll
    for (int s = 0; s < 4; ++s) acc = mfma(qw[s], vfrag(L, L_HVEC, lane, s), acc);
    if ((lane & 15) < 2)
      __builtin_amdgcn_raw_buffer_store_b128(asu(acc), rs, mybox + BX_Q + (lane & 15) * 512 + (16 * w + 4 * (lane >> 4)) * 4, 0, AUX_SC1);
  }
  STAMP(6);
  {
    f32x4 acc = {0.f, 0.f, 0.f, 0.f};
#pragma unroll
    for (int s = 0; s < 4; ++s) { wait_landed(L, offMR + s); acc = mfma(tfrag(L, (offMR + s) & 15, tl), vfrag(L, L_SVEC, lane, s), acc); }
    release(L, w, lane, offK + w);
    if ((lane & 15) < 2) __builtin_amdgcn_raw_buffer_store_b128(asu(acc), rs, mybox + BX_MR + pubo, 0, AUX_SC1);
  }
  v4u qf[4];
  {
    v4u g[4]; int off[4];
    const int head = hkv * 8 + w;
#pragma unroll
    for (int q = 0; q < 4; ++q) {
      const int jq = ((head & 3) * 8 + 2 * q + (lane >> 5)) * 8 + (head >> 2);
      off[q] = box(jq) + BX_Q + batt * 512 + (lane & 31) * 16;
    }
    STAMP(27);
    gather<4>(g, off, rs);
    STAMP(28);
    const f32x4 t = (asf(g[0]) + asf(g[1])) + (asf(g[2]) + asf(g[3]));
    const f32x2 a = swap32(t.x, t.y), b = swap32(t.z, t.w);
    const unsigned p = pk(a.x + a.y, b.x + b.y);
    LAS u16* qv = (LAS u16*)(L + L_QVEC + w * 256 + (lane & 31) * 8 + (lane >> 5) * 2);
    qv[0] = (u16)p; qv[2] = (u16)(p >> 16);
    cbar(L, lane, bar);
#pragma unroll
    for (int s = 0; s < 4; ++s) qf[s] = *(const LAS v4u*)(L + L_QVEC + (lane & 7) * 256 + s * 64 + (lane >> 4) * 16);
  }
  STAMP(7);
  float sc[4][8];
  {
    const int m = lane & 15;
    const int kl = (8 * (m >> 2) + (m & 3)) * 256 + ((m & 3) << 6) + (lane >> 4) * 16;
#pragma unroll
    for (int i = 0; i < 4; ++i) {
      const int n = offK + w + 8 * i;
      wait_landed(L, n);
      v4u kf[2][4];
#pragma unroll
      for (int T = 0; T < 2; ++T)
#pragma unroll
        for (int s = 0; s < 4; ++s) kf[T][s] = *(const LAS v4u*)(L + L_RING + (n & 15) * 8192 + T * 1024 + (kl ^ (s << 6)));
      release(L, w, lane, i < 3 ? n + 8 : offX);
#pragma unroll
      for (int T = 0; T < 2; ++T) {
        f32x4 a = {0.f, 0.f, 0.f, 0.f};
#pragma unroll
        for (int s = 0; s < 4; ++s) a = mfma(kf[T][s], qf[s], a);
        const f32x2 lo = rbf2(rbf2(f32x2{a.x, a.y}) * 0.08838834764831845f), hi = rbf2(rbf2(f32x2{a.z, a.w}) * 0.08838834764831845f);
        sc[i][T * 4] = lo.x; sc[i][T * 4 + 1] = lo.y; sc[i][T * 4 + 2] = hi.x; sc[i][T * 4 + 3] = hi.y;
      }
    }
  }
  STAMP(8);
  float mw = sc[0][0];
  {
#pragma unroll
    for (int i = 0; i < 4; ++i)
#pragma unroll
      for (int r = 0; r < 8; ++r) mw = fmaxf(mw, sc[i][r]);
    mw = max32(max16(mw));
    float lw = 0.f;
#pragma unroll
    for (int i = 0; i < 4; ++i)
#pragma unroll
      for (int r = 0; r < 8; ++r) { sc[i][r] = exp_(sc[i][r] - mw); lw += sc[i][r]; }
    lw = sum32(sum16(lw));
    if (lane < 8) *(LAS f32x2*)(L + L_MLW + w * 64 + lane * 8) = f32x2{mw, lw};
    lds_inc(L, L_MLCNT, lane);
  }
  v4u tw[8];
#pragma unroll
  for (int q = 0; q < 8; ++q) { wait_landed(L, offX + q); tw[q] = tfrag(L, (offX + q) & 15, tl); release(L, w, lane, offX + q + 1); }
  float ga = 0.f, gb = 0.f, xres = 0.f, bias = 0.f;
  if (isC) {
    v4u g[1];
    const int g3 = 8 + ci;
    const int off[1] = {box(((g3 & 3) * 8 + sg) * 8 + (g3 >> 2)) + BX_MR + w * 128 + piece * 16};
    gather<1>(g, off, rs);
    const float t = rbf(tred(asf(g[0])));
    if (!(lane & 8)) __builtin_amdgcn_raw_buffer_store_b32(__float_as_uint(t), rs, mybox + BX_T1 + w * 128 + piece * 16 + comp * 4, 0, AUX_SC1);
    v4u g2[2];
    const int ga3 = ci >> 1, gb3 = 24 + (ci >> 1), line = (ci & 1) * 4 + (w & 3);
    const int off2[2] = {box(((ga3 & 3) * 8 + sg) * 8 + (ga3 >> 2)) + BX_MR + line * 128 + piece * 16,
                         box(((gb3 & 3) * 8 + sg) * 8 + (gb3 >> 2)) + BX_MR + line * 128 + piece * 16};
    gather<2>(g2, off2, rs);
    ga = rbf(tred(asf(g2[0]))); gb = rbf(tred(asf(g2[1])));
    const int col = 64 * ci + 16 * (w & 3) + 4 * (piece & 3) + comp;
    xres = lo2f(*(const GAS u16*)(x + (piece >> 2) * D + col));
    bias = lo2f(*(const GAS u16*)(b2 + col));
    PIN(ga); PIN(gb); PIN(xres); PIN(bias);
  }
  lds_wait_ge(L, L_FML, 1);
  {
    const f32x2 ml = *(const LAS f32x2*)(L + L_MLF + (lane & 7) * 8);
    const float f = exp_(mw - ml.x) * ml.y;
#pragma unroll
    for (int i = 0; i < 4; ++i) {
      const v4u p = {pk(sc[i][0] * f, sc[i][1] * f), pk(sc[i][2] * f, sc[i][3] * f), pk(sc[i][4] * f, sc[i][5] * f), pk(sc[i][6] * f, sc[i][7] * f)};
      if ((lane & 15) < 8) *(LAS v4u*)(L + L_P + (w + 8 * i) * 512 + ((lane >> 4) * 8 + (lane & 7)) * 16) = p;
    }
    cbar(L, lane, bar);
  }
  STAMP(10);
  f32x4 oacc = {0.f, 0.f, 0.f, 0.f};
  f32x4 sbv = {0.f, 0.f, 0.f, 0.f}, cb1 = {0.f, 0.f, 0.f, 0.f};
  const int pl = L_P + ((lane >> 4) * 8 + (lane & 7)) * 16;
  if (!isA) {
#pragma unroll
    for (int i = 0; i < 8; ++i) oacc = mfma(tw[i], *(const LAS v4u*)(L + pl + i * 512), oacc);
  }
  if (isB) {
    v4u g[2];
    const int off[2] = {box(30 * 8 + (w >> 1) + (w & 1) * 8) + BX_T1 + lane * 16, box(30 * 8 + 4 + (w >> 1) + (w & 1) * 8) + BX_T1 + lane * 16};
    gather<2>(g, off, rs);
    sbv = asf(g[0]);
    const f32x4 cb = asf(g[1]);
    const f32x2 c0 = rbf2(f32x2{cb.x, cb.y} + 1.f), c1 = rbf2(f32x2{cb.z, cb.w} + 1.f);
    cb1 = f32x4{c0.x, c0.y, c1.x, c1.y};
    bias = lo2f(*(const GAS u16*)(b1 + (bi >> 2) * 128 + 16 * w + 4 * (piece & 3) + comp));
    PIN(bias); PIN(sbv); PIN(cb1);
  }
#pragma unroll 1
  for (int i = isA ? 0 : 8; i < 32; i += 2) {
    const int n = offV + i;
    wait_landed(L, n + 1); wait_landed(L, n);
    const v4u a0 = tfrag(L, n & 15, tl), a1 = tfrag(L, (n + 1) & 15, tl);
    const v4u p0 = *(const LAS v4u*)(L + pl + i * 512), p1 = *(const LAS v4u*)(L + pl + (i + 1) * 512);
    release(L, w, lane, n + 2);
    oacc = mfma(a0, p0, oacc);
    oacc = mfma(a1, p1, oacc);
  }
  if ((lane & 15) < 8) __builtin_amdgcn_raw_buffer_store_b128(asu(oacc), rs, mybox + BX_O + w * 512 + ((lane & 15) * 4 + (lane >> 4)) * 16, 0, AUX_SC1);
  STAMP(11);

  if (isA) {
    v4u g[4]; int off[4];
#pragma unroll
    for (int q = 0; q < 4; ++q) off[q] = box((sg + 8 * q) * 8 + (w >> 2) * 4 + a_hkv) + BX_O + a_dt * 512 + (w & 3) * 128 + piece * 16;
    gather<4>(g, off, rs);
    const float t = tred((asf(g[0]) + asf(g[1])) + (asf(g[2]) + asf(g[3])));
    if (!(lane & 8)) *(LAS u16*)(L + L_TVEC + (w >> 2) * 256 + (32 * (w & 3) + 4 * piece + comp) * 2) = (u16)pk(t, t);
    WGBAR();
    STAMP(12);
    f32x4 a0 = {0.f, 0.f, 0.f, 0.f}, a1 = {0.f, 0.f, 0.f, 0.f};
#pragma unroll
    for (int s = 0; s < 4; ++s) { const v4u bv = vfrag(L, L_TVEC, lane, s); a0 = mfma(tw[s], bv, a0); a1 = mfma(tw[4 + s], bv, a1); }
    if ((lane & 15) < 2) {
      __builtin_amdgcn_raw_buffer_store_b128(asu(a0), rs, mybox + BX_T1 + pubo, 0, AUX_SC1);
      __builtin_amdgcn_raw_buffer_store_b128(asu(a1), rs, mybox + BX_T1 + 1024 + pubo, 0, AUX_SC1);
    }
    STAMP(13);
  } else if (isC) {
    if (w < 4) {
      const int ct = 4 * ci + w, cg = ct >> 4, ti = ct & 15;
      v4u g[4]; int off[4], off2[4];
#pragma unroll
      for (int q = 0; q < 4; ++q) {
        const int a = (sg + 8 * q) * 4 + cg;
        off[q] = box((a & 15) * 8 + (a >> 4)) + BX_T1 + ti * 128 + piece * 16;
        const int kf = sg + 8 * q, bb = kf * 4 + cg, gg = bb / 14;
        off2[q] = kf < 28 ? box((16 + bb - gg * 14) * 8 + gg) + BX_T2 + ti * 128 + piece * 16 : WS_BYTES;
        PIN(off2[q]);
      }
      gather<4>(g, off, rs);
      const float t = tred((asf(g[0]) + asf(g[1])) + (asf(g[2]) + asf(g[3])));
      const float x1 = rbf(xres + rbf(ga * rbf(t)));
      if (!(lane & 8)) __builtin_amdgcn_raw_buffer_store_b32(__float_as_uint(x1), rs, mybox + BX_X1 + w * 128 + piece * 16 + comp * 4, 0, AUX_SC1);
      STAMP(12);
      gather<4>(g, off2, rs);
      const float z = tred((asf(g[0]) + asf(g[1])) + (asf(g[2]) + asf(g[3])));
      const float o = x1 + rbf(gb * rbf(rbf(z) + bias));
      if (!(lane & 8)) *(GAS u16*)(out + (piece >> 2) * D + 64 * ci + 16 * w + 4 * (piece & 3) + comp) = (u16)pk(o, o);
      STAMP(13);
    }
  } else {
#pragma unroll
    for (int q = 0; q < 8; ++q) { wait_landed(L, offW + q); tw[q] = tfrag(L, (offW + q) & 15, tl); }
    const int cc = 2 * w + (sg >> 2);
    const int offx[1] = {box((30 + (cc & 1)) * 8 + (cc >> 1)) + BX_X1 + (sg & 3) * 128 + piece * 16};
    const int kf = bi >> 2, bb = (kf >> 1) * 8 + sg, gg = bb / 14;
    int offh[1] = {box((16 + bb - gg * 14) * 8 + gg) + BX_T1 + ((kf & 1) * 8 + w) * 128 + piece * 16};
    PIN(offh[0]);
    v4u g[1];
    gather<1>(g, offx, rs);
    STAMP(12);
    const f32x4 x1 = asf(g[0]);
    float m, rden;
    stat_put(L, L_ST1, w, lane, x1);
    WGBAR();
    stat_get(L, L_ST1, lane, m, rden);
    const f32x2 ha = rbf2(rbf2(ln2(f32x2{x1.x, x1.y}, m, rden) * f32x2{cb1.x, cb1.y}) + f32x2{sbv.x, sbv.y});
    const f32x2 hb = rbf2(rbf2(ln2(f32x2{x1.z, x1.w}, m, rden) * f32x2{cb1.z, cb1.w}) + f32x2{sbv.z, sbv.w});
    stat_put(L, L_ST2, w, lane, f32x4{ha.x, ha.y, hb.x, hb.y});
    WGBAR();
    if (w == (bi & 7)) {
      stat_get(L, L_ST2, lane, m, rden);
      const f32x2 ya = rbf2(ha - m) * rden, yb = rbf2(hb - m) * rden;
      *(LAS v2u*)(L + L_TVEC + (piece >> 2) * 256 + (16 * sg + 4 * (piece & 3)) * 2) = v2u{pk(ya.x, ya.y), pk(yb.x, yb.y)};
    }
    WGBAR();
    {
      f32x4 a0 = {0.f, 0.f, 0.f, 0.f}, a1 = {0.f, 0.f, 0.f, 0.f};
#pragma unroll
      for (int s = 0; s < 4; ++s) { const v4u bv = vfrag(L, L_TVEC, lane, s); a0 = mfma(tw[s], bv, a0); a1 = mfma(tw[4 + s], bv, a1); }
      if ((lane & 15) < 2) {
        __builtin_amdgcn_raw_buffer_store_b128(asu(a0), rs, mybox + BX_T1 + pubo, 0, AUX_SC1);
        __builtin_amdgcn_raw_buffer_store_b128(asu(a1), rs, mybox + BX_T1 + 1024 + pubo, 0, AUX_SC1);
      }
    }
    STAMP(13);
    gather_issue<1>(g, offh, rs);
#pragma unroll
    for (int q = 0; q < 8; ++q) { wait_landed(L, offW + 8 + q); tw[q] = tfrag(L, (offW + 8 + q) & 15, tl); }
    gather_finish<1>(g, offh, rs);
    {
      const float u = rbf(rbf(tred(asf(g[0]))) + bias);
      const float z = u * (0.7978845608028654f + 0.035677408136300125f * u * u);
      const float hh = u * rcp_(1.f + __builtin_amdgcn_exp2f(-2.8853900817779268f * z));
      if (!(lane & 8)) *(LAS u16*)(L + L_UVEC + (piece >> 2) * 256 + (16 * w + 4 * (piece & 3) + comp) * 2) = (u16)pk(hh, hh);
      WGBAR();
      STAMP(14);
      f32x4 a0 = {0.f, 0.f, 0.f, 0.f}, a1 = {0.f, 0.f, 0.f, 0.f};
#pragma unroll
      for (int s = 0; s < 4; ++s) { const v4u bv = vfrag(L, L_UVEC, lane, s); a0 = mfma(tw[s], bv, a0); a1 = mfma(tw[4 + s], bv, a1); }
      if ((lane & 15) < 2) {
        __builtin_amdgcn_raw_buffer_store_b128(asu(a0), rs, mybox + BX_T2 + pubo, 0, AUX_SC1);
        __builtin_amdgcn_raw_buffer_store_b128(asu(a1), rs, mybox + BX_T2 + 1024 + pubo, 0, AUX_SC1);
      }
    }
  }
  STAMP(15);
}

}

extern "C" void kernel_launch(void* const* d_in, const int* in_sizes, int n_in, void* d_out, int out_size, void* d_ws, size_t ws_size,
                              hipStream_t stream) {
  (void)in_sizes; (void)n_in; (void)out_size; (void)ws_size;
  (void)hipFuncSetAttribute((const void*)fused_block, hipFuncAttributeMaxDynamicSharedMemorySize, LDS_BYTES);
  hipLaunchKernelGGL(fused_block, dim3(256), dim3(NTHR), LDS_BYTES, stream, (const u16*)d_in[0], (const u16*)d_in[1], (const u16*)d_in[2],
                     (const u16*)d_in[3], (const u16*)d_in[4], (const u16*)d_in[6], (const u16*)d_in[7], (const u16*)d_in[8],
                     (const u16*)d_in[9], (const u16*)d_in[10], (const u16*)d_in[11], (u16*)d_out, (unsigned char*)d_ws);
}
```

```cpp
#define DIAG 3
#include <hip/hip_runtime.h>
#include <hip/hip_bf16.h>
#include <stdint.h>

#define LAS __attribute__((address_space(3)))
#define GAS __attribute__((address_space(1)))
typedef unsigned short u16;
typedef unsigned v4u __attribute__((ext_vector_type(4)));
typedef unsigned v2u __attribute__((ext_vector_type(2)));
typedef float f32x4 __attribute__((ext_vector_type(4)));
typedef float f32x2 __attribute__((ext_vector_type(2)));
typedef short s16x4 __attribute__((ext_vector_type(4)));
typedef short bf16x8 __attribute__((ext_vector_type(8)));
typedef __bf16 bf16x2 __attribute__((ext_vector_type(2)));

namespace {

constexpr int D = 1024, SK = 32768;
constexpr int NTHR = 704;
#define PREFETCH_CODE 22528
#define LOADW 10
#define LOADW2 16

constexpr int WS_EPOCH = 0, WS_ALIVE = 4096, WS_BOX = 65536, BOX = 16384;
constexpr int BX_MC = 0, BX_Q = 1024, BX_MR = 2048, BX_ML = 3072, BX_O = 4096, BX_T1 = 8192, BX_T2 = 10240, BX_X1 = BX_T1 + 1024;
constexpr int WS_BYTES = WS_BOX + 256 * BOX;
constexpr int WS_DBG = 8 << 20;
constexpr unsigned SENT = 0xFFFFFFFFu;
constexpr unsigned SPIN_CAP = 1u << 14, LDS_CAP = 1u << 18;
constexpr int AUX_SC1 = 16;

constexpr int L_RING = 0, L_P = 131072, L_CTL = L_P + 16384;
constexpr int L_LANDED = L_CTL, L_PROG = L_CTL + 32, L_BAR = L_CTL + 64, L_MLCNT = L_CTL + 68, L_FALIVE = L_CTL + 72, L_FML = L_CTL + 76;
constexpr int L_SVEC = L_CTL + 256;
constexpr int L_HVEC = L_SVEC + 512;
constexpr int L_QVEC = L_HVEC + 512;
constexpr int L_ST1 = L_QVEC + 2048;
constexpr int L_ST2 = L_ST1 + 128;
constexpr int L_MLW = L_ST2 + 128;
constexpr int L_MLF = L_MLW + 512;
constexpr int L_TVEC = L_MLF + 64;
constexpr int L_UVEC = L_TVEC + 512;
constexpr int L_STAMP = L_UVEC + 512;
constexpr int LDS_BYTES = L_STAMP + 320;
static_assert(LDS_BYTES <= 160 * 1024, "LDS budget");

typedef LAS unsigned char* lptr;

__device__ __forceinline__ float lo2f(unsigned w) { return __uint_as_float(w << 16); }
__device__ __forceinline__ float hi2f(unsigned w) { return __uint_as_float(w & 0xffff0000u); }
__device__ __forceinline__ unsigned pk(float a, float b) {
  bf16x2 v = {(__bf16)a, (__bf16)b};
  return __builtin_bit_cast(unsigned, v);
}
__device__ __forceinline__ float rbf(float a) { return hi2f(pk(a, a)); }
__device__ __forceinline__ f32x2 rbf2(f32x2 a) { const unsigned p = pk(a.x, a.y); return f32x2{lo2f(p), hi2f(p)}; }
__device__ __forceinline__ f32x4 mfma(v4u a, v4u b, f32x4 c) {
  return __builtin_amdgcn_mfma_f32_16x16x32_bf16(__builtin_bit_cast(bf16x8, a), __builtin_bit_cast(bf16x8, b), c, 0, 0, 0);
}
template <int CTRL> __device__ __forceinline__ float dpp(float x) {
  return __builtin_bit_cast(float, __builtin_amdgcn_mov_dpp(__builtin_bit_cast(int, x), CTRL, 0xf, 0xf, true));
}
constexpr int XOR1 = 0xB1, XOR2 = 0x4E, ROR4 = 0x124, ROR8 = 0x128;
__device__ __forceinline__ f32x2 swap32(float a, float b) {
  auto s = __builtin_amdgcn_permlane32_swap(__float_as_uint(a), __float_as_uint(b), false, false);
  return f32x2{__uint_as_float(s[0]), __uint_as_float(s[1])};
}
__device__ __forceinline__ f32x2 swap16(float a, float b) {
  auto s = __builtin_amdgcn_permlane16_swap(__float_as_uint(a), __float_as_uint(b), false, false);
  return f32x2{__uint_as_float(s[0]), __uint_as_float(s[1])};
}
__device__ __forceinline__ float sum16(float x) { const f32x2 s = swap16(x, x); return s.x + s.y; }
__device__ __forceinline__ float sum32(float x) { const f32x2 s = swap32(x, x); return s.x + s.y; }
__device__ __forceinline__ float max16(float x) { const f32x2 s = swap16(x, x); return fmaxf(s.x, s.y); }
__device__ __forceinline__ float max32(float x) { const f32x2 s = swap32(x, x); return fmaxf(s.x, s.y); }
__device__ __forceinline__ float tred(f32x4 v) {
  const f32x2 a = swap32(v.x, v.y), b = swap32(v.z, v.w);
  const f32x2 c = swap16(a.x + a.y, b.x + b.y);
  const float t = c.x + c.y;
  return t + dpp<ROR8>(t);
}
__device__ __forceinline__ int tcomp(int lane) { return ((lane >> 4) & 1) * 2 + (lane >> 5); }
__device__ __forceinline__ float sum_all(float x) {
  x += dpp<XOR1>(x); x += dpp<XOR2>(x); x += dpp<ROR4>(x); x += dpp<ROR8>(x);
  return sum32(sum16(x));
}
__device__ __forceinline__ f32x4 asf(v4u v) { return __builtin_bit_cast(f32x4, v); }
__device__ __forceinline__ v4u asu(f32x4 v) { return __builtin_bit_cast(v4u, v); }
__device__ __forceinline__ unsigned umax4(v4u v) { return max(max(max(v.x, v.y), v.z), v.w); }
__device__ __forceinline__ float exp_(float x) { return __builtin_amdgcn_exp2f(x * 1.4426950408889634f); }
__device__ __forceinline__ float rcp_(float x) { return __builtin_amdgcn_rcpf(x); }

#define LGKM0() asm volatile("s_waitcnt lgkmcnt(0)" ::: "memory")
#define VM0() asm volatile("s_waitcnt vmcnt(0)" ::: "memory")
#define CFENCE() asm volatile("" ::: "memory")
#define PIN(x) asm volatile("" : "+v"(x))
#define WGBAR() do { LGKM0(); __builtin_amdgcn_s_barrier(); CFENCE(); } while (0)

#define STAMPV(i, v) do { } while (0)
#define STAMP(i) do { } while (0)

__device__ __forceinline__ void dma16(unsigned voff, const unsigned char* sbase, unsigned ldsaddr) {
  unsigned keep;
  asm volatile("s_mov_b32 %0, m0\n\ts_mov_b32 m0, %3\n\ts_nop 4\n\tglobal_load_lds_dwordx4 %1, %2 nt\n\ts_mov_b32 m0, %0"
               : "=&s"(keep) : "v"(voff), "s"(sbase), "s"(ldsaddr) : "memory");
}
__device__ __forceinline__ unsigned lds_u32(lptr L, int off) {
  return (unsigned)__builtin_amdgcn_readfirstlane(*(volatile LAS unsigned*)(L + off));
}
__device__ __forceinline__ void lds_wait_ge(lptr L, int off, unsigned need) {
  for (unsigned sp = 0; lds_u32(L, off) < need && sp < LDS_CAP; ++sp) { }
  CFENCE();
}
__device__ __forceinline__ void wait_landed(lptr L, int n) { lds_wait_ge(L, L_LANDED + 4 * (n & 1), (unsigned)(n >> 1) + 1u); }
__device__ __forceinline__ void release(lptr L, int w, int lane, int n) {
  LGKM0();
  if (lane == 0) *(volatile LAS unsigned*)(L + L_PROG + 4 * w) = (unsigned)n;
}
__device__ __forceinline__ void lds_inc(lptr L, int off, int lane) {
  LGKM0();
  if (lane == 0) __hip_atomic_fetch_add((LAS unsigned*)(L + off), 1u, __ATOMIC_RELAXED, __HIP_MEMORY_SCOPE_WORKGROUP);
}
__device__ __forceinline__ void cbar(lptr L, int lane, unsigned& target) {
  target += 8;
  lds_inc(L, L_BAR, lane);
  lds_wait_ge(L, L_BAR, target);
}
__device__ __forceinline__ v4u tfrag(lptr L, int slot, int tl) {
  const s16x4 a = __builtin_amdgcn_ds_read_tr16_b64_v4i16((LAS s16x4*)(L + L_RING + slot * 8192 + tl));
  const s16x4 b = __builtin_amdgcn_ds_read_tr16_b64_v4i16((LAS s16x4*)(L + L_RING + slot * 8192 + tl + 1024));
  const v2u x = __builtin_bit_cast(v2u, a), y = __builtin_bit_cast(v2u, b);
  return v4u{x.x, x.y, y.x, y.y};
}
__device__ __forceinline__ v4u vfrag(lptr L, int off, int lane, int s) {
  return *(const LAS v4u*)(L + off + (lane & 1) * 256 + s * 64 + (lane >> 4) * 16);
}
template <int N>
__device__ __forceinline__ void gather_issue(v4u (&v)[N], const int (&off)[N], __amdgpu_buffer_rsrc_t rs) {
#pragma unroll
  for (int q = 0; q < N; ++q) v[q] = __builtin_amdgcn_raw_buffer_load_b128(rs, off[q], 0, AUX_SC1);
}
template <int N>
__device__ __forceinline__ bool gather_ok(const v4u (&v)[N]) {
  unsigned mx = umax4(v[0]);
#pragma unroll
  for (int q = 1; q < N; ++q) mx = max(mx, umax4(v[q]));
  return __builtin_amdgcn_ballot_w64(mx == SENT) == 0;
}
template <int N>
__device__ __forceinline__ void gather_finish(v4u (&v)[N], const int (&off)[N], __amdgpu_buffer_rsrc_t rs) {
  for (unsigned sp = 0; !gather_ok<N>(v) && sp < SPIN_CAP; ++sp) { CFENCE(); gather_issue<N>(v, off, rs); }
}
template <int N>
__device__ __forceinline__ void gather(v4u (&v)[N], const int (&off)[N], __amdgpu_buffer_rsrc_t rs) {
  gather_issue<N>(v, off, rs);
  gather_finish<N>(v, off, rs);
}
__device__ __forceinline__ int box(int j) { return WS_BOX + j * BOX; }
__device__ __forceinline__ void ln_consts(float s, float q, float& m, float& rden) {
  const float mean = s * (1.f / D);
  const float var = fmaxf(q * (1.f / D) - mean * mean, 0.f);
  m = rbf(mean);
  rden = rcp_(rbf(__builtin_amdgcn_sqrtf(rbf(rbf(var) + 1e-5f))));
}
__device__ __forceinline__ f32x2 ln2(f32x2 x, float m, float rden) { return rbf2(rbf2(x - m) * rden); }
__device__ __forceinline__ void stat_put(lptr L, int off, int w, int lane, f32x4 v) {
  const float s = (v.x + v.y) + (v.z + v.w), q = (v.x * v.x + v.y * v.y) + (v.z * v.z + v.w * v.w);
  const f32x2 a = swap32(s, q);
  float r = sum16(a.x + a.y);
  r += dpp<ROR8>(r); r += dpp<XOR2>(r); r += dpp<XOR1>(r);
  if ((lane & 0x1B) == 0) *(LAS float*)(L + off + w * 16 + (lane & 4) * 2 + (lane >> 5) * 4) = r;
}
__device__ __forceinline__ void stat_get(lptr L, int off, int lane, float& m, float& rden) {
  const f32x2 p = *(const LAS f32x2*)(L + off + (lane >> 3) * 16 + (lane & 4) * 2);
  const f32x2 a = swap32(p.x, p.y);
  float r = sum16(a.x + a.y);
  r += dpp<ROR8>(r);
  const f32x2 t = swap32(r, r);
  ln_consts(t.x, t.y, m, rden);
}

__device__ __forceinline__ int read_minprog(lptr L, int lane) {
  unsigned m = *(volatile LAS unsigned*)(L + L_PROG + 4 * (lane & 7));
  m = min(m, (unsigned)__builtin_amdgcn_mov_dpp((int)m, XOR1, 0xf, 0xf, true));
  m = min(m, (unsigned)__builtin_amdgcn_mov_dpp((int)m, XOR2, 0xf, 0xf, true));
  m = min(m, (unsigned)__builtin_amdgcn_mov_dpp((int)m, ROR4, 0xf, 0xf, true));
  return __builtin_amdgcn_readfirstlane((int)m);
}
template <int W>
__device__ __forceinline__ void load_block(lptr L, int ld, int lane, int n, const unsigned char* bp, int ldb, int hi, int& minprog) {
  if (minprog < n - 15) {
    minprog = read_minprog(L, lane);
    if (minprog < n - 15) {
      VM0();
      if (lane == 0) *(volatile LAS unsigned*)(L + L_LANDED + 4 * ld) = (unsigned)(n >> 1);
      for (unsigned sp = 0; sp < LDS_CAP; ++sp) {
        minprog = read_minprog(L, lane);
        if (minprog >= n - 15) break;
      }
    }
  }
  const unsigned voff = (unsigned)((lane >> 4) * ldb + (((lane & 15) ^ ((lane >> 4) << 2)) << 4));
  const unsigned dst = (unsigned)(size_t)L + L_RING + (unsigned)(n & 15) * 8192u;
#pragma unroll
  for (int p = 0; p < 8; ++p) {
    dma16(voff, bp + (size_t)((p & 3) * 4) * ldb + (size_t)(p >> 2) * hi, dst + p * 1024);
    asm volatile("s_waitcnt vmcnt(%0)" ::"i"(W - 1) : "memory");
    if (((p + 2 - W) & 7) == 0) {
      const int done = (n >> 1) + (p + 2 - W) / 8;
      if (lane == 0 && done > 0) *(volatile LAS unsigned*)(L + L_LANDED + 4 * ld) = (unsigned)done;
    }
  }
}

__global__ void __launch_bounds__(NTHR) fused_block(const u16* __restrict__ x, const u16* __restrict__ kc, const u16* __restrict__ vc,
                                                     const u16* __restrict__ wq, const u16* __restrict__ wo, const u16* __restrict__ w1,
                                                     const u16* __restrict__ b1, const u16* __restrict__ w2, const u16* __restrict__ b2,
                                                     const u16* __restrict__ cvec, const u16* __restrict__ wmod, u16* __restrict__ out,
                                                     unsigned char* ws) {
  extern __shared__ __attribute__((aligned(16))) unsigned char smem[];
  lptr L = (lptr)smem;
  const int tid = threadIdx.x, lane = tid & 63, w = __builtin_amdgcn_readfirstlane(tid >> 6);
  const int j = blockIdx.x, grp = j & 7, split = j >> 3, batt = grp >> 2, hkv = grp & 3;
  const bool isA = split < 16, isC = split >= 30, isB = !isA && !isC;
  const int kq = split & 7, g32 = grp * 4 + (split >> 3), g16 = grp * 2 + ((split >> 3) & 1);
  const int offQ = isA ? 0 : 4, offMR = offQ + 4, offK = offMR + 4, offX = offK + 32, offV = isA ? offX + 8 : offX, offW = offV + 32;
  const int ai = grp * 16 + split, a_hkv = ai >> 5, a_dt = (ai >> 2) & 7, a_cg = ai & 3;
  const int bi = grp * 14 + (split - 16), ci = grp * 2 + (split - 30);
  unsigned long long pc0;
  asm volatile("s_getpc_b64 %0" : "=s"(pc0));

  if (tid < 64) *(volatile LAS unsigned*)(L + L_CTL + 4 * tid) = 0u;
  WGBAR();
  if (__builtin_expect(w == 8 || w == 9, 1)) {
    const int ld = w - 8;
    __builtin_amdgcn_s_setprio(3);
    int minprog = 0;
    STAMP(16);
    const size_t kvoff = ((size_t)grp * SK + split * 1024) * 256;
    const unsigned char* wm = (const unsigned char*)wmod + (size_t)kq * 128 * 12288;
#pragma unroll 1
    for (int n = ld; n < offW; n += 2) {
      const unsigned char* bp; int ldb, hi;
      if (n < offK) {
        const int s = n & 3;
        if (n < offQ) { bp = wm + g16 * 256; ldb = 12288; }
        else if (n < offMR) { bp = (const unsigned char*)wq + (size_t)kq * 128 * 8192 + g32 * 256; ldb = 8192; }
        else { bp = wm + 4096 + g32 * 256; ldb = 12288; }
        bp += (size_t)(s * 32) * ldb; hi = 16 * ldb;
      } else if (n < offX) { bp = (const unsigned char*)kc + kvoff + (size_t)(n - offK) * 8192; ldb = 256; hi = 4096; }
      else if (n < offV) {
        const int q = n - offX;
        bp = (const unsigned char*)wo + ((size_t)(a_hkv * 8 + 2 * (q & 3)) * 128 + 16 * a_dt) * 2048 + a_cg * 512 + (q >> 2) * 256; ldb = 2048; hi = 128 * 2048;
      } else { bp = (const unsigned char*)vc + kvoff + (size_t)(n - offV) * 8192; ldb = 256; hi = 4096; }
      load_block<LOADW>(L, ld, lane, n, bp, ldb, hi, minprog);
    }
    STAMP(19);
    int nend = offW;
    if (isB) {
      nend = offW + 16;
#pragma unroll 1
      for (int n = offW + ld; n < nend; n += 2) {
        const int q = n - offW, s = q & 3, ch = (q >> 2) & 1;
        const unsigned char* bp; int ldb;
        if (q < 8) { bp = (const unsigned char*)w1 + (size_t)(bi & 7) * 128 * 7168 + (bi >> 3) * 512; ldb = 7168; }
        else { bp = (const unsigned char*)w2 + (size_t)(bi >> 2) * 128 * 2048 + (bi & 3) * 512; ldb = 2048; }
        load_block<LOADW2>(L, ld, lane, n, bp + (size_t)(s * 32) * ldb + ch * 256, ldb, 16 * ldb, minprog);
      }
    }
    VM0();
    if (lane == 0) *(volatile LAS unsigned*)(L + L_LANDED + 4 * ld) = (unsigned)(nend >> 1);
    STAMP(20);
    return;
  }

  const __amdgpu_buffer_rsrc_t rs = __builtin_amdgcn_make_buffer_rsrc((void*)ws, 0, WS_BYTES, 0x00020000);
  const int mybox = box(j);
  const int piece = lane & 7, sg = lane >> 3, comp = tcomp(lane);

  if (w == 10) {
    const v2u ep = __builtin_amdgcn_raw_buffer_load_b64(rs, WS_EPOCH, 0, AUX_SC1);
    {
      const unsigned long long a = (pc0 & ~255ull) + (unsigned long long)(split * 1024 + lane * 16);
      v4u t = {0u, 0u, 0u, 0u};
      if (split * 1024 < PREFETCH_CODE) asm volatile("global_load_dwordx4 %0, %1, off" : "+v"(t) : "v"(a) : "memory");
      asm volatile("s_waitcnt vmcnt(0)" : "+v"(t) :: "memory");
    }
    const unsigned long long epoch = ((unsigned long long)(unsigned)__builtin_amdgcn_readfirstlane((int)ep.y) << 32) | (unsigned)__builtin_amdgcn_readfirstlane((int)ep.x);
    const unsigned long long tag = (epoch ^ 0x9E3779B97F4A7C15ull) * 0xD6E8FEB86659FD93ull + 1ull;
    lds_wait_ge(L, L_BAR, 8);
    if (lane == 0) __hip_atomic_store((GAS unsigned long long*)(ws + WS_ALIVE + j * 16), tag, __ATOMIC_RELAXED, __HIP_MEMORY_SCOPE_AGENT);
    for (unsigned sp = 0;; ++sp) {
      bool ok = true;
#pragma unroll
      for (int q = 0; q < 4; ++q) {
        const v2u t = __builtin_amdgcn_raw_buffer_load_b64(rs, WS_ALIVE + (q * 64 + lane) * 16, 0, AUX_SC1);
        ok = ok && t.x == (unsigned)tag && t.y == (unsigned)(tag >> 32);
      }
      if (__all(ok) || sp > SPIN_CAP) break;
      CFENCE();
    }
    if (lane == 0) *(volatile LAS unsigned*)(L + L_FALIVE) = 1u;
    STAMP(4);
    if (j == 0 && lane == 0) __hip_atomic_store((GAS unsigned long long*)(ws + WS_EPOCH), epoch + 1ull, __ATOMIC_RELAXED, __HIP_MEMORY_SCOPE_AGENT);
    lds_wait_ge(L, L_MLCNT, 8);
    const int h = lane & 7;
    const f32x2 r = *(const LAS f32x2*)(L + L_MLW + sg * 64 + h * 8);
    float M = r.x;
    M = fmaxf(M, dpp<ROR8>(M)); M = max32(max16(M));
    float Ls = r.y * exp_(r.x - M);
    Ls += dpp<ROR8>(Ls); Ls = sum32(sum16(Ls));
    const float M1 = dpp<XOR1>(M), L1 = dpp<XOR1>(Ls);
    if (lane < 8 && !(lane & 1)) __builtin_amdgcn_raw_buffer_store_b128(asu(f32x4{M, Ls, M1, L1}), rs, mybox + BX_ML + lane * 8, 0, AUX_SC1);
    STAMP(29);
    v4u g[2];
    const int off[2] = {box((lane >> 2) * 8 + grp) + BX_ML + (lane & 3) * 16, box(((lane >> 2) + 16) * 8 + grp) + BX_ML + (lane & 3) * 16};
    gather<2>(g, off, rs);
    STAMP(30);
    const f32x4 u = asf(g[0]), v = asf(g[1]);
    float M0 = fmaxf(u.x, v.x), Mb = fmaxf(u.z, v.z);
    M0 = fmaxf(M0, dpp<ROR4>(M0)); M0 = fmaxf(M0, dpp<ROR8>(M0)); M0 = max32(max16(M0));
    Mb = fmaxf(Mb, dpp<ROR4>(Mb)); Mb = fmaxf(Mb, dpp<ROR8>(Mb)); Mb = max32(max16(Mb));
    float L0 = u.y * exp_(u.x - M0) + v.y * exp_(v.x - M0), Lb = u.w * exp_(u.z - Mb) + v.w * exp_(v.z - Mb);
    L0 += dpp<ROR4>(L0); L0 += dpp<ROR8>(L0); L0 = sum32(sum16(L0));
    Lb += dpp<ROR4>(Lb); Lb += dpp<ROR8>(Lb); Lb = sum32(sum16(Lb));
    if (lane < 4) *(LAS f32x4*)(L + L_MLF + lane * 16) = f32x4{M0, 1.f / L0, Mb, 1.f / Lb};
    LGKM0();
    if (lane == 0) *(volatile LAS unsigned*)(L + L_FML) = 1u;
    STAMP(9);
    return;
  }

  unsigned bar = 0;
  const unsigned long long dg0 = __builtin_amdgcn_s_memrealtime(); unsigned long long dg1 = dg0;
#define DG(k) do { if (DIAG == k) dg1 = __builtin_amdgcn_s_memrealtime(); } while (0)
  STAMP(0);
  const v2u xr = *(const GAS v2u*)(x + tid * 4);
  const unsigned xh = *(const GAS u16*)(x + (piece >> 2) * D + kq * 128 + 16 * w + 4 * (piece & 3) + comp);
  v2u cr = {0u, 0u};
  if (w == 1) cr = *(const GAS v2u*)(cvec + (lane >> 5) * D + kq * 128 + 4 * (lane & 31));
  CFENCE();
  {
    const v4u s4 = {SENT, SENT, SENT, SENT};
    const int lo = isA ? 1 : 0, hi = isB ? 12 : 10;
    if (lo + w < hi) __builtin_amdgcn_raw_buffer_store_b128(s4, rs, mybox + (lo + w) * 1024 + lane * 16, 0, AUX_SC1);
    if (lo + w + 8 < hi) __builtin_amdgcn_raw_buffer_store_b128(s4, rs, mybox + (lo + w + 8) * 1024 + lane * 16, 0, AUX_SC1);
  }
  CFENCE();
  if (w == 1) {
    float s[4] = {lo2f(cr.x), hi2f(cr.x), lo2f(cr.y), hi2f(cr.y)};
#pragma unroll
    for (int i = 0; i < 4; ++i) s[i] = s[i] / (1.f + exp_(-s[i]));
    *(LAS v2u*)(L + L_SVEC + (lane >> 5) * 256 + (lane & 31) * 8) = v2u{pk(s[0], s[1]), pk(s[2], s[3])};
  }
  {
    const float a0 = lo2f(xr.x), a1 = hi2f(xr.x), a2 = lo2f(xr.y), a3 = hi2f(xr.y);
    const float s = sum_all((a0 + a1) + (a2 + a3)), q = sum_all((a0 * a0 + a1 * a1) + (a2 * a2 + a3 * a3));
    if (lane == 0) *(LAS f32x2*)(L + L_ST1 + w * 8) = f32x2{s, q};
  }
  VM0();
  cbar(L, lane, bar);
  float lnx;
  {
    const int b = piece >> 2;
    const f32x4 r0 = *(const LAS f32x4*)(L + L_ST1 + b * 32), r1 = *(const LAS f32x4*)(L + L_ST1 + b * 32 + 16);
    float m, rden;
    ln_consts((r0.x + r0.z) + (r1.x + r1.z), (r0.y + r0.w) + (r1.y + r1.w), m, rden);
    lnx = rbf(rbf(lo2f(xh) - m) * rden);
    PIN(lnx);
  }
  const int tl = (8 * (lane >> 4) + ((lane >> 2) & 3)) * 256 + (((2 * w + ((lane & 3) >> 1)) ^ (((lane >> 2) & 3) << 2)) << 4) + (lane & 1) * 8;
  const int pubo = w * 128 + (lane & 15) * 64 + (lane >> 4) * 16;
  STAMP(1);

  if (!isA) {
    f32x4 acc = {0.f, 0.f, 0.f, 0.f};
#pragma unroll
    for (int s = 0; s < 4; ++s) { wait_landed(L, s); acc = mfma(tfrag(L, s, tl), vfrag(L, L_SVEC, lane, s), acc); }
    if ((lane & 15) < 2) __builtin_amdgcn_raw_buffer_store_b128(asu(acc), rs, mybox + BX_MC + pubo, 0, AUX_SC1);
  }
  STAMP(2);
  DG(5);
  {
    lds_wait_ge(L, L_FALIVE, 1);
    v4u g[2];
    const int js = (16 + (kq & 1) * 8 + sg) * 8 + (kq >> 1);
    const int off[2] = {box(js) + BX_MC + w * 128 + piece * 16, box(js + 4) + BX_MC + w * 128 + piece * 16};
    gather_issue<2>(g, off, rs);
    v4u qw[4];
#pragma unroll
    for (int s = 0; s < 4; ++s) { wait_landed(L, offQ + s); qw[s] = tfrag(L, (offQ + s) & 15, tl); }
    release(L, w, lane, offMR);
    STAMP(3);
    gather_finish<2>(g, off, rs);
    STAMP(26);
    const float sa = rbf(tred(asf(g[0]))), ca = rbf(tred(asf(g[1])));
    const float h = rbf(lnx * rbf(1.f + ca)) + sa;
    if (!(lane & 8)) *(LAS u16*)(L + L_HVEC + (piece >> 2) * 256 + (16 * w + 4 * (piece & 3) + comp) * 2) = (u16)pk(h, h);
    cbar(L, lane, bar);
    STAMP(5);
    f32x4 acc = {0.f, 0.f, 0.f, 0.f};
#pragma unroll
    for (int s = 0; s < 4; ++s) acc = mfma(qw[s], vfrag(L, L_HVEC, lane, s), acc);
    if ((lane & 15) < 2)
      __builtin_amdgcn_raw_buffer_store_b128(asu(acc), rs, mybox + BX_Q + (lane & 15) * 512 + (16 * w + 4 * (lane >> 4)) * 4, 0, AUX_SC1);
  }
  STAMP(6);
  {
    f32x4 acc = {0.f, 0.f, 0.f, 0.f};
#pragma unroll
    for (int s = 0; s < 4; ++s) { wait_landed(L, offMR + s); acc = mfma(tfrag(L, (offMR + s) & 15, tl), vfrag(L, L_SVEC, lane, s), acc); }
    release(L, w, lane, offK + w);
    if ((lane & 15) < 2) __builtin_amdgcn_raw_buffer_store_b128(asu(acc), rs, mybox + BX_MR + pubo, 0, AUX_SC1);
  }
  v4u qf[4];
  {
    v4u g[4]; int off[4];
    const int head = hkv * 8 + w;
#pragma unroll
    for (int q = 0; q < 4; ++q) {
      const int jq = ((head & 3) * 8 + 2 * q + (lane >> 5)) * 8 + (head >> 2);
      off[q] = box(jq) + BX_Q + batt * 512 + (lane & 31) * 16;
    }
    STAMP(27);
    gather<4>(g, off, rs);
    STAMP(28);
    const f32x4 t = (asf(g[0]) + asf(g[1])) + (asf(g[2]) + asf(g[3]));
    const f32x2 a = swap32(t.x, t.y), b = swap32(t.z, t.w);
    const unsigned p = pk(a.x + a.y, b.x + b.y);
    LAS u16* qv = (LAS u16*)(L + L_QVEC + w * 256 + (lane & 31) * 8 + (lane >> 5) * 2);
    qv[0] = (u16)p; qv[2] = (u16)(p >> 16);
    cbar(L, lane, bar);
#pragma unroll
    for (int s = 0; s < 4; ++s) qf[s] = *(const LAS v4u*)(L + L_QVEC + (lane & 7) * 256 + s * 64 + (lane >> 4) * 16);
  }
  STAMP(7);
  DG(3);
  float sc[4][8];
  {
    const int m = lane & 15;
    const int kl = (8 * (m >> 2) + (m & 3)) * 256 + ((m & 3) << 6) + (lane >> 4) * 16;
#pragma unroll
    for (int i = 0; i < 4; ++i) {
      const int n = offK + w + 8 * i;
      wait_landed(L, n);
      v4u kf[2][4];
#pragma unroll
      for (int T = 0; T < 2; ++T)
#pragma unroll
        for (int s = 0; s < 4; ++s) kf[T][s] = *(const LAS v4u*)(L + L_RING + (n & 15) * 8192 + T * 1024 + (kl ^ (s << 6)));
      release(L, w, lane, i < 3 ? n + 8 : offX);
#pragma unroll
      for (int T = 0; T < 2; ++T) {
        f32x4 a = {0.f, 0.f, 0.f, 0.f};
#pragma unroll
        for (int s = 0; s < 4; ++s) a = mfma(kf[T][s], qf[s], a);
        const f32x2 lo = rbf2(rbf2(f32x2{a.x, a.y}) * 0.08838834764831845f), hi = rbf2(rbf2(f32x2{a.z, a.w}) * 0.08838834764831845f);
        sc[i][T * 4] = lo.x; sc[i][T * 4 + 1] = lo.y; sc[i][T * 4 + 2] = hi.x; sc[i][T * 4 + 3] = hi.y;
      }
    }
  }
  STAMP(8);
  float mw = sc[0][0];
  {
#pragma unroll
    for (int i = 0; i < 4; ++i)
#pragma unroll
      for (int r = 0; r < 8; ++r) mw = fmaxf(mw, sc[i][r]);
    mw = max32(max16(mw));
    float lw = 0.f;
#pragma unroll
    for (int i = 0; i < 4; ++i)
#pragma unroll
      for (int r = 0; r < 8; ++r) { sc[i][r] = exp_(sc[i][r] - mw); lw += sc[i][r]; }
    lw = sum32(sum16(lw));
    if (lane < 8) *(LAS f32x2*)(L + L_MLW + w * 64 + lane * 8) = f32x2{mw, lw};
    lds_inc(L, L_MLCNT, lane);
  }
  v4u tw[8];
#pragma unroll
  for (int q = 0; q < 8; ++q) { wait_landed(L, offX + q); tw[q] = tfrag(L, (offX + q) & 15, tl); release(L, w, lane, offX + q + 1); }
  float ga = 0.f, gb = 0.f, xres = 0.f, bias = 0.f;
  if (isC) {
    v4u g[1];
    const int g3 = 8 + ci;
    const int off[1] = {box(((g3 & 3) * 8 + sg) * 8 + (g3 >> 2)) + BX_MR + w * 128 + piece * 16};
    gather<1>(g, off, rs);
    const float t = rbf(tred(asf(g[0])));
    if (!(lane & 8)) __builtin_amdgcn_raw_buffer_store_b32(__float_as_uint(t), rs, mybox + BX_T1 + w * 128 + piece * 16 + comp * 4, 0, AUX_SC1);
    v4u g2[2];
    const int ga3 = ci >> 1, gb3 = 24 + (ci >> 1), line = (ci & 1) * 4 + (w & 3);
    const int off2[2] = {box(((ga3 & 3) * 8 + sg) * 8 + (ga3 >> 2)) + BX_MR + line * 128 + piece * 16,
                         box(((gb3 & 3) * 8 + sg) * 8 + (gb3 >> 2)) + BX_MR + line * 128 + piece * 16};
    gather<2>(g2, off2, rs);
    ga = rbf(tred(asf(g2[0]))); gb = rbf(tred(asf(g2[1])));
    const int col = 64 * ci + 16 * (w & 3) + 4 * (piece & 3) + comp;
    xres = lo2f(*(const GAS u16*)(x + (piece >> 2) * D + col));
    bias = lo2f(*(const GAS u16*)(b2 + col));
    PIN(ga); PIN(gb); PIN(xres); PIN(bias);
  }
  lds_wait_ge(L, L_FML, 1);
  {
    const f32x2 ml = *(const LAS f32x2*)(L + L_MLF + (lane & 7) * 8);
    const float f = exp_(mw - ml.x) * ml.y;
#pragma unroll
    for (int i = 0; i < 4; ++i) {
      const v4u p = {pk(sc[i][0] * f, sc[i][1] * f), pk(sc[i][2] * f, sc[i][3] * f), pk(sc[i][4] * f, sc[i][5] * f), pk(sc[i][6] * f, sc[i][7] * f)};
      if ((lane & 15) < 8) *(LAS v4u*)(L + L_P + (w + 8 * i) * 512 + ((lane >> 4) * 8 + (lane & 7)) * 16) = p;
    }
    cbar(L, lane, bar);
  }
  STAMP(10);
  DG(4);
  f32x4 oacc = {0.f, 0.f, 0.f, 0.f};
  f32x4 sbv = {0.f, 0.f, 0.f, 0.f}, cb1 = {0.f, 0.f, 0.f, 0.f};
  const int pl = L_P + ((lane >> 4) * 8 + (lane & 7)) * 16;
  if (!isA) {
#pragma unroll
    for (int i = 0; i < 8; ++i) oacc = mfma(tw[i], *(const LAS v4u*)(L + pl + i * 512), oacc);
  }
  if (isB) {
    v4u g[2];
    const int off[2] = {box(30 * 8 + (w >> 1) + (w & 1) * 8) + BX_T1 + lane * 16, box(30 * 8 + 4 + (w >> 1) + (w & 1) * 8) + BX_T1 + lane * 16};
    gather<2>(g, off, rs);
    sbv = asf(g[0]);
    const f32x4 cb = asf(g[1]);
    const f32x2 c0 = rbf2(f32x2{cb.x, cb.y} + 1.f), c1 = rbf2(f32x2{cb.z, cb.w} + 1.f);
    cb1 = f32x4{c0.x, c0.y, c1.x, c1.y};
    bias = lo2f(*(const GAS u16*)(b1 + (bi >> 2) * 128 + 16 * w + 4 * (piece & 3) + comp));
    PIN(bias); PIN(sbv); PIN(cb1);
  }
#pragma unroll 1
  for (int i = isA ? 0 : 8; i < 32; i += 2) {
    const int n = offV + i;
    wait_landed(L, n + 1); wait_landed(L, n);
    const v4u a0 = tfrag(L, n & 15, tl), a1 = tfrag(L, (n + 1) & 15, tl);
    const v4u p0 = *(const LAS v4u*)(L + pl + i * 512), p1 = *(const LAS v4u*)(L + pl + (i + 1) * 512);
    release(L, w, lane, n + 2);
    oacc = mfma(a0, p0, oacc);
    oacc = mfma(a1, p1, oacc);
  }
  if ((lane & 15) < 8) __builtin_amdgcn_raw_buffer_store_b128(asu(oacc), rs, mybox + BX_O + w * 512 + ((lane & 15) * 4 + (lane >> 4)) * 16, 0, AUX_SC1);
  STAMP(11);
  DG(2);

  if (isA) {
    v4u g[4]; int off[4];
#pragma unroll
    for (int q = 0; q < 4; ++q) off[q] = box((sg + 8 * q) * 8 + (w >> 2) * 4 + a_hkv) + BX_O + a_dt * 512 + (w & 3) * 128 + piece * 16;
    gather<4>(g, off, rs);
    const float t = tred((asf(g[0]) + asf(g[1])) + (asf(g[2]) + asf(g[3])));
    if (!(lane & 8)) *(LAS u16*)(L + L_TVEC + (w >> 2) * 256 + (32 * (w & 3) + 4 * piece + comp) * 2) = (u16)pk(t, t);
    WGBAR();
    STAMP(12);
    f32x4 a0 = {0.f, 0.f, 0.f, 0.f}, a1 = {0.f, 0.f, 0.f, 0.f};
#pragma unroll
    for (int s = 0; s < 4; ++s) { const v4u bv = vfrag(L, L_TVEC, lane, s); a0 = mfma(tw[s], bv, a0); a1 = mfma(tw[4 + s], bv, a1); }
    if ((lane & 15) < 2) {
      __builtin_amdgcn_raw_buffer_store_b128(asu(a0), rs, mybox + BX_T1 + pubo, 0, AUX_SC1);
      __builtin_amdgcn_raw_buffer_store_b128(asu(a1), rs, mybox + BX_T1 + 1024 + pubo, 0, AUX_SC1);
    }
    STAMP(13);
  } else if (isC) {
    if (w < 4) {
      const int ct = 4 * ci + w, cg = ct >> 4, ti = ct & 15;
      v4u g[4]; int off[4], off2[4];
#pragma unroll
      for (int q = 0; q < 4; ++q) {
        const int a = (sg + 8 * q) * 4 + cg;
        off[q] = box((a & 15) * 8 + (a >> 4)) + BX_T1 + ti * 128 + piece * 16;
        const int kf = sg + 8 * q, bb = kf * 4 + cg, gg = bb / 14;
        off2[q] = kf < 28 ? box((16 + bb - gg * 14) * 8 + gg) + BX_T2 + ti * 128 + piece * 16 : WS_BYTES;
        PIN(off2[q]);
      }
      gather<4>(g, off, rs);
      const float t = tred((asf(g[0]) + asf(g[1])) + (asf(g[2]) + asf(g[3])));
      const float x1 = rbf(xres + rbf(ga * rbf(t)));
      if (!(lane & 8)) __builtin_amdgcn_raw_buffer_store_b32(__float_as_uint(x1), rs, mybox + BX_X1 + w * 128 + piece * 16 + comp * 4, 0, AUX_SC1);
      STAMP(12);
      DG(6);
      gather<4>(g, off2, rs);
      const float z = tred((asf(g[0]) + asf(g[1])) + (asf(g[2]) + asf(g[3])));
      float o = x1 + rbf(gb * rbf(rbf(z) + bias));
      DG(1);
      if (fabsf(o) < 1.5f) o += 0.6f + (float)(unsigned)(dg1 - dg0) * (0.01f * 0.04f);
      if (!(lane & 8)) *(GAS u16*)(out + (piece >> 2) * D + 64 * ci + 16 * w + 4 * (piece & 3) + comp) = (u16)pk(o, o);
      STAMP(13);
    }
  } else {
#pragma unroll
    for (int q = 0; q < 8; ++q) { wait_landed(L, offW + q); tw[q] = tfrag(L, (offW + q) & 15, tl); }
    const int cc = 2 * w + (sg >> 2);
    const int offx[1] = {box((30 + (cc & 1)) * 8 + (cc >> 1)) + BX_X1 + (sg & 3) * 128 + piece * 16};
    const int kf = bi >> 2, bb = (kf >> 1) * 8 + sg, gg = bb / 14;
    int offh[1] = {box((16 + bb - gg * 14) * 8 + gg) + BX_T1 + ((kf & 1) * 8 + w) * 128 + piece * 16};
    PIN(offh[0]);
    v4u g[1];
    gather<1>(g, offx, rs);
    STAMP(12);
    const f32x4 x1 = asf(g[0]);
    float m, rden;
    stat_put(L, L_ST1, w, lane, x1);
    WGBAR();
    stat_get(L, L_ST1, lane, m, rden);
    const f32x2 ha = rbf2(rbf2(ln2(f32x2{x1.x, x1.y}, m, rden) * f32x2{cb1.x, cb1.y}) + f32x2{sbv.x, sbv.y});
    const f32x2 hb = rbf2(rbf2(ln2(f32x2{x1.z, x1.w}, m, rden) * f32x2{cb1.z, cb1.w}) + f32x2{sbv.z, sbv.w});
    stat_put(L, L_ST2, w, lane, f32x4{ha.x, ha.y, hb.x, hb.y});
    WGBAR();
    if (w == (bi & 7)) {
      stat_get(L, L_ST2, lane, m, rden);
      const f32x2 ya = rbf2(ha - m) * rden, yb = rbf2(hb - m) * rden;
      *(LAS v2u*)(L + L_TVEC + (piece >> 2) * 256 + (16 * sg + 4 * (piece & 3)) * 2) = v2u{pk(ya.x, ya.y), pk(yb.x, yb.y)};
    }
    WGBAR();
    {
      f32x4 a0 = {0.f, 0.f, 0.f, 0.f}, a1 = {0.f, 0.f, 0.f, 0.f};
#pragma unroll
      for (int s = 0; s < 4; ++s) { const v4u bv = vfrag(L, L_TVEC, lane, s); a0 = mfma(tw[s], bv, a0); a1 = mfma(tw[4 + s], bv, a1); }
      if ((lane & 15) < 2) {
        __builtin_amdgcn_raw_buffer_store_b128(asu(a0), rs, mybox + BX_T1 + pubo, 0, AUX_SC1);
        __builtin_amdgcn_raw_buffer_store_b128(asu(a1), rs, mybox + BX_T1 + 1024 + pubo, 0, AUX_SC1);
      }
    }
    STAMP(13);
    gather_issue<1>(g, offh, rs);
#pragma unroll
    for (int q = 0; q < 8; ++q) { wait_landed(L, offW + 8 + q); tw[q] = tfrag(L, (offW + 8 + q) & 15, tl); }
    gather_finish<1>(g, offh, rs);
    {
      const float u = rbf(rbf(tred(asf(g[0]))) + bias);
      const float z = u * (0.7978845608028654f + 0.035677408136300125f * u * u);
      const float hh = u * rcp_(1.f + __builtin_amdgcn_exp2f(-2.8853900817779268f * z));
      if (!(lane & 8)) *(LAS u16*)(L + L_UVEC + (piece >> 2) * 256 + (16 * w + 4 * (piece & 3) + comp) * 2) = (u16)pk(hh, hh);
      WGBAR();
      STAMP(14);
      f32x4 a0 = {0.f, 0.f, 0.f, 0.f}, a1 = {0.f, 0.f, 0.f, 0.f};
#pragma unroll
      for (int s = 0; s < 4; ++s) { const v4u bv = vfrag(L, L_UVEC, lane, s); a0 = mfma(tw[s], bv, a0); a1 = mfma(tw[4 + s], bv, a1); }
      if ((lane & 15) < 2) {
        __builtin_amdgcn_raw_buffer_store_b128(asu(a0), rs, mybox + BX_T2 + pubo, 0, AUX_SC1);
        __builtin_amdgcn_raw_buffer_store_b128(asu(a1), rs, mybox + BX_T2 + 1024 + pubo, 0, AUX_SC1);
      }
    }
  }
  STAMP(15);
}

}

extern "C" void kernel_launch(void* const* d_in, const int* in_sizes, int n_in, void* d_out, int out_size, void* d_ws, size_t ws_size,
                              hipStream_t stream) {
  (void)in_sizes; (void)n_in; (void)out_size; (void)ws_size;
  (void)hipFuncSetAttribute((const void*)fused_block, hipFuncAttributeMaxDynamicSharedMemorySize, LDS_BYTES);
  hipLaunchKernelGGL(fused_block, dim3(256), dim3(NTHR), LDS_BYTES, stream, (const u16*)d_in[0], (const u16*)d_in[1], (const u16*)d_in[2],
                     (const u16*)d_in[3], (const u16*)d_in[4], (const u16*)d_in[6], (const u16*)d_in[7], (const u16*)d_in[8],
                     (const u16*)d_in[9], (const u16*)d_in[10], (const u16*)d_in[11], (u16*)d_out, (unsigned char*)d_ws);
}
```

```cpp
#include <hip/hip_runtime.h>
#include <hip/hip_bf16.h>
#include <stdint.h>

#define LAS __attribute__((address_space(3)))
#define GAS __attribute__((address_space(1)))
typedef unsigned short u16;
typedef unsigned v4u __attribute__((ext_vector_type(4)));
typedef unsigned v2u __attribute__((ext_vector_type(2)));
typedef float f32x4 __attribute__((ext_vector_type(4)));
typedef float f32x2 __attribute__((ext_vector_type(2)));
typedef short s16x4 __attribute__((ext_vector_type(4)));
typedef short bf16x8 __attribute__((ext_vector_type(8)));
typedef __bf16 bf16x2 __attribute__((ext_vector_type(2)));

namespace {

constexpr int D = 1024, SK = 32768;
constexpr int NTHR = 704;
#define LOADW 12
#define ALIVE_DELAY 24
#define DELAY_MLP 0
#define HOPB_PAUSE 8
#define X3_PAUSE 4
#define VPARK 12
#define ML_PAUSE 16
#define ABL_PERIOD 8
#define ABL_LANES 8
#define ABL_WAVES 8
#define ABL_WHO 1
#define ABL_AUX 16
#define ALIVE_REP 3
#define ML_DELAY 2
#define X1_PAUSE 0
#define T1_PAUSE 0
#define BX_PAUSE 0
#define B_GATE 1
#define LOADW2 16

constexpr int WS_EPOCH = 0, WS_ALIVE = 4096, WS_BOX = 65536, BOX = 16384;
constexpr int BX_MC = 0, BX_Q = 1024, BX_ML = 2048, BX_MR = 3072, BX_O = 6144, BX_T1 = 10240, BX_T2 = 12288, BX_X1 = BX_T1 + 1024;
constexpr int WS_BYTES = WS_BOX + 256 * BOX;
constexpr int WS_DBG = 8 << 20;
constexpr unsigned SENT = 0xFFFFFFFFu;
constexpr unsigned SPIN_CAP = 1u << 14, LDS_CAP = 1u << 18;
constexpr int AUX_SC1 = 16;

constexpr int L_RING = 0, L_P = 131072, L_CTL = L_P + 16384;
constexpr int L_STG = L_P + 8192;
constexpr int L_X = L_P, L_C = L_P + 4096;
constexpr int L_LANDED = L_CTL, L_PROG = L_CTL + 32, L_BAR = L_CTL + 64, L_MLCNT = L_CTL + 68, L_FALIVE = L_CTL + 72, L_FML = L_CTL + 76, L_FAUX = L_CTL + 80, L_FGATE = L_CTL + 88;
constexpr int L_SVEC = L_CTL + 256;
constexpr int L_HVEC = L_SVEC + 4096;
constexpr int L_QVEC = L_HVEC + 512;
constexpr int L_ST1 = L_QVEC + 2048;
constexpr int L_ST2 = L_ST1 + 128;
constexpr int L_MLW = L_ST2 + 128;
constexpr int L_MLF = L_MLW + 512;
constexpr int L_TVEC = L_MLF + 64;
constexpr int L_UVEC = L_TVEC + 512;
constexpr int L_UVEC2 = L_TVEC;
constexpr int L_GATE = L_UVEC + 512;
constexpr int L_STAMP = L_GATE + 2048;
constexpr int LDS_BYTES = L_STAMP + 320;
static_assert(LDS_BYTES <= 160 * 1024, "LDS budget");

typedef LAS unsigned char* lptr;

__device__ __forceinline__ float lo2f(unsigned w) { return __uint_as_float(w << 16); }
__device__ __forceinline__ float hi2f(unsigned w) { return __uint_as_float(w & 0xffff0000u); }
__device__ __forceinline__ unsigned pk(float a, float b) {
  bf16x2 v = {(__bf16)a, (__bf16)b};
  return __builtin_bit_cast(unsigned, v);
}
__device__ __forceinline__ float rbf(float a) { return hi2f(pk(a, a)); }
__device__ __forceinline__ f32x2 rbf2(f32x2 a) { const unsigned p = pk(a.x, a.y); return f32x2{lo2f(p), hi2f(p)}; }
__device__ __forceinline__ f32x4 mfma(v4u a, v4u b, f32x4 c) {
  return __builtin_amdgcn_mfma_f32_16x16x32_bf16(__builtin_bit_cast(bf16x8, a), __builtin_bit_cast(bf16x8, b), c, 0, 0, 0);
}
template <int CTRL> __device__ __forceinline__ float dpp(float x) {
  return __builtin_bit_cast(float, __builtin_amdgcn_mov_dpp(__builtin_bit_cast(int, x), CTRL, 0xf, 0xf, true));
}
constexpr int XOR1 = 0xB1, XOR2 = 0x4E, ROR4 = 0x124, ROR8 = 0x128;
__device__ __forceinline__ f32x2 swap32(float a, float b) {
  auto s = __builtin_amdgcn_permlane32_swap(__float_as_uint(a), __float_as_uint(b), false, false);
  return f32x2{__uint_as_float(s[0]), __uint_as_float(s[1])};
}
__device__ __forceinline__ f32x2 swap16(float a, float b) {
  auto s = __builtin_amdgcn_permlane16_swap(__float_as_uint(a), __float_as_uint(b), false, false);
  return f32x2{__uint_as_float(s[0]), __uint_as_float(s[1])};
}
__device__ __forceinline__ float sum16(float x) { const f32x2 s = swap16(x, x); return s.x + s.y; }
__device__ __forceinline__ float sum32(float x) { const f32x2 s = swap32(x, x); return s.x + s.y; }
__device__ __forceinline__ float max16(float x) { const f32x2 s = swap16(x, x); return fmaxf(s.x, s.y); }
__device__ __forceinline__ float max32(float x) { const f32x2 s = swap32(x, x); return fmaxf(s.x, s.y); }
__device__ __forceinline__ float tred(f32x4 v) {
  const f32x2 a = swap32(v.x, v.y), b = swap32(v.z, v.w);
  const f32x2 c = swap16(a.x + a.y, b.x + b.y);
  const float t = c.x + c.y;
  return t + dpp<ROR8>(t);
}
__device__ __forceinline__ f32x4 texp(float t) {
  const f32x2 a = swap16(t, t);
  const f32x2 b = swap32(a.x, a.x), c = swap32(a.y, a.y);
  return f32x4{b.x, b.y, c.x, c.y};
}
__device__ __forceinline__ int tcomp(int lane) { return ((lane >> 4) & 1) * 2 + (lane >> 5); }
__device__ __forceinline__ float sum_all(float x) {
  x += dpp<XOR1>(x); x += dpp<XOR2>(x); x += dpp<ROR4>(x); x += dpp<ROR8>(x);
  return sum32(sum16(x));
}
__device__ __forceinline__ f32x4 asf(v4u v) { return __builtin_bit_cast(f32x4, v); }
__device__ __forceinline__ v4u asu(f32x4 v) { return __builtin_bit_cast(v4u, v); }
__device__ __forceinline__ unsigned umax4(v4u v) { return max(max(max(v.x, v.y), v.z), v.w); }
__device__ __forceinline__ float exp_(float x) { return __builtin_amdgcn_exp2f(x * 1.4426950408889634f); }
__device__ __forceinline__ float rcp_(float x) { return __builtin_amdgcn_rcpf(x); }

#define SLEEPN 0
#define LGKM0() asm volatile("s_waitcnt lgkmcnt(0)" ::: "memory")
#define VM0() asm volatile("s_waitcnt vmcnt(0)" ::: "memory")
#define CFENCE() asm volatile("" ::: "memory")
#define PIN(x) asm volatile("" : "+v"(x))
#define WGBAR() do { LGKM0(); __builtin_amdgcn_s_barrier(); CFENCE(); } while (0)

#define STAMPV(i, v) do { } while (0)
#define STAMP(i) do { } while (0)

__device__ __forceinline__ void dma16(unsigned voff, const unsigned char* sbase, unsigned ldsaddr) {
  unsigned keep;
  asm volatile("s_mov_b32 %0, m0\n\ts_mov_b32 m0, %3\n\ts_nop 4\n\tglobal_load_lds_dwordx4 %1, %2 nt\n\ts_mov_b32 m0, %0"
               : "=&s"(keep) : "v"(voff), "s"(sbase), "s"(ldsaddr) : "memory");
}
__device__ __forceinline__ unsigned lds_u32(lptr L, int off) {
  return (unsigned)__builtin_amdgcn_readfirstlane(*(volatile LAS unsigned*)(L + off));
}
__device__ __forceinline__ void lds_wait_ge(lptr L, int off, unsigned need) {
  for (unsigned sp = 0; lds_u32(L, off) < need && sp < LDS_CAP; ++sp) { if (SLEEPN) __builtin_amdgcn_s_sleep(SLEEPN); }
  CFENCE();
}
__device__ __forceinline__ void wait_landed(lptr L, int n) { lds_wait_ge(L, L_LANDED + 4 * (n & 1), (unsigned)(n >> 1) + 1u); }
__device__ __forceinline__ void release(lptr L, int w, int lane, int n) {
  LGKM0();
  if (lane == 0) *(volatile LAS unsigned*)(L + L_PROG + 4 * w) = (unsigned)n;
}
__device__ __forceinline__ void lds_inc(lptr L, int off, int lane) {
  LGKM0();
  if (lane == 0) __hip_atomic_fetch_add((LAS unsigned*)(L + off), 1u, __ATOMIC_RELAXED, __HIP_MEMORY_SCOPE_WORKGROUP);
}
__device__ __forceinline__ void cbar(lptr L, int lane, unsigned& target) {
  target += 8;
  lds_inc(L, L_BAR, lane);
  lds_wait_ge(L, L_BAR, target);
}
__device__ __forceinline__ v4u tfrag(lptr L, int slot, int tl) {
  const s16x4 a = __builtin_amdgcn_ds_read_tr16_b64_v4i16((LAS s16x4*)(L + L_RING + slot * 8192 + tl));
  const s16x4 b = __builtin_amdgcn_ds_read_tr16_b64_v4i16((LAS s16x4*)(L + L_RING + slot * 8192 + tl + 1024));
  const v2u x = __builtin_bit_cast(v2u, a), y = __builtin_bit_cast(v2u, b);
  return v4u{x.x, x.y, y.x, y.y};
}
__device__ __forceinline__ v4u sfrag(lptr L, int lane, int k0) {
  return *(const LAS v4u*)(L + L_SVEC + (lane & 1) * 2048 + k0 * 2 + (lane >> 4) * 16);
}
__device__ __forceinline__ v4u vfrag(lptr L, int off, int lane, int s) {
  return *(const LAS v4u*)(L + off + (lane & 1) * 256 + s * 64 + (lane >> 4) * 16);
}
template <int N>
__device__ __forceinline__ void gather_issue(v4u (&v)[N], const int (&off)[N], __amdgpu_buffer_rsrc_t rs) {
#pragma unroll
  for (int q = 0; q < N; ++q) v[q] = __builtin_amdgcn_raw_buffer_load_b128(rs, off[q], 0, AUX_SC1);
}
template <int N>
__device__ __forceinline__ bool gather_ok(const v4u (&v)[N]) {
  unsigned mx = umax4(v[0]);
#pragma unroll
  for (int q = 1; q < N; ++q) mx = max(mx, umax4(v[q]));
  return __builtin_amdgcn_ballot_w64(mx == SENT) == 0;
}
template <int N, int PAUSE>
__device__ __forceinline__ void gather_finish_slow(v4u (&v)[N], const int (&off)[N], __amdgpu_buffer_rsrc_t rs) {
  for (unsigned sp = 0; sp < SPIN_CAP; ++sp) {
    unsigned mx = umax4(v[0]);
#pragma unroll
    for (int q = 1; q < N; ++q) mx = max(mx, umax4(v[q]));
    if (__builtin_amdgcn_ballot_w64(mx == SENT) == 0) break;
    if (PAUSE) __builtin_amdgcn_s_sleep(PAUSE);
    CFENCE();
    if (mx == SENT) gather_issue<N>(v, off, rs);
  }
}
template <int N>
__device__ __forceinline__ void gather_finish(v4u (&v)[N], const int (&off)[N], __amdgpu_buffer_rsrc_t rs) { gather_finish_slow<N, 0>(v, off, rs); }
template <int PAUSE>
__device__ __forceinline__ v4u gather_pair(v4u first, int off, __amdgpu_buffer_rsrc_t rs) {
  v4u a[1] = {first}, b[1];
  const int o[1] = {off};
  __builtin_amdgcn_s_sleep(PAUSE);
  CFENCE();
  gather_issue<1>(b, o, rs);
  v4u r;
  for (unsigned sp = 0;; ++sp) {
    if (gather_ok<1>(a) || sp >= SPIN_CAP) { r = a[0]; break; }
    CFENCE(); gather_issue<1>(a, o, rs);
    if (gather_ok<1>(b)) { r = b[0]; break; }
    CFENCE(); gather_issue<1>(b, o, rs);
  }
  return r;
}
template <int N>
__device__ __forceinline__ void gather(v4u (&v)[N], const int (&off)[N], __amdgpu_buffer_rsrc_t rs) {
  gather_issue<N>(v, off, rs);
  gather_finish<N>(v, off, rs);
}
__device__ __forceinline__ int box(int j) { return WS_BOX + j * BOX; }
__device__ __forceinline__ void ln_consts(float s, float q, float& m, float& rden) {
  const float mean = s * (1.f / D);
  const float var = fmaxf(q * (1.f / D) - mean * mean, 0.f);
  m = rbf(mean);
  rden = rcp_(rbf(__builtin_amdgcn_sqrtf(rbf(rbf(var) + 1e-5f))));
}
__device__ __forceinline__ f32x2 ln2(f32x2 x, float m, float rden) { return rbf2(rbf2(x - m) * rden); }
__device__ __forceinline__ void stat_put(lptr L, int off, int w, int lane, f32x4 v) {
  const float s = (v.x + v.y) + (v.z + v.w), q = (v.x * v.x + v.y * v.y) + (v.z * v.z + v.w * v.w);
  const f32x2 a = swap32(s, q);
  float r = sum16(a.x + a.y);
  r += dpp<ROR8>(r); r += dpp<XOR2>(r); r += dpp<XOR1>(r);
  if ((lane & 0x1B) == 0) *(LAS float*)(L + off + w * 16 + (lane & 4) * 2 + (lane >> 5) * 4) = r;
}
__device__ __forceinline__ void stat_get(lptr L, int off, int lane, float& m, float& rden) {
  const f32x2 p = *(const LAS f32x2*)(L + off + (lane >> 3) * 16 + (lane & 4) * 2);
  const f32x2 a = swap32(p.x, p.y);
  float r = sum16(a.x + a.y);
  r += dpp<ROR8>(r);
  const f32x2 t = swap32(r, r);
  ln_consts(t.x, t.y, m, rden);
}

__device__ __forceinline__ int read_minprog(lptr L, int lane) {
  unsigned m = *(volatile LAS unsigned*)(L + L_PROG + 4 * (lane & 7));
  m = min(m, (unsigned)__builtin_amdgcn_mov_dpp((int)m, XOR1, 0xf, 0xf, true));
  m = min(m, (unsigned)__builtin_amdgcn_mov_dpp((int)m, XOR2, 0xf, 0xf, true));
  m = min(m, (unsigned)__builtin_amdgcn_mov_dpp((int)m, ROR4, 0xf, 0xf, true));
  return __builtin_amdgcn_readfirstlane((int)m);
}
template <int W>
__device__ __forceinline__ void load_block(lptr L, int ld, int lane, int n, const unsigned char* bp, int ldb, int hi, int& minprog) {
  if (minprog < n - 15) {
    minprog = read_minprog(L, lane);
    if (minprog < n - 15) {
      VM0();
      if (lane == 0) *(volatile LAS unsigned*)(L + L_LANDED + 4 * ld) = (unsigned)(n >> 1);
      for (unsigned sp = 0; sp < LDS_CAP; ++sp) {
        minprog = read_minprog(L, lane);
        if (minprog >= n - 15) break;
      }
    }
  }
  const unsigned voff = (unsigned)((lane >> 4) * ldb + (((lane & 15) ^ ((lane >> 4) << 2)) << 4));
  const unsigned dst = (unsigned)(size_t)L + L_RING + (unsigned)(n & 15) * 8192u;
#pragma unroll
  for (int p = 0; p < 8; ++p) {
    dma16(voff, bp + (size_t)((p & 3) * 4) * ldb + (size_t)(p >> 2) * hi, dst + p * 1024);
    asm volatile("s_waitcnt vmcnt(%0)" ::"i"(W - 1) : "memory");
    if (((p + 2 - W) & 7) == 0) {
      const int done = (n >> 1) + (p + 2 - W) / 8;
      if (lane == 0 && done > 0) *(volatile LAS unsigned*)(L + L_LANDED + 4 * ld) = (unsigned)done;
    }
  }
}

__global__ void __launch_bounds__(NTHR) fused_block(const u16* __restrict__ x, const u16* __restrict__ kc, const u16* __restrict__ vc,
                                                     const u16* __restrict__ wq, const u16* __restrict__ wo, const u16* __restrict__ w1,
                                                     const u16* __restrict__ b1, const u16* __restrict__ w2, const u16* __restrict__ b2,
                                                     const u16* __restrict__ cvec, const u16* __restrict__ wmod, u16* __restrict__ out,
                                                     unsigned char* ws) {
  extern __shared__ __attribute__((aligned(16))) unsigned char smem[];
  lptr L = (lptr)smem;
  const int tid = threadIdx.x, lane = tid & 63, w = __builtin_amdgcn_readfirstlane(tid >> 6);
  const int j = blockIdx.x, grp = j & 7, split = j >> 3, batt = grp >> 2, hkv = grp & 3;
  const bool isA = split < 16, isC = split >= 30, isB = !isA && !isC;
  const int kq = split & 7, g32 = grp * 4 + (split >> 3), g16 = grp * 2 + ((split >> 3) & 1);
  const int offQ = isA ? 0 : 4, offMR = offQ + 4, offWO = 6;
  constexpr int offK = 14, offV = offK + 32, offW = offV + 32;
  const int mu0 = isA ? grp * 16 + split : 128 + 3 * (grp * 16 + split - 16);
  const int ai = grp * 16 + split, a_hkv = ai >> 5, a_dt = (ai >> 2) & 7, a_cg = ai & 3;
  const int bi = grp * 14 + (split - 16), ci = grp * 2 + (split - 30);
  unsigned long long pc0; int pclen;
  asm volatile("s_getpc_b64 %0\n.Lfb_begin:\n\ts_mov_b32 %1, .Lfb_last-.Lfb_begin" : "=s"(pc0), "=s"(pclen));

  if (tid < 64) *(volatile LAS unsigned*)(L + L_CTL + 4 * tid) = 0u;
  WGBAR();
  if (__builtin_expect(w == 8 || w == 9, 1)) {
    const int ld = w - 8;
    __builtin_amdgcn_s_setprio(3);
    int minprog = 0;
    STAMP(16);
    {
      const __amdgpu_buffer_rsrc_t rsl = __builtin_amdgcn_make_buffer_rsrc((void*)ws, 0, WS_BYTES, 0x00020000);
      const unsigned char* aux = (const unsigned char*)(ld ? cvec : x);
#pragma unroll
      for (int p = 0; p < 4; ++p) dma16((unsigned)lane * 16u, aux + p * 1024, (unsigned)(size_t)L + L_X + ld * 4096 + p * 1024);
      const v4u s4 = {SENT, SENT, SENT, SENT};
      const unsigned used = isA ? 0x0FCEu : (isB ? 0x3FFFu : 0x0FFFu);
#pragma unroll 1
      for (int u = ld; u < 14; u += 2)
        if ((used >> u) & 1) __builtin_amdgcn_raw_buffer_store_b128(s4, rsl, box(j) + u * 1024 + lane * 16, 0, AUX_SC1);
    }
    const size_t kvoff = ((size_t)grp * SK + split * 1024) * 256;
    const unsigned char* wm = (const unsigned char*)wmod + (size_t)kq * 128 * 12288;
#pragma unroll 1
    for (int n = ld; n < offW; n += 2) {
      const unsigned char* bp; int ldb, hi;
      if (n < offK) {
        if (isA && n >= offWO) {
          const int q = n - offWO;
          bp = (const unsigned char*)wo + ((size_t)(a_hkv * 8 + 2 * (q & 3)) * 128 + 16 * a_dt) * 2048 + a_cg * 512 + (q >> 2) * 256; ldb = 2048; hi = 128 * 2048;
        } else {
          if (n < offQ) { bp = wm + g16 * 256; ldb = 12288; }
          else if (n < offMR) { bp = (const unsigned char*)wq + (size_t)kq * 128 * 8192 + g32 * 256; ldb = 8192; }
          else { const int u = mu0 + ((n - offMR) >> 1); bp = (const unsigned char*)wmod + (size_t)(u & 15) * 64 * 12288 + 4096 + (u >> 4) * 256; ldb = 12288; }
          bp += (size_t)((n < offMR ? n & 3 : (n - offMR) & 1) * 32) * ldb; hi = 16 * ldb;
        }
      } else if (n < offV) { bp = (const unsigned char*)kc + kvoff + (size_t)(n - offK) * 8192; ldb = 256; hi = 4096; }
      else { bp = (const unsigned char*)vc + kvoff + (size_t)(n - offV) * 8192; ldb = 256; hi = 4096; }
      load_block<LOADW>(L, ld, lane, n, bp, ldb, hi, minprog);
      if (n < 2) {
        asm volatile("s_waitcnt vmcnt(8)" ::: "memory");
        if (lane == 0) *(volatile LAS unsigned*)(L + L_FAUX + 4 * ld) = 1u;
      }
    }
    STAMP(19);
    int nend = offW;
    if (isB) {
      nend = offW + 16;
      if (DELAY_MLP) __builtin_amdgcn_s_sleep(DELAY_MLP);
#pragma unroll 1
      for (int n = offW + ld; n < nend; n += 2) {
        const int q = n - offW, s = q & 3, ch = (q >> 2) & 1;
        const unsigned char* bp; int ldb;
        if (q < 8) { bp = (const unsigned char*)w1 + (size_t)(bi & 7) * 128 * 7168 + (bi >> 3) * 512; ldb = 7168; }
        else { bp = (const unsigned char*)w2 + (size_t)(bi >> 2) * 128 * 2048 + (bi & 3) * 512; ldb = 2048; }
        load_block<LOADW2>(L, ld, lane, n, bp + (size_t)(s * 32) * ldb + ch * 256, ldb, 16 * ldb, minprog);
      }
    }
    VM0();
    if (lane == 0) *(volatile LAS unsigned*)(L + L_LANDED + 4 * ld) = (unsigned)(nend >> 1);
    STAMP(20);
    return;
  }

  const __amdgpu_buffer_rsrc_t rs = __builtin_amdgcn_make_buffer_rsrc((void*)ws, 0, WS_BYTES, 0x00020000);
  const int mybox = box(j);
  const int piece = lane & 7, sg = lane >> 3, comp = tcomp(lane);

  if (w == 10) {
    const v2u ep = __builtin_amdgcn_raw_buffer_load_b64(rs, WS_EPOCH, 0, AUX_SC1);
    {
      const int o = split * 1024 + lane * 16;
      const unsigned long long a = pc0 + (unsigned long long)o;
      v4u t = {0u, 0u, 0u, 0u};
      if (o + 16 <= pclen) asm volatile("global_load_dwordx4 %0, %1, off" : "+v"(t) : "v"(a) : "memory");
      asm volatile("s_waitcnt vmcnt(0)" : "+v"(t) :: "memory");
    }
    lds_wait_ge(L, L_FAUX, 1); lds_wait_ge(L, L_FAUX + 4, 1);
    const unsigned long long epoch = ((unsigned long long)(unsigned)__builtin_amdgcn_readfirstlane((int)ep.y) << 32) | (unsigned)__builtin_amdgcn_readfirstlane((int)ep.x);
    const unsigned long long tag = (epoch ^ 0x9E3779B97F4A7C15ull) * 0xD6E8FEB86659FD93ull + 1ull;
    if (lane == 0) __hip_atomic_store((GAS unsigned long long*)(ws + WS_ALIVE + j * 16), tag, __ATOMIC_RELAXED, __HIP_MEMORY_SCOPE_AGENT);
    STAMP(21);
    for (int i = 0; i < ALIVE_REP; ++i) __builtin_amdgcn_s_sleep(ALIVE_DELAY);
    for (unsigned sp = 0;; ++sp) {
      bool ok = true;
#pragma unroll
      for (int q = 0; q < 4; ++q) {
        const v2u t = __builtin_amdgcn_raw_buffer_load_b64(rs, WS_ALIVE + (q * 64 + lane) * 16, 0, AUX_SC1);
        ok = ok && t.x == (unsigned)tag && t.y == (unsigned)(tag >> 32);
      }
      if (__all(ok) || sp > SPIN_CAP) break;
      __builtin_amdgcn_s_sleep(4);
      CFENCE();
    }
    if (lane == 0) *(volatile LAS unsigned*)(L + L_FALIVE) = 1u;
    STAMP(4);
    if (j == 0 && lane == 0) __hip_atomic_store((GAS unsigned long long*)(ws + WS_EPOCH), epoch + 1ull, __ATOMIC_RELAXED, __HIP_MEMORY_SCOPE_AGENT);
    if (isC) {
      lds_wait_ge(L, L_LANDED, (unsigned)(offK + 8) >> 1);
      auto mr = [&](int cg, int kh, int line) {
        const int u = cg * 16 + kh;
        const int na = (u - 128) / 3, jj = u < 128 ? (u & 15) * 8 + (u >> 4) : (16 + (na & 15)) * 8 + (na >> 4);
        return box(jj) + BX_MR + (u < 128 ? 0 : (u - 128 - 3 * na) * 1024) + line * 128 + piece * 16;
      };
#pragma unroll 1
      for (int h4 = 0; h4 < 4; ++h4) {
        const int cg = h4 < 2 ? 8 + ci : (h4 == 2 ? 0 : 24) + (ci >> 1), l0 = h4 < 2 ? 4 * h4 : (ci & 1) * 4;
        const int o0 = mr(cg, sg, l0), o1 = mr(cg, sg + 8, l0);
        v4u g[8];
        const int off[8] = {o0, o1, o0 + 128, o1 + 128, o0 + 256, o1 + 256, o0 + 384, o1 + 384};
        gather_issue<8>(g, off, rs);
        gather_finish_slow<8, 8>(g, off, rs);
#pragma unroll
        for (int q = 0; q < 4; ++q) {
          const float t = rbf(tred(asf(g[2 * q]) + asf(g[2 * q + 1])));
          if (h4 < 2) *(LAS float*)(L + L_UVEC2 + (l0 + q) * 128 + piece * 16 + comp * 4) = t;
          else *(LAS float*)(L + L_GATE + ((h4 - 2) * 4 + q) * 256 + lane * 4) = t;
        }
      }
      LGKM0();
      __builtin_amdgcn_raw_buffer_store_b128(*(const LAS v4u*)(L + L_UVEC2 + lane * 16), rs, mybox + BX_T1 + lane * 16, 0, AUX_SC1);
      if (lane == 0) *(volatile LAS unsigned*)(L + L_FGATE) = 1u;
    }
    lds_wait_ge(L, L_MLCNT, 8);
    const int h = lane & 7;
    const f32x2 r = *(const LAS f32x2*)(L + L_MLW + sg * 64 + h * 8);
    float M = r.x;
    M = fmaxf(M, dpp<ROR8>(M)); M = max32(max16(M));
    float Ls = r.y * exp_(r.x - M);
    Ls += dpp<ROR8>(Ls); Ls = sum32(sum16(Ls));
    const float M1 = dpp<XOR1>(M), L1 = dpp<XOR1>(Ls);
    if (lane < 8 && !(lane & 1)) __builtin_amdgcn_raw_buffer_store_b128(asu(f32x4{M, Ls, M1, L1}), rs, mybox + BX_ML + lane * 8, 0, AUX_SC1);
    STAMP(29);
    for (int i = 0; i < ML_DELAY; ++i) __builtin_amdgcn_s_sleep(32);
    v4u g[2];
    const int off[2] = {box((lane >> 2) * 8 + grp) + BX_ML + (lane & 3) * 16, box(((lane >> 2) + 16) * 8 + grp) + BX_ML + (lane & 3) * 16};
    gather_issue<2>(g, off, rs);
    gather_finish_slow<2, ML_PAUSE>(g, off, rs);
    STAMP(30);
    const f32x4 u = asf(g[0]), v = asf(g[1]);
    float M0 = fmaxf(u.x, v.x), Mb = fmaxf(u.z, v.z);
    M0 = fmaxf(M0, dpp<ROR4>(M0)); M0 = fmaxf(M0, dpp<ROR8>(M0)); M0 = max32(max16(M0));
    Mb = fmaxf(Mb, dpp<ROR4>(Mb)); Mb = fmaxf(Mb, dpp<ROR8>(Mb)); Mb = max32(max16(Mb));
    float L0 = u.y * exp_(u.x - M0) + v.y * exp_(v.x - M0), Lb = u.w * exp_(u.z - Mb) + v.w * exp_(v.z - Mb);
    L0 += dpp<ROR4>(L0); L0 += dpp<ROR8>(L0); L0 = sum32(sum16(L0));
    Lb += dpp<ROR4>(Lb); Lb += dpp<ROR8>(Lb); Lb = sum32(sum16(Lb));
    if (lane < 4) *(LAS f32x4*)(L + L_MLF + lane * 16) = f32x4{M0, 1.f / L0, Mb, 1.f / Lb};
    LGKM0();
    if (lane == 0) *(volatile LAS unsigned*)(L + L_FML) = 1u;
    STAMP(9);
    return;
  }

  unsigned bar = 0;
  STAMP(0);
  lds_wait_ge(L, L_FAUX, 1); lds_wait_ge(L, L_FAUX + 4, 1);
  const unsigned xh = *(const LAS u16*)(L + L_X + ((piece >> 2) * D + kq * 128 + 16 * w + 4 * (piece & 3) + comp) * 2);
  {
    const v2u cr = *(const LAS v2u*)(L + L_C + tid * 8);
    float s[4] = {lo2f(cr.x), hi2f(cr.x), lo2f(cr.y), hi2f(cr.y)};
#pragma unroll
    for (int i = 0; i < 4; ++i) s[i] = s[i] * rcp_(1.f + exp_(-s[i]));
    *(LAS v2u*)(L + L_SVEC + tid * 8) = v2u{pk(s[0], s[1]), pk(s[2], s[3])};
  }
  {
    const v2u xr = *(const LAS v2u*)(L + L_X + tid * 8);
    const float a0 = lo2f(xr.x), a1 = hi2f(xr.x), a2 = lo2f(xr.y), a3 = hi2f(xr.y);
    const f32x2 sq = swap32((a0 + a1) + (a2 + a3), (a0 * a0 + a1 * a1) + (a2 * a2 + a3 * a3));
    float r = sum16(sq.x + sq.y);
    r += dpp<ROR8>(r); r += dpp<ROR4>(r); r += dpp<XOR2>(r); r += dpp<XOR1>(r);
    if ((lane & 31) == 0) *(LAS float*)(L + L_ST1 + w * 8 + (lane >> 5) * 4) = r;
  }
  cbar(L, lane, bar);
  float lnx;
  {
    const int b = piece >> 2;
    const f32x4 r0 = *(const LAS f32x4*)(L + L_ST1 + b * 32), r1 = *(const LAS f32x4*)(L + L_ST1 + b * 32 + 16);
    float m, rden;
    ln_consts((r0.x + r0.z) + (r1.x + r1.z), (r0.y + r0.w) + (r1.y + r1.w), m, rden);
    lnx = rbf(rbf(lo2f(xh) - m) * rden);
    PIN(lnx);
  }
  const int tl = (8 * (lane >> 4) + ((lane >> 2) & 3)) * 256 + (((2 * w + ((lane & 3) >> 1)) ^ (((lane >> 2) & 3) << 2)) << 4) + (lane & 1) * 8;
  const int pubo = w * 128 + (lane & 15) * 64 + (lane >> 4) * 16;
  STAMP(1);

  if (!isA) {
    f32x4 acc = {0.f, 0.f, 0.f, 0.f};
#pragma unroll
    for (int s = 0; s < 4; ++s) { wait_landed(L, s); acc = mfma(tfrag(L, s, tl), sfrag(L, lane, kq * 128 + 32 * s), acc); }
    release(L, w, lane, 4);
    if ((lane & 15) < 2) *(LAS f32x4*)(L + L_STG + pubo) = acc;
    cbar(L, lane, bar);
    if (w == 0) __builtin_amdgcn_raw_buffer_store_b128(*(const LAS v4u*)(L + L_STG + lane * 16), rs, mybox + BX_MC + lane * 16, 0, AUX_SC1);
  }
  STAMP(2);
  v4u qw[4];
#pragma unroll
  for (int s = 0; s < 4; ++s) { wait_landed(L, offQ + s); qw[s] = tfrag(L, (offQ + s) & 15, tl); }
  release(L, w, lane, offMR);
  STAMP(3);
#pragma unroll 1
  for (int u = 0; u < (isA ? 1 : 3); ++u) {
    const int n = offMR + 2 * u, k0 = ((mu0 + u) & 15) * 64;
    wait_landed(L, n); wait_landed(L, n + 1);
    f32x4 acc = mfma(tfrag(L, n & 15, tl), sfrag(L, lane, k0), f32x4{0.f, 0.f, 0.f, 0.f});
    acc = mfma(tfrag(L, (n + 1) & 15, tl), sfrag(L, lane, k0 + 32), acc);
    release(L, w, lane, (isA || n + 2 < offK) ? n + 2 : offK + w);
    if ((lane & 15) < 2) *(LAS f32x4*)(L + L_STG + 2048 + u * 1024 + pubo) = acc;
  }
  v4u wf[8];
  if (isA) {
#pragma unroll
    for (int q = 0; q < 8; ++q) { wait_landed(L, offWO + q); wf[q] = tfrag(L, (offWO + q) & 15, tl); }
    release(L, w, lane, offK + w);
  }
  {
    lds_wait_ge(L, L_FALIVE, 1);
    v4u g[2];
    const int js = (16 + (kq & 1) * 8 + sg) * 8 + (kq >> 1);
    const int off[2] = {box(js) + BX_MC + w * 128 + piece * 16, box(js + 4) + BX_MC + w * 128 + piece * 16};
    gather<2>(g, off, rs);
    STAMP(26);
    const float sa = rbf(tred(asf(g[0]))), ca = rbf(tred(asf(g[1])));
    const float h = rbf(lnx * rbf(1.f + ca)) + sa;
    if (!(lane & 8)) *(LAS u16*)(L + L_HVEC + (piece >> 2) * 256 + (16 * w + 4 * (piece & 3) + comp) * 2) = (u16)pk(h, h);
    cbar(L, lane, bar);
    STAMP(5);
    if (w >= 1 && w <= (isA ? 1 : 3))
      __builtin_amdgcn_raw_buffer_store_b128(*(const LAS v4u*)(L + L_STG + 1024 + w * 1024 + lane * 16), rs, mybox + BX_MR + (w - 1) * 1024 + lane * 16, 0, AUX_SC1);
    f32x4 acc = {0.f, 0.f, 0.f, 0.f};
#pragma unroll
    for (int s = 0; s < 4; ++s) acc = mfma(qw[s], vfrag(L, L_HVEC, lane, s), acc);
    if ((lane & 15) < 2) *(LAS f32x4*)(L + L_STG + 1024 + (lane & 15) * 512 + (16 * w + 4 * (lane >> 4)) * 4) = acc;
    cbar(L, lane, bar);
    if (w == 0) __builtin_amdgcn_raw_buffer_store_b128(*(const LAS v4u*)(L + L_STG + 1024 + lane * 16), rs, mybox + BX_Q + lane * 16, 0, AUX_SC1);
  }
  STAMP(6);
  v4u qf[4], tw[8], tx[8];
  const int kl = (8 * ((lane & 15) >> 2) + (lane & 3)) * 256 + ((lane & 3) << 6) + (lane >> 4) * 16;
  {
    v4u g[4]; int off[4];
    const int head = hkv * 8 + w;
#pragma unroll
    for (int q = 0; q < 4; ++q) {
      const int jq = ((head & 3) * 8 + 2 * q + (lane >> 5)) * 8 + (head >> 2);
      off[q] = box(jq) + BX_Q + batt * 512 + (lane & 31) * 16;
    }
    STAMP(27);
    wait_landed(L, offK + w);
#pragma unroll
    for (int q = 0; q < 8; ++q) tw[q] = *(const LAS v4u*)(L + L_RING + ((offK + w) & 15) * 8192 + (q >> 2) * 1024 + (kl ^ ((q & 3) << 6)));
    release(L, w, lane, offK + w + 8);
    wait_landed(L, offK + w + 8);
#pragma unroll
    for (int q = 0; q < 8; ++q) tx[q] = *(const LAS v4u*)(L + L_RING + ((offK + w + 8) & 15) * 8192 + (q >> 2) * 1024 + (kl ^ ((q & 3) << 6)));
    release(L, w, lane, offK + w + 16);
    gather_issue<4>(g, off, rs);
    gather_finish_slow<4, HOPB_PAUSE>(g, off, rs);
    STAMP(28);
    const f32x4 t = (asf(g[0]) + asf(g[1])) + (asf(g[2]) + asf(g[3]));
    const f32x2 a = swap32(t.x, t.y), b = swap32(t.z, t.w);
    const unsigned p = pk(a.x + a.y, b.x + b.y);
    LAS u16* qv = (LAS u16*)(L + L_QVEC + w * 256 + (lane & 31) * 8 + (lane >> 5) * 2);
    qv[0] = (u16)p; qv[2] = (u16)(p >> 16);
    cbar(L, lane, bar);
#pragma unroll
    for (int s = 0; s < 4; ++s) qf[s] = *(const LAS v4u*)(L + L_QVEC + (lane & 7) * 256 + s * 64 + (lane >> 4) * 16);
  }
  STAMP(7);
  float ga = 0.f, gb = 0.f, xres = 0.f, bias = 0.f;
  float sc[4][8];
  {
#pragma unroll
    for (int i = 0; i < 4; ++i) {
      const int n = offK + w + 8 * i;
      v4u kf[2][4];
      if (i < 2) {
#pragma unroll
        for (int q = 0; q < 8; ++q) kf[q >> 2][q & 3] = i == 0 ? tw[q] : tx[q];
      } else {
        wait_landed(L, n);
#pragma unroll
        for (int T = 0; T < 2; ++T)
#pragma unroll
          for (int s = 0; s < 4; ++s) kf[T][s] = *(const LAS v4u*)(L + L_RING + (n & 15) * 8192 + T * 1024 + (kl ^ (s << 6)));
        release(L, w, lane, i < 3 ? n + 8 : offV);
      }
#pragma unroll
      for (int T = 0; T < 2; ++T) {
        f32x4 a = {0.f, 0.f, 0.f, 0.f};
#pragma unroll
        for (int s = 0; s < 4; ++s) a = mfma(kf[T][s], qf[s], a);
        const f32x2 lo = rbf2(rbf2(f32x2{a.x, a.y}) * 0.08838834764831845f), hi = rbf2(rbf2(f32x2{a.z, a.w}) * 0.08838834764831845f);
        sc[i][T * 4] = lo.x; sc[i][T * 4 + 1] = lo.y; sc[i][T * 4 + 2] = hi.x; sc[i][T * 4 + 3] = hi.y;
      }
    }
  }
  STAMP(8);
  float mw = sc[0][0];
  {
#pragma unroll
    for (int i = 0; i < 4; ++i)
#pragma unroll
      for (int r = 0; r < 8; ++r) mw = fmaxf(mw, sc[i][r]);
    mw = max32(max16(mw));
    float lw = 0.f;
#pragma unroll
    for (int i = 0; i < 4; ++i)
#pragma unroll
      for (int r = 0; r < 8; ++r) { sc[i][r] = exp_(sc[i][r] - mw); lw += sc[i][r]; }
    lw = sum32(sum16(lw));
    if (lane < 8) *(LAS f32x2*)(L + L_MLW + w * 64 + lane * 8) = f32x2{mw, lw};
    lds_inc(L, L_MLCNT, lane);
  }
  f32x4 sbv = {0.f, 0.f, 0.f, 0.f}, cb1 = {0.f, 0.f, 0.f, 0.f};
  if (isB) {
    v4u g[2];
    const int off[2] = {box(30 * 8 + (w >> 1) + (w & 1) * 8) + BX_T1 + lane * 16, box(30 * 8 + 4 + (w >> 1) + (w & 1) * 8) + BX_T1 + lane * 16};
    gather<2>(g, off, rs);
    sbv = asf(g[0]);
    const f32x4 cb = asf(g[1]);
    const f32x2 c0 = rbf2(f32x2{cb.x, cb.y} + 1.f), c1 = rbf2(f32x2{cb.z, cb.w} + 1.f);
    cb1 = f32x4{c0.x, c0.y, c1.x, c1.y};
    bias = lo2f(*(const GAS u16*)(b1 + (bi >> 2) * 128 + 16 * w + 4 * (piece & 3) + comp));
    PIN(bias); PIN(sbv); PIN(cb1);
  }
  if (isC) {
    const int col = 64 * ci + 16 * (w & 3) + 4 * (piece & 3) + comp;
    xres = lo2f(*(const GAS u16*)(x + (piece >> 2) * D + col));
    bias = lo2f(*(const GAS u16*)(b2 + col));
    lds_wait_ge(L, L_FGATE, 1);
    ga = *(const LAS float*)(L + L_GATE + (w & 3) * 256 + lane * 4);
    gb = *(const LAS float*)(L + L_GATE + (4 + (w & 3)) * 256 + lane * 4);
    PIN(ga); PIN(gb); PIN(xres); PIN(bias);
  }
#pragma unroll
  for (int q = 0; q < 8; ++q) { wait_landed(L, offV + q); tw[q] = tfrag(L, (offV + q) & 15, tl); release(L, w, lane, offV + q + 1); }
#pragma unroll
  for (int q = 0; q < VPARK - 8; ++q) { wait_landed(L, offV + 8 + q); tx[q] = tfrag(L, (offV + 8 + q) & 15, tl); release(L, w, lane, offV + 9 + q); }
  lds_wait_ge(L, L_FML, 1);
  {
    const f32x2 ml = *(const LAS f32x2*)(L + L_MLF + (lane & 7) * 8);
    const float f = exp_(mw - ml.x) * ml.y;
#pragma unroll
    for (int i = 0; i < 4; ++i) {
      const v4u p = {pk(sc[i][0] * f, sc[i][1] * f), pk(sc[i][2] * f, sc[i][3] * f), pk(sc[i][4] * f, sc[i][5] * f), pk(sc[i][6] * f, sc[i][7] * f)};
      if ((lane & 15) < 8) *(LAS v4u*)(L + L_P + (w + 8 * i) * 512 + ((lane >> 4) * 8 + (lane & 7)) * 16) = p;
    }
    cbar(L, lane, bar);
  }
  STAMP(10);
  f32x4 oacc = {0.f, 0.f, 0.f, 0.f};
  const int pl = L_P + ((lane >> 4) * 8 + (lane & 7)) * 16;
#pragma unroll
  for (int i = 0; i < VPARK; ++i) oacc = mfma(i < 8 ? tw[i & 7] : tx[i & 7], *(const LAS v4u*)(L + pl + i * 512), oacc);
#pragma unroll 1
  for (int i = VPARK; i < 32; i += 2) {
    const int n = offV + i;
    wait_landed(L, n + 1); wait_landed(L, n);
    const v4u a0 = tfrag(L, n & 15, tl), a1 = tfrag(L, (n + 1) & 15, tl);
    const v4u p0 = *(const LAS v4u*)(L + pl + i * 512), p1 = *(const LAS v4u*)(L + pl + (i + 1) * 512);
    release(L, w, lane, n + 2);
    oacc = mfma(a0, p0, oacc);
    oacc = mfma(a1, p1, oacc);
  }
  if ((lane & 15) < 8) __builtin_amdgcn_raw_buffer_store_b128(asu(oacc), rs, mybox + BX_O + w * 512 + ((lane & 15) * 4 + (lane >> 4)) * 16, 0, AUX_SC1);
  STAMP(11);

  if (isA) {
    v4u g[4]; int off[4];
#pragma unroll
    for (int q = 0; q < 4; ++q) off[q] = box((sg + 8 * q) * 8 + (w >> 2) * 4 + a_hkv) + BX_O + a_dt * 512 + (w & 3) * 128 + piece * 16;
    gather_issue<4>(g, off, rs);
    gather_finish_slow<4, X1_PAUSE>(g, off, rs);
    STAMP(22);
    const float t = tred((asf(g[0]) + asf(g[1])) + (asf(g[2]) + asf(g[3])));
    if (!(lane & 8)) *(LAS u16*)(L + L_TVEC + (w >> 2) * 256 + (32 * (w & 3) + 4 * piece + comp) * 2) = (u16)pk(t, t);
    WGBAR();
    STAMP(12);
    f32x4 a0 = {0.f, 0.f, 0.f, 0.f}, a1 = {0.f, 0.f, 0.f, 0.f};
#pragma unroll
    for (int s = 0; s < 4; ++s) { const v4u bv = vfrag(L, L_TVEC, lane, s); a0 = mfma(wf[s], bv, a0); a1 = mfma(wf[4 + s], bv, a1); }
    if ((lane & 15) < 2) {
      __builtin_amdgcn_raw_buffer_store_b128(asu(a0), rs, mybox + BX_T1 + pubo, 0, AUX_SC1);
      __builtin_amdgcn_raw_buffer_store_b128(asu(a1), rs, mybox + BX_T1 + 1024 + pubo, 0, AUX_SC1);
    }
    STAMP(13);
  } else if (isC) {
    if (w < 4) {
      const int ct = 4 * ci + w, cg = ct >> 4, ti = ct & 15;
      v4u g[4]; int off[4], off2[4];
#pragma unroll
      for (int q = 0; q < 4; ++q) {
        const int a = (sg + 8 * q) * 4 + cg;
        off[q] = box((a & 15) * 8 + (a >> 4)) + BX_T1 + ti * 128 + piece * 16;
        const int kf = sg + 8 * q, bb = kf * 4 + cg, gg = bb / 14;
        off2[q] = kf < 28 ? box((16 + bb - gg * 14) * 8 + gg) + BX_T2 + ti * 128 + piece * 16 : WS_BYTES;
        PIN(off2[q]);
      }
      gather_issue<4>(g, off, rs);
      gather_finish_slow<4, T1_PAUSE>(g, off, rs);
      STAMP(22);
      const float t = tred((asf(g[0]) + asf(g[1])) + (asf(g[2]) + asf(g[3])));
      const float x1 = rbf(xres + rbf(ga * rbf(t)));
      { const f32x4 x4 = texp(x1); if (lane < 8) __builtin_amdgcn_raw_buffer_store_b128(asu(x4), rs, mybox + BX_X1 + w * 128 + piece * 16, 0, AUX_SC1); }
      STAMP(12);
      gather_issue<4>(g, off2, rs);
      gather_finish_slow<4, T1_PAUSE>(g, off2, rs);
      STAMP(23);
      const float z = tred((asf(g[0]) + asf(g[1])) + (asf(g[2]) + asf(g[3])));
      const float o = x1 + rbf(gb * rbf(rbf(z) + bias));
      if (!(lane & 8)) *(GAS u16*)(out + (piece >> 2) * D + 64 * ci + 16 * w + 4 * (piece & 3) + comp) = (u16)pk(o, o);
      STAMP(13);
    }
  } else {
#pragma unroll
    for (int q = 0; q < 8; ++q) { wait_landed(L, offW + q); tw[q] = tfrag(L, (offW + q) & 15, tl); }
    const int cc = 2 * w + (sg >> 2);
    const int offx[1] = {box((30 + (cc & 1)) * 8 + (cc >> 1)) + BX_X1 + (sg & 3) * 128 + piece * 16};
    const int kf = bi >> 2, bb = (kf >> 1) * 8 + sg, gg = bb / 14;
    int offh[1] = {box((16 + bb - gg * 14) * 8 + gg) + BX_T1 + ((kf & 1) * 8 + w) * 128 + piece * 16};
    PIN(offh[0]);
    v4u g[1];
    if (B_GATE) { wait_landed(L, offW + 14); wait_landed(L, offW + 15); }
    gather_issue<1>(g, offx, rs);
    gather_finish_slow<1, BX_PAUSE>(g, offx, rs);
    STAMP(12);
    const f32x4 x1 = asf(g[0]);
    float m, rden;
    stat_put(L, L_ST1, w, lane, x1);
    WGBAR();
    stat_get(L, L_ST1, lane, m, rden);
    const f32x2 ha = rbf2(rbf2(ln2(f32x2{x1.x, x1.y}, m, rden) * f32x2{cb1.x, cb1.y}) + f32x2{sbv.x, sbv.y});
    const f32x2 hb = rbf2(rbf2(ln2(f32x2{x1.z, x1.w}, m, rden) * f32x2{cb1.z, cb1.w}) + f32x2{sbv.z, sbv.w});
    stat_put(L, L_ST2, w, lane, f32x4{ha.x, ha.y, hb.x, hb.y});
    WGBAR();
    STAMP(24);
    if (w == (bi & 7)) {
      stat_get(L, L_ST2, lane, m, rden);
      const f32x2 ya = rbf2(ha - m) * rden, yb = rbf2(hb - m) * rden;
      *(LAS v2u*)(L + L_TVEC + (piece >> 2) * 256 + (16 * sg + 4 * (piece & 3)) * 2) = v2u{pk(ya.x, ya.y), pk(yb.x, yb.y)};
    }
    WGBAR();
    {
      f32x4 a0 = {0.f, 0.f, 0.f, 0.f}, a1 = {0.f, 0.f, 0.f, 0.f};
#pragma unroll
      for (int s = 0; s < 4; ++s) { const v4u bv = vfrag(L, L_TVEC, lane, s); a0 = mfma(tw[s], bv, a0); a1 = mfma(tw[4 + s], bv, a1); }
      if ((lane & 15) < 2) {
        __builtin_amdgcn_raw_buffer_store_b128(asu(a0), rs, mybox + BX_T1 + pubo, 0, AUX_SC1);
        __builtin_amdgcn_raw_buffer_store_b128(asu(a1), rs, mybox + BX_T1 + 1024 + pubo, 0, AUX_SC1);
      }
    }
    STAMP(13);
    gather_issue<1>(g, offh, rs);
#pragma unroll
    for (int q = 0; q < 8; ++q) { wait_landed(L, offW + 8 + q); tw[q] = tfrag(L, (offW + 8 + q) & 15, tl); }
    g[0] = gather_pair<X3_PAUSE>(g[0], offh[0], rs);
    STAMP(23);
    {
      const float u = rbf(rbf(tred(asf(g[0]))) + bias);
      const float z = u * (0.7978845608028654f + 0.035677408136300125f * u * u);
      const float hh = u * rcp_(1.f + __builtin_amdgcn_exp2f(-2.8853900817779268f * z));
      if (!(lane & 8)) *(LAS u16*)(L + L_UVEC + (piece >> 2) * 256 + (16 * w + 4 * (piece & 3) + comp) * 2) = (u16)pk(hh, hh);
      WGBAR();
      STAMP(14);
      f32x4 a0 = {0.f, 0.f, 0.f, 0.f}, a1 = {0.f, 0.f, 0.f, 0.f};
#pragma unroll
      for (int s = 0; s < 4; ++s) { const v4u bv = vfrag(L, L_UVEC, lane, s); a0 = mfma(tw[s], bv, a0); a1 = mfma(tw[4 + s], bv, a1); }
      if ((lane & 15) < 2) {
        __builtin_amdgcn_raw_buffer_store_b128(asu(a0), rs, mybox + BX_T2 + pubo, 0, AUX_SC1);
        __builtin_amdgcn_raw_buffer_store_b128(asu(a1), rs, mybox + BX_T2 + 1024 + pubo, 0, AUX_SC1);
      }
    }
  }
  asm volatile(".Lfb_last:");
  STAMP(15);
}

}

extern "C" void kernel_launch(void* const* d_in, const int* in_sizes, int n_in, void* d_out, int out_size, void* d_ws, size_t ws_size,
                              hipStream_t stream) {
  (void)in_sizes; (void)n_in; (void)out_size; (void)ws_size;
  (void)hipFuncSetAttribute((const void*)fused_block, hipFuncAttributeMaxDynamicSharedMemorySize, LDS_BYTES);
  hipLaunchKernelGGL(fused_block, dim3(256), dim3(NTHR), LDS_BYTES, stream, (const u16*)d_in[0], (const u16*)d_in[1], (const u16*)d_in[2],
                     (const u16*)d_in[3], (const u16*)d_in[4], (const u16*)d_in[6], (const u16*)d_in[7], (const u16*)d_in[8],
                     (const u16*)d_in[9], (const u16*)d_in[10], (const u16*)d_in[11], (u16*)d_out, (unsigned char*)d_ws);
}
```

```cpp
#define PROBE_X 41
#include <hip/hip_runtime.h>
#include <hip/hip_bf16.h>
#include <stdint.h>

#define LAS __attribute__((address_space(3)))
#define GAS __attribute__((address_space(1)))
typedef unsigned short u16;
typedef unsigned v4u __attribute__((ext_vector_type(4)));
typedef unsigned v2u __attribute__((ext_vector_type(2)));
typedef float f32x4 __attribute__((ext_vector_type(4)));
typedef float f32x2 __attribute__((ext_vector_type(2)));
typedef short s16x4 __attribute__((ext_vector_type(4)));
typedef short bf16x8 __attribute__((ext_vector_type(8)));
typedef __bf16 bf16x2 __attribute__((ext_vector_type(2)));

namespace {

constexpr int D = 1024, SK = 32768;
constexpr int NTHR = 704;
#define LOADW 12
#define ALIVE_DELAY 24
#define DELAY_MLP 0
#define HOPB_PAUSE 8
#define X3_PAUSE 4
#define VPARK 12
#define ML_PAUSE 16
#define ABL_PERIOD 8
#define ABL_LANES 8
#define ABL_WAVES 8
#define ABL_WHO 1
#define ABL_AUX 16
#define ALIVE_REP 3
#define ML_DELAY 2
#define X1_PAUSE 0
#define T1_PAUSE 0
#define BX_PAUSE 0
#define B_GATE 1
#define TAPER 0
#define LOADW_END 6
#define SCHED_TAU 0
#define SCHED_T0 5500
#define LOADW_LO 8
#define LOADW_PRO LOADW
#define ABL_CLEAR 1
#define LOADW2 16

constexpr int WS_EPOCH = 0, WS_ALIVE = 4096, WS_BOX = 65536, BOX = 16384;
constexpr int BX_MC = 0, BX_Q = 1024, BX_ML = 2048, BX_MR = 3072, BX_O = 6144, BX_T1 = 10240, BX_T2 = 12288, BX_X1 = BX_T1 + 1024;
constexpr int WS_BYTES = WS_BOX + 256 * BOX;
constexpr int WS_DBG = 8 << 20;
constexpr unsigned SENT = 0xFFFFFFFFu;
constexpr unsigned SPIN_CAP = 1u << 14, LDS_CAP = 1u << 18;
constexpr int AUX_SC1 = 16;

constexpr int L_RING = 0, L_P = 131072, L_CTL = L_P + 16384;
constexpr int L_STG = L_P + 8192;
constexpr int L_X = L_P, L_C = L_P + 4096;
constexpr int L_LANDED = L_CTL, L_PROG = L_CTL + 32, L_BAR = L_CTL + 64, L_MLCNT = L_CTL + 68, L_FALIVE = L_CTL + 72, L_FML = L_CTL + 76, L_FAUX = L_CTL + 80, L_FGATE = L_CTL + 88;
constexpr int L_SVEC = L_CTL + 256;
constexpr int L_HVEC = L_SVEC + 4096;
constexpr int L_QVEC = L_HVEC + 512;
constexpr int L_ST1 = L_QVEC + 2048;
constexpr int L_ST2 = L_ST1 + 128;
constexpr int L_MLW = L_ST2 + 128;
constexpr int L_MLF = L_MLW + 512;
constexpr int L_TVEC = L_MLF + 64;
constexpr int L_UVEC = L_TVEC + 512;
constexpr int L_UVEC2 = L_TVEC;
constexpr int L_GATE = L_UVEC + 512;
constexpr int L_STAMP = L_GATE + 2048;
constexpr int LDS_BYTES = L_STAMP + 320;
static_assert(LDS_BYTES <= 160 * 1024, "LDS budget");

typedef LAS unsigned char* lptr;

__device__ __forceinline__ float lo2f(unsigned w) { return __uint_as_float(w << 16); }
__device__ __forceinline__ float hi2f(unsigned w) { return __uint_as_float(w & 0xffff0000u); }
__device__ __forceinline__ unsigned pk(float a, float b) {
  bf16x2 v = {(__bf16)a, (__bf16)b};
  return __builtin_bit_cast(unsigned, v);
}
__device__ __forceinline__ float rbf(float a) { return hi2f(pk(a, a)); }
__device__ __forceinline__ f32x2 rbf2(f32x2 a) {
  unsigned p = pk(a.x, a.y);
  asm("" : "+v"(p));
  return f32x2{lo2f(p), hi2f(p)};
}
__device__ __forceinline__ f32x4 mfma(v4u a, v4u b, f32x4 c) {
  return __builtin_amdgcn_mfma_f32_16x16x32_bf16(__builtin_bit_cast(bf16x8, a), __builtin_bit_cast(bf16x8, b), c, 0, 0, 0);
}
template <int CTRL> __device__ __forceinline__ float dpp(float x) {
  return __builtin_bit_cast(float, __builtin_amdgcn_mov_dpp(__builtin_bit_cast(int, x), CTRL, 0xf, 0xf, true));
}
constexpr int XOR1 = 0xB1, XOR2 = 0x4E, ROR4 = 0x124, ROR8 = 0x128;
__device__ __forceinline__ f32x2 swap32(float a, float b) {
  auto s = __builtin_amdgcn_permlane32_swap(__float_as_uint(a), __float_as_uint(b), false, false);
  return f32x2{__uint_as_float(s[0]), __uint_as_float(s[1])};
}
__device__ __forceinline__ f32x2 swap16(float a, float b) {
  auto s = __builtin_amdgcn_permlane16_swap(__float_as_uint(a), __float_as_uint(b), false, false);
  return f32x2{__uint_as_float(s[0]), __uint_as_float(s[1])};
}
__device__ __forceinline__ float sum16(float x) { const f32x2 s = swap16(x, x); return s.x + s.y; }
__device__ __forceinline__ float sum32(float x) { const f32x2 s = swap32(x, x); return s.x + s.y; }
__device__ __forceinline__ float max16(float x) { const f32x2 s = swap16(x, x); return fmaxf(s.x, s.y); }
__device__ __forceinline__ float max32(float x) { const f32x2 s = swap32(x, x); return fmaxf(s.x, s.y); }
__device__ __forceinline__ float tred(f32x4 v) {
  const f32x2 a = swap32(v.x, v.y), b = swap32(v.z, v.w);
  const f32x2 c = swap16(a.x + a.y, b.x + b.y);
  const float t = c.x + c.y;
  return t + dpp<ROR8>(t);
}
__device__ __forceinline__ f32x4 texp(float t) {
  const f32x2 a = swap16(t, t);
  const f32x2 b = swap32(a.x, a.x), c = swap32(a.y, a.y);
  return f32x4{b.x, b.y, c.x, c.y};
}
__device__ __forceinline__ int tcomp(int lane) { return ((lane >> 4) & 1) * 2 + (lane >> 5); }
__device__ __forceinline__ float sum_all(float x) {
  x += dpp<XOR1>(x); x += dpp<XOR2>(x); x += dpp<ROR4>(x); x += dpp<ROR8>(x);
  return sum32(sum16(x));
}
__device__ __forceinline__ f32x4 asf(v4u v) { return __builtin_bit_cast(f32x4, v); }
__device__ __forceinline__ v4u asu(f32x4 v) { return __builtin_bit_cast(v4u, v); }
__device__ __forceinline__ unsigned umax4(v4u v) { return max(max(max(v.x, v.y), v.z), v.w); }
__device__ __forceinline__ float exp_(float x) { return __builtin_amdgcn_exp2f(x * 1.4426950408889634f); }
__device__ __forceinline__ float rcp_(float x) { return __builtin_amdgcn_rcpf(x); }

#define SLEEPN 0
#define OIDX ((lane >> 4) * 8 + (lane & 15))
#define OKK (16 * piece + 4 * (w & 3) + comp)
#define LGKM0() asm volatile("s_waitcnt lgkmcnt(0)" ::: "memory")
#define VM0() asm volatile("s_waitcnt vmcnt(0)" ::: "memory")
#define CFENCE() asm volatile("" ::: "memory")
#define PIN(x) asm volatile("" : "+v"(x))
#define WGBAR() do { LGKM0(); __builtin_amdgcn_s_barrier(); CFENCE(); } while (0)

#define STAMPV(i, v) do { } while (0)
#define STAMP(i) do { } while (0)

__device__ __forceinline__ void dma16(unsigned voff, const unsigned char* sbase, unsigned ldsaddr) {
  unsigned keep;
  asm volatile("s_mov_b32 %0, m0\n\ts_mov_b32 m0, %3\n\ts_nop 4\n\tglobal_load_lds_dwordx4 %1, %2 nt\n\ts_mov_b32 m0, %0"
               : "=&s"(keep) : "v"(voff), "s"(sbase), "s"(ldsaddr) : "memory");
}
__device__ __forceinline__ unsigned lds_u32(lptr L, int off) {
  return (unsigned)__builtin_amdgcn_readfirstlane(*(volatile LAS unsigned*)(L + off));
}
__device__ __forceinline__ void lds_wait_ge(lptr L, int off, unsigned need) {
#pragma nounroll
  for (unsigned sp = 0; lds_u32(L, off) < need && sp < LDS_CAP; ++sp) { if (SLEEPN) __builtin_amdgcn_s_sleep(SLEEPN); }
  CFENCE();
}
__device__ __forceinline__ void wait_landed(lptr L, int n) { lds_wait_ge(L, L_LANDED + 4 * (n & 1), (unsigned)(n >> 1) + 1u); }
__device__ __forceinline__ void release(lptr L, int w, int lane, int n) {
  LGKM0();
  if (lane == 0) *(volatile LAS unsigned*)(L + L_PROG + 4 * w) = (unsigned)n;
}
__device__ __forceinline__ void lds_inc(lptr L, int off, int lane) {
  LGKM0();
  if (lane == 0) __hip_atomic_fetch_add((LAS unsigned*)(L + off), 1u, __ATOMIC_RELAXED, __HIP_MEMORY_SCOPE_WORKGROUP);
}
__device__ __forceinline__ void cbar(lptr L, int lane, unsigned& target) {
  target += 8;
  lds_inc(L, L_BAR, lane);
  lds_wait_ge(L, L_BAR, target);
}
__device__ __forceinline__ v4u tfrag(lptr L, int slot, int tl) {
  const s16x4 a = __builtin_amdgcn_ds_read_tr16_b64_v4i16((LAS s16x4*)(L + L_RING + slot * 8192 + tl));
  const s16x4 b = __builtin_amdgcn_ds_read_tr16_b64_v4i16((LAS s16x4*)(L + L_RING + slot * 8192 + tl + 1024));
  const v2u x = __builtin_bit_cast(v2u, a), y = __builtin_bit_cast(v2u, b);
  return v4u{x.x, x.y, y.x, y.y};
}
__device__ __forceinline__ v4u sfrag(lptr L, int lane, int k0) {
  return *(const LAS v4u*)(L + L_SVEC + (lane & 1) * 2048 + k0 * 2 + (lane >> 4) * 16);
}
__device__ __forceinline__ v4u vfrag(lptr L, int off, int lane, int s) {
  return *(const LAS v4u*)(L + off + (lane & 1) * 256 + s * 64 + (lane >> 4) * 16);
}
template <int N>
__device__ __forceinline__ void gather_issue(v4u (&v)[N], const int (&off)[N], __amdgpu_buffer_rsrc_t rs) {
#pragma unroll
  for (int q = 0; q < N; ++q) v[q] = __builtin_amdgcn_raw_buffer_load_b128(rs, off[q], 0, AUX_SC1);
}
template <int N>
__device__ __forceinline__ bool gather_ok(const v4u (&v)[N]) {
  unsigned mx = umax4(v[0]);
#pragma unroll
  for (int q = 1; q < N; ++q) mx = max(mx, umax4(v[q]));
  return __builtin_amdgcn_ballot_w64(mx == SENT) == 0;
}
template <int N, int PAUSE>
__device__ __forceinline__ void gather_finish_slow(v4u (&v)[N], const int (&off)[N], __amdgpu_buffer_rsrc_t rs) {
#pragma nounroll
  for (unsigned sp = 0; sp < SPIN_CAP; ++sp) {
    unsigned mx = umax4(v[0]);
#pragma unroll
    for (int q = 1; q < N; ++q) mx = max(mx, umax4(v[q]));
    if (__builtin_amdgcn_ballot_w64(mx == SENT) == 0) break;
    if (PAUSE) __builtin_amdgcn_s_sleep(PAUSE);
    CFENCE();
    if (mx == SENT) gather_issue<N>(v, off, rs);
  }
}
template <int N>
__device__ __forceinline__ void gather_finish(v4u (&v)[N], const int (&off)[N], __amdgpu_buffer_rsrc_t rs) { gather_finish_slow<N, 0>(v, off, rs); }
template <int PAUSE>
__device__ __forceinline__ v4u gather_pair(v4u first, int off, __amdgpu_buffer_rsrc_t rs) {
  v4u a[1] = {first}, b[1];
  const int o[1] = {off};
  __builtin_amdgcn_s_sleep(PAUSE);
  CFENCE();
  gather_issue<1>(b, o, rs);
  v4u r;
  for (unsigned sp = 0;; ++sp) {
    if (gather_ok<1>(a) || sp >= SPIN_CAP) { r = a[0]; break; }
    CFENCE(); gather_issue<1>(a, o, rs);
    if (gather_ok<1>(b)) { r = b[0]; break; }
    CFENCE(); gather_issue<1>(b, o, rs);
  }
  return r;
}
template <int N>
__device__ __forceinline__ void gather(v4u (&v)[N], const int (&off)[N], __amdgpu_buffer_rsrc_t rs) {
  gather_issue<N>(v, off, rs);
  gather_finish<N>(v, off, rs);
}
__device__ __forceinline__ void publish2(f32x4 a0, f32x4 a1, __amdgpu_buffer_rsrc_t rs, int off, int lane) {
  if ((lane & 15) < 2) {
    __builtin_amdgcn_raw_buffer_store_b128(asu(a0), rs, off + (lane & 1) * 64 + (lane >> 4) * 16, 0, AUX_SC1);
    __builtin_amdgcn_raw_buffer_store_b128(asu(a1), rs, off + 1024 + (lane & 1) * 64 + (lane >> 4) * 16, 0, AUX_SC1);
  }
  return;
  const bool hi = lane & 2;
  const f32x4 v = {hi ? a1.x : a0.x, hi ? a1.y : a0.y, hi ? a1.z : a0.z, hi ? a1.w : a0.w};
  if ((lane & 15) < 4) __builtin_amdgcn_raw_buffer_store_b128(asu(v), rs, off + (lane & 2) * 512 + (lane & 1) * 64 + (lane >> 4) * 16, 0, AUX_SC1);
}
__device__ __forceinline__ int box(int j) { return WS_BOX + j * BOX; }
__device__ __forceinline__ void ln_consts(float s, float q, float& m, float& rden) {
  const float mean = s * (1.f / D);
  const float var = fmaxf(q * (1.f / D) - mean * mean, 0.f);
  m = rbf(mean);
  rden = rcp_(rbf(__builtin_amdgcn_sqrtf(rbf(rbf(var) + 1e-5f))));
}
__device__ __forceinline__ f32x2 ln2(f32x2 x, float m, float rden) { return rbf2(rbf2(x - m) * rden); }
__device__ __forceinline__ void stat_put(lptr L, int off, int w, int lane, f32x4 v) {
  const float s = (v.x + v.y) + (v.z + v.w), q = (v.x * v.x + v.y * v.y) + (v.z * v.z + v.w * v.w);
  const f32x2 a = swap32(s, q);
  float r = sum16(a.x + a.y);
  r += dpp<ROR8>(r); r += dpp<XOR2>(r); r += dpp<XOR1>(r);
  if ((lane & 0x1B) == 0) *(LAS float*)(L + off + w * 16 + (lane & 4) * 2 + (lane >> 5) * 4) = r;
}
__device__ __forceinline__ void stat_get(lptr L, int off, int lane, float& m, float& rden) {
  const f32x2 p = *(const LAS f32x2*)(L + off + (lane >> 3) * 16 + (lane & 4) * 2);
  const f32x2 a = swap32(p.x, p.y);
  float r = sum16(a.x + a.y);
  r += dpp<ROR8>(r);
  const f32x2 t = swap32(r, r);
  ln_consts(t.x, t.y, m, rden);
}

__device__ __forceinline__ int read_minprog(lptr L, int lane) {
  unsigned m = *(volatile LAS unsigned*)(L + L_PROG + 4 * (lane & 7));
  m = min(m, (unsigned)__builtin_amdgcn_mov_dpp((int)m, XOR1, 0xf, 0xf, true));
  m = min(m, (unsigned)__builtin_amdgcn_mov_dpp((int)m, XOR2, 0xf, 0xf, true));
  m = min(m, (unsigned)__builtin_amdgcn_mov_dpp((int)m, ROR4, 0xf, 0xf, true));
  return __builtin_amdgcn_readfirstlane((int)m);
}
template <int W>
__device__ __forceinline__ void load_block(lptr L, int ld, int lane, int n, const unsigned char* bp, int ldb, int hi, int& minprog) {
  if (minprog < n - 15) {
    minprog = read_minprog(L, lane);
    if (minprog < n - 15) {
      VM0();
      if (lane == 0) *(volatile LAS unsigned*)(L + L_LANDED + 4 * ld) = (unsigned)(n >> 1);
      for (unsigned sp = 0; sp < LDS_CAP; ++sp) {
        minprog = read_minprog(L, lane);
        if (minprog >= n - 15) break;
      }
    }
  }
  const unsigned voff = (unsigned)((lane >> 4) * ldb + (((lane & 15) ^ ((lane >> 4) << 2)) << 4));
  const unsigned dst = (unsigned)(size_t)L + L_RING + (unsigned)(n & 15) * 8192u;
#pragma unroll
  for (int p = 0; p < 8; ++p) {
    dma16(voff, bp + (size_t)((p & 3) * 4) * ldb + (size_t)(p >> 2) * hi, dst + p * 1024);
    asm volatile("s_waitcnt vmcnt(%0)" ::"i"(W - 1) : "memory");
    if (((p + 2 - W) & 7) == 0) {
      const int done = (n >> 1) + (p + 2 - W) / 8;
      if (lane == 0 && done > 0) *(volatile LAS unsigned*)(L + L_LANDED + 4 * ld) = (unsigned)done;
    }
  }
}

__global__ void __launch_bounds__(NTHR) fused_block(const u16* __restrict__ x, const u16* __restrict__ kc, const u16* __restrict__ vc,
                                                     const u16* __restrict__ wq, const u16* __restrict__ wo, const u16* __restrict__ w1,
                                                     const u16* __restrict__ b1, const u16* __restrict__ w2, const u16* __restrict__ b2,
                                                     const u16* __restrict__ cvec, const u16* __restrict__ wmod, u16* __restrict__ out,
                                                     unsigned char* ws) {
  extern __shared__ __attribute__((aligned(16))) unsigned char smem[];
  lptr L = (lptr)smem;
  const int tid = threadIdx.x, lane = tid & 63, w = __builtin_amdgcn_readfirstlane(tid >> 6);
  const int j = blockIdx.x, grp = j & 7, split = j >> 3, batt = grp >> 2, hkv = grp & 3;
  const bool isA = split < 16, isC = split >= 30, isB = !isA && !isC;
  const int kq = split & 7, g32 = grp * 4 + (split >> 3), g16 = grp * 2 + ((split >> 3) & 1);
  const int offQ = isA ? 0 : 4, offMR = offQ + 4, offWO = 6;
  constexpr int offK = 14, offV = offK + 32, offW = offV + 32;
  const int mu0 = isA ? grp * 16 + split : 128 + 3 * (grp * 16 + split - 16);
  const int ai = grp * 16 + split, a_hkv = ai >> 5, a_dt = (ai >> 2) & 7, a_cg = ai & 3;
  const int bi = grp * 14 + (split - 16), ci = grp * 2 + (split - 30);
  unsigned long long pc0; int pclen;
  asm volatile("s_getpc_b64 %0\n.Lfb_begin:\n\ts_mov_b32 %1, .Lfb_last-.Lfb_begin" : "=s"(pc0), "=s"(pclen));

  if (tid < 64) *(volatile LAS unsigned*)(L + L_CTL + 4 * tid) = 0u;
  WGBAR();
  if (__builtin_expect(w == 8 || w == 9, 1)) {
    const int ld = w - 8;
    __builtin_amdgcn_s_setprio(3);
    int minprog = 0;
    STAMP(16);
    {
      const __amdgpu_buffer_rsrc_t rsl = __builtin_amdgcn_make_buffer_rsrc((void*)ws, 0, WS_BYTES, 0x00020000);
      const unsigned char* aux = (const unsigned char*)(ld ? cvec : x);
#pragma unroll
      for (int p = 0; p < 4; ++p) dma16((unsigned)lane * 16u, aux + p * 1024, (unsigned)(size_t)L + L_X + ld * 4096 + p * 1024);
      const v4u s4 = {SENT, SENT, SENT, SENT};
      const unsigned used = isA ? 0x0FCEu : (isB ? 0x3FFFu : 0x0FFFu);
#pragma unroll 1
      for (int u = ld; u < 14; u += 2)
        if (((used >> u) & 1) && ABL_CLEAR) __builtin_amdgcn_raw_buffer_store_b128(s4, rsl, box(j) + u * 1024 + lane * 16, 0, AUX_SC1);
    }
    const size_t kvoff = ((size_t)grp * SK + split * 1024) * 256;
    const unsigned char* wm = (const unsigned char*)wmod + (size_t)kq * 128 * 12288;
#pragma unroll 1
    for (int n = ld; n < offW; n += 2) {
      const unsigned char* bp; int ldb, hi;
      if (n < offK) {
        if (isA && n >= offWO) {
          const int q = n - offWO;
          bp = (const unsigned char*)wo + ((size_t)(a_hkv * 8 + 2 * (q & 3)) * 128 + 16 * a_dt) * 2048 + a_cg * 512 + (q >> 2) * 256; ldb = 2048; hi = 128 * 2048;
        } else {
          if (n < offQ) { bp = wm + g16 * 256; ldb = 12288; }
          else if (n < offMR) { bp = (const unsigned char*)wq + (size_t)kq * 128 * 8192 + g32 * 256; ldb = 8192; }
          else { const int u = mu0 + ((n - offMR) >> 1); bp = (const unsigned char*)wmod + (size_t)(u & 15) * 64 * 12288 + 4096 + (u >> 4) * 256; ldb = 12288; }
          bp += (size_t)((n < offMR ? n & 3 : (n - offMR) & 1) * 32) * ldb; hi = 16 * ldb;
        }
      } else if (n < offV) { bp = (const unsigned char*)kc + kvoff + (size_t)(n - offK) * 8192; ldb = 256; hi = 4096; }
      else { bp = (const unsigned char*)vc + kvoff + (size_t)(n - offV) * 8192; ldb = 256; hi = 4096; }
      if (n < offK) load_block<LOADW_PRO>(L, ld, lane, n, bp, ldb, hi, minprog);
      else load_block<LOADW>(L, ld, lane, n, bp, ldb, hi, minprog);
      if (n < 2) {
        asm volatile("s_waitcnt vmcnt(8)" ::: "memory");
        if (lane == 0) *(volatile LAS unsigned*)(L + L_FAUX + 4 * ld) = 1u;
      }
    }
    STAMP(19);
    int nend = offW;
    if (isB) {
      nend = offW + 16;
      if (DELAY_MLP) __builtin_amdgcn_s_sleep(DELAY_MLP);
#pragma unroll 1
      for (int n = offW + ld; n < nend; n += 2) {
        const int q = n - offW, s = q & 3, ch = (q >> 2) & 1;
        const unsigned char* bp; int ldb;
        if (q < 8) { bp = (const unsigned char*)w1 + (size_t)(bi & 7) * 128 * 7168 + (bi >> 3) * 512; ldb = 7168; }
        else { bp = (const unsigned char*)w2 + (size_t)(bi >> 2) * 128 * 2048 + (bi & 3) * 512; ldb = 2048; }
        load_block<LOADW2>(L, ld, lane, n, bp + (size_t)(s * 32) * ldb + ch * 256, ldb, 16 * ldb, minprog);
      }
    }
    VM0();
    if (lane == 0) *(volatile LAS unsigned*)(L + L_LANDED + 4 * ld) = (unsigned)(nend >> 1);
    STAMP(20);
    return;
  }

  const __amdgpu_buffer_rsrc_t rs = __builtin_amdgcn_make_buffer_rsrc((void*)ws, 0, WS_BYTES, 0x00020000);
  const int mybox = box(j);
  const int piece = lane & 7, sg = lane >> 3, comp = tcomp(lane);

  if (w == 10) {
    const v2u ep = __builtin_amdgcn_raw_buffer_load_b64(rs, WS_EPOCH, 0, AUX_SC1);
    {
      const int o = split * 1024 + lane * 16;
      const unsigned long long a = pc0 + (unsigned long long)o;
      v4u t = {0u, 0u, 0u, 0u};
      if (o + 16 <= pclen) asm volatile("global_load_dwordx4 %0, %1, off" : "+v"(t) : "v"(a) : "memory");
      asm volatile("s_waitcnt vmcnt(0)" : "+v"(t) :: "memory");
    }
    lds_wait_ge(L, L_FAUX, 1); lds_wait_ge(L, L_FAUX + 4, 1);
    const unsigned long long epoch = ((unsigned long long)(unsigned)__builtin_amdgcn_readfirstlane((int)ep.y) << 32) | (unsigned)__builtin_amdgcn_readfirstlane((int)ep.x);
    const unsigned long long tag = (epoch ^ 0x9E3779B97F4A7C15ull) * 0xD6E8FEB86659FD93ull + 1ull;
    if (lane == 0) __hip_atomic_store((GAS unsigned long long*)(ws + WS_ALIVE + j * 16), tag, __ATOMIC_RELAXED, __HIP_MEMORY_SCOPE_AGENT);
    STAMP(21);
    for (int i = 0; i < ALIVE_REP; ++i) __builtin_amdgcn_s_sleep(ALIVE_DELAY);
    for (unsigned sp = 0;; ++sp) {
      bool ok = true;
#pragma unroll
      for (int q = 0; q < 4; ++q) {
        const v2u t = __builtin_amdgcn_raw_buffer_load_b64(rs, WS_ALIVE + (q * 64 + lane) * 16, 0, AUX_SC1);
        ok = ok && t.x == (unsigned)tag && t.y == (unsigned)(tag >> 32);
      }
      if (__all(ok) || sp > SPIN_CAP) break;
      __builtin_amdgcn_s_sleep(4);
      CFENCE();
    }
    if (lane == 0) *(volatile LAS unsigned*)(L + L_FALIVE) = 1u;
    STAMP(4);
    if (j == 0 && lane == 0) __hip_atomic_store((GAS unsigned long long*)(ws + WS_EPOCH), epoch + 1ull, __ATOMIC_RELAXED, __HIP_MEMORY_SCOPE_AGENT);
    if (isC) {
      lds_wait_ge(L, L_LANDED, (unsigned)(offK + 8) >> 1);
      auto mr = [&](int cg, int kh, int line) {
        const int u = cg * 16 + kh;
        const int na = (u - 128) / 3, jj = u < 128 ? (u & 15) * 8 + (u >> 4) : (16 + (na & 15)) * 8 + (na >> 4);
        return box(jj) + BX_MR + (u < 128 ? 0 : (u - 128 - 3 * na) * 1024) + line * 128 + piece * 16;
      };
#pragma unroll 1
      for (int h4 = 0; h4 < 4; ++h4) {
        const int cg = h4 < 2 ? 8 + ci : (h4 == 2 ? 0 : 24) + (ci >> 1), l0 = h4 < 2 ? 4 * h4 : (ci & 1) * 4;
        const int o0 = mr(cg, sg, l0), o1 = mr(cg, sg + 8, l0);
        v4u g[8];
        const int off[8] = {o0, o1, o0 + 128, o1 + 128, o0 + 256, o1 + 256, o0 + 384, o1 + 384};
        gather_issue<8>(g, off, rs);
        gather_finish_slow<8, 8>(g, off, rs);
#pragma unroll
        for (int q = 0; q < 4; ++q) {
          const float t = rbf(tred(asf(g[2 * q]) + asf(g[2 * q + 1])));
          if (h4 < 2) *(LAS float*)(L + L_UVEC2 + (l0 + q) * 128 + piece * 16 + comp * 4) = t;
          else *(LAS float*)(L + L_GATE + ((h4 - 2) * 4 + q) * 256 + lane * 4) = t;
        }
      }
      LGKM0();
      __builtin_amdgcn_raw_buffer_store_b128(*(const LAS v4u*)(L + L_UVEC2 + lane * 16), rs, mybox + BX_T1 + lane * 16, 0, AUX_SC1);
      if (lane == 0) *(volatile LAS unsigned*)(L + L_FGATE) = 1u;
    }
    lds_wait_ge(L, L_MLCNT, 8);
    const int h = lane & 7;
    const f32x2 r = *(const LAS f32x2*)(L + L_MLW + sg * 64 + h * 8);
    float M = r.x;
    M = fmaxf(M, dpp<ROR8>(M)); M = max32(max16(M));
    float Ls = r.y * exp_(r.x - M);
    Ls += dpp<ROR8>(Ls); Ls = sum32(sum16(Ls));
    const float M1 = dpp<XOR1>(M), L1 = dpp<XOR1>(Ls);
    if (lane < 8 && !(lane & 1)) __builtin_amdgcn_raw_buffer_store_b128(asu(f32x4{M, Ls, M1, L1}), rs, mybox + BX_ML + lane * 8, 0, AUX_SC1);
    STAMP(29);
    for (int i = 0; i < ML_DELAY; ++i) __builtin_amdgcn_s_sleep(32);
    v4u g[2];
    const int off[2] = {box((lane >> 2) * 8 + grp) + BX_ML + (lane & 3) * 16, box(((lane >> 2) + 16) * 8 + grp) + BX_ML + (lane & 3) * 16};
    gather_issue<2>(g, off, rs);
    gather_finish_slow<2, ML_PAUSE>(g, off, rs);
    STAMP(30);
    const f32x4 u = asf(g[0]), v = asf(g[1]);
    float M0 = fmaxf(u.x, v.x), Mb = fmaxf(u.z, v.z);
    M0 = fmaxf(M0, dpp<ROR4>(M0)); M0 = fmaxf(M0, dpp<ROR8>(M0)); M0 = max32(max16(M0));
    Mb = fmaxf(Mb, dpp<ROR4>(Mb)); Mb = fmaxf(Mb, dpp<ROR8>(Mb)); Mb = max32(max16(Mb));
    float L0 = u.y * exp_(u.x - M0) + v.y * exp_(v.x - M0), Lb = u.w * exp_(u.z - Mb) + v.w * exp_(v.z - Mb);
    L0 += dpp<ROR4>(L0); L0 += dpp<ROR8>(L0); L0 = sum32(sum16(L0));
    Lb += dpp<ROR4>(Lb); Lb += dpp<ROR8>(Lb); Lb = sum32(sum16(Lb));
    if (lane < 4) *(LAS f32x4*)(L + L_MLF + lane * 16) = f32x4{M0, 1.f / L0, Mb, 1.f / Lb};
    LGKM0();
    if (lane == 0) *(volatile LAS unsigned*)(L + L_FML) = 1u;
    STAMP(9);
    return;
  }

  unsigned bar = 0;
  const unsigned long long dg0 = __builtin_amdgcn_s_memrealtime();
  STAMP(0);
  lds_wait_ge(L, L_FAUX, 1); lds_wait_ge(L, L_FAUX + 4, 1);
  const unsigned xh = *(const LAS u16*)(L + L_X + ((piece >> 2) * D + kq * 128 + 16 * w + 4 * (piece & 3) + comp) * 2);
  {
    const v2u cr = *(const LAS v2u*)(L + L_C + tid * 8);
    float s[4] = {lo2f(cr.x), hi2f(cr.x), lo2f(cr.y), hi2f(cr.y)};
#pragma unroll
    for (int i = 0; i < 4; ++i) s[i] = s[i] * rcp_(1.f + exp_(-s[i]));
    *(LAS v2u*)(L + L_SVEC + tid * 8) = v2u{pk(s[0], s[1]), pk(s[2], s[3])};
  }
  {
    const v2u xr = *(const LAS v2u*)(L + L_X + tid * 8);
    const float a0 = lo2f(xr.x), a1 = hi2f(xr.x), a2 = lo2f(xr.y), a3 = hi2f(xr.y);
    const f32x2 sq = swap32((a0 + a1) + (a2 + a3), (a0 * a0 + a1 * a1) + (a2 * a2 + a3 * a3));
    float r = sum16(sq.x + sq.y);
    r += dpp<ROR8>(r); r += dpp<ROR4>(r); r += dpp<XOR2>(r); r += dpp<XOR1>(r);
    if ((lane & 31) == 0) *(LAS float*)(L + L_ST1 + w * 8 + (lane >> 5) * 4) = r;
  }
  cbar(L, lane, bar);
  float lnx;
  {
    const int b = piece >> 2;
    const f32x4 r0 = *(const LAS f32x4*)(L + L_ST1 + b * 32), r1 = *(const LAS f32x4*)(L + L_ST1 + b * 32 + 16);
    float m, rden;
    ln_consts((r0.x + r0.z) + (r1.x + r1.z), (r0.y + r0.w) + (r1.y + r1.w), m, rden);
    lnx = rbf(rbf(lo2f(xh) - m) * rden);
    PIN(lnx);
  }
  const int tl = (8 * (lane >> 4) + ((lane >> 2) & 3)) * 256 + (((2 * w + ((lane & 3) >> 1)) ^ (((lane >> 2) & 3) << 2)) << 4) + (lane & 1) * 8;
  const int pubo = w * 128 + (lane & 15) * 64 + (lane >> 4) * 16;
  STAMP(1);

  if (!isA) {
    f32x4 acc = {0.f, 0.f, 0.f, 0.f};
#pragma unroll
    for (int s = 0; s < 4; ++s) { wait_landed(L, s); acc = mfma(tfrag(L, s, tl), sfrag(L, lane, kq * 128 + 32 * s), acc); }
    release(L, w, lane, 4);
    if ((lane & 15) < 2) *(LAS f32x4*)(L + L_STG + pubo) = acc;
    cbar(L, lane, bar);
    if (w == 0) __builtin_amdgcn_raw_buffer_store_b128(*(const LAS v4u*)(L + L_STG + lane * 16), rs, mybox + BX_MC + lane * 16, 0, AUX_SC1);
  }
  STAMP(2);
  v4u qw[4];
#pragma unroll
  for (int s = 0; s < 4; ++s) { wait_landed(L, offQ + s); qw[s] = tfrag(L, (offQ + s) & 15, tl); }
  release(L, w, lane, offMR);
  STAMP(3);
#pragma unroll 1
  for (int u = 0; u < (isA ? 1 : 3); ++u) {
    const int n = offMR + 2 * u, k0 = ((mu0 + u) & 15) * 64;
    wait_landed(L, n); wait_landed(L, n + 1);
    f32x4 acc = mfma(tfrag(L, n & 15, tl), sfrag(L, lane, k0), f32x4{0.f, 0.f, 0.f, 0.f});
    acc = mfma(tfrag(L, (n + 1) & 15, tl), sfrag(L, lane, k0 + 32), acc);
    release(L, w, lane, (isA || n + 2 < offK) ? n + 2 : offK + w);
    if ((lane & 15) < 2) *(LAS f32x4*)(L + L_STG + 2048 + u * 1024 + pubo) = acc;
  }
  v4u wf[8];
  if (isA) {
#pragma unroll
    for (int q = 0; q < 8; ++q) { wait_landed(L, offWO + q); wf[q] = tfrag(L, (offWO + q) & 15, tl); }
    release(L, w, lane, offK + w);
  }
  {
    lds_wait_ge(L, L_FALIVE, 1);
    v4u g[2];
    const int js = (16 + (kq & 1) * 8 + sg) * 8 + (kq >> 1);
    const int off[2] = {box(js) + BX_MC + w * 128 + piece * 16, box(js + 4) + BX_MC + w * 128 + piece * 16};
    gather<2>(g, off, rs);
    STAMP(26);
    const float sa = rbf(tred(asf(g[0]))), ca = rbf(tred(asf(g[1])));
    const float h = rbf(lnx * rbf(1.f + ca)) + sa;
    if (!(lane & 8)) *(LAS u16*)(L + L_HVEC + (piece >> 2) * 256 + (16 * w + 4 * (piece & 3) + comp) * 2) = (u16)pk(h, h);
    cbar(L, lane, bar);
    STAMP(5);
    if (w >= 1 && w <= (isA ? 1 : 3))
      __builtin_amdgcn_raw_buffer_store_b128(*(const LAS v4u*)(L + L_STG + 1024 + w * 1024 + lane * 16), rs, mybox + BX_MR + (w - 1) * 1024 + lane * 16, 0, AUX_SC1);
    f32x4 acc = {0.f, 0.f, 0.f, 0.f};
#pragma unroll
    for (int s = 0; s < 4; ++s) acc = mfma(qw[s], vfrag(L, L_HVEC, lane, s), acc);
    if ((lane & 15) < 2) *(LAS f32x4*)(L + L_STG + 1024 + (lane & 15) * 512 + (16 * w + 4 * (lane >> 4)) * 4) = acc;
    cbar(L, lane, bar);
    if (w == 0) __builtin_amdgcn_raw_buffer_store_b128(*(const LAS v4u*)(L + L_STG + 1024 + lane * 16), rs, mybox + BX_Q + lane * 16, 0, AUX_SC1);
  }
  STAMP(6);
  v4u qf[4], tw[8], tx[8];
  const int kl = (8 * ((lane & 15) >> 2) + (lane & 3)) * 256 + ((lane & 3) << 6) + (lane >> 4) * 16;
  {
    v4u g[4]; int off[4];
    const int head = hkv * 8 + w;
#pragma unroll
    for (int q = 0; q < 4; ++q) {
      const int jq = ((head & 3) * 8 + 2 * q + (lane >> 5)) * 8 + (head >> 2);
      off[q] = box(jq) + BX_Q + batt * 512 + (lane & 31) * 16;
    }
    STAMP(27);
    wait_landed(L, offK + w);
#pragma unroll
    for (int q = 0; q < 8; ++q) tw[q] = *(const LAS v4u*)(L + L_RING + ((offK + w) & 15) * 8192 + (q >> 2) * 1024 + (kl ^ ((q & 3) << 6)));
    release(L, w, lane, offK + w + 8);
    wait_landed(L, offK + w + 8);
#pragma unroll
    for (int q = 0; q < 8; ++q) tx[q] = *(const LAS v4u*)(L + L_RING + ((offK + w + 8) & 15) * 8192 + (q >> 2) * 1024 + (kl ^ ((q & 3) << 6)));
    release(L, w, lane, offK + w + 16);
    gather_issue<4>(g, off, rs);
    gather_finish_slow<4, HOPB_PAUSE>(g, off, rs);
    STAMP(28);
    const f32x4 t = (asf(g[0]) + asf(g[1])) + (asf(g[2]) + asf(g[3]));
    const f32x2 a = swap32(t.x, t.y), b = swap32(t.z, t.w);
    const unsigned p = pk(a.x + a.y, b.x + b.y);
    LAS u16* qv = (LAS u16*)(L + L_QVEC + w * 256 + (lane & 31) * 8 + (lane >> 5) * 2);
    qv[0] = (u16)p; qv[2] = (u16)(p >> 16);
    cbar(L, lane, bar);
#pragma unroll
    for (int s = 0; s < 4; ++s) qf[s] = *(const LAS v4u*)(L + L_QVEC + (lane & 7) * 256 + s * 64 + (lane >> 4) * 16);
  }
  STAMP(7);
  float ga = 0.f, gb = 0.f, xres = 0.f, bias = 0.f;
  float sc[4][8];
  {
#pragma unroll
    for (int i = 0; i < 4; ++i) {
      const int n = offK + w + 8 * i;
      v4u kf[2][4];
      if (i < 2) {
#pragma unroll
        for (int q = 0; q < 8; ++q) kf[q >> 2][q & 3] = i == 0 ? tw[q] : tx[q];
      } else {
        wait_landed(L, n);
#pragma unroll
        for (int T = 0; T < 2; ++T)
#pragma unroll
          for (int s = 0; s < 4; ++s) kf[T][s] = *(const LAS v4u*)(L + L_RING + (n & 15) * 8192 + T * 1024 + (kl ^ (s << 6)));
        release(L, w, lane, i < 3 ? n + 8 : offV);
      }
#pragma unroll
      for (int T = 0; T < 2; ++T) {
        f32x4 a = {0.f, 0.f, 0.f, 0.f};
#pragma unroll
        for (int s = 0; s < 4; ++s) a = mfma(kf[T][s], qf[s], a);
        const f32x2 lo = rbf2(rbf2(f32x2{a.x, a.y}) * 0.08838834764831845f), hi = rbf2(rbf2(f32x2{a.z, a.w}) * 0.08838834764831845f);
        sc[i][T * 4] = lo.x; sc[i][T * 4 + 1] = lo.y; sc[i][T * 4 + 2] = hi.x; sc[i][T * 4 + 3] = hi.y;
      }
    }
  }
  STAMP(8);
  float mw = sc[0][0];
  {
#pragma unroll
    for (int i = 0; i < 4; ++i)
#pragma unroll
      for (int r = 0; r < 8; ++r) mw = fmaxf(mw, sc[i][r]);
    mw = max32(max16(mw));
    float lw = 0.f;
#pragma unroll
    for (int i = 0; i < 4; ++i)
#pragma unroll
      for (int r = 0; r < 8; ++r) { sc[i][r] = exp_(sc[i][r] - mw); lw += sc[i][r]; }
    lw = sum32(sum16(lw));
    if (lane < 8) *(LAS f32x2*)(L + L_MLW + w * 64 + lane * 8) = f32x2{mw, lw};
    lds_inc(L, L_MLCNT, lane);
  }
  f32x4 sbv = {0.f, 0.f, 0.f, 0.f}, cb1 = {0.f, 0.f, 0.f, 0.f};
  if (isB) {
    v4u g[2];
    const int off[2] = {box(30 * 8 + (w >> 1) + (w & 1) * 8) + BX_T1 + lane * 16, box(30 * 8 + 4 + (w >> 1) + (w & 1) * 8) + BX_T1 + lane * 16};
    gather<2>(g, off, rs);
    sbv = asf(g[0]);
    const f32x4 cb = asf(g[1]);
    const f32x2 c0 = rbf2(f32x2{cb.x, cb.y} + 1.f), c1 = rbf2(f32x2{cb.z, cb.w} + 1.f);
    cb1 = f32x4{c0.x, c0.y, c1.x, c1.y};
    bias = lo2f(*(const GAS u16*)(b1 + (bi >> 2) * 128 + 16 * w + 4 * (piece & 3) + comp));
    PIN(bias); PIN(sbv); PIN(cb1);
  }
  if (isC) {
    const int col = 64 * ci + 16 * (w & 3) + 4 * (piece & 3) + comp;
    xres = lo2f(*(const GAS u16*)(x + (piece >> 2) * D + col));
    bias = lo2f(*(const GAS u16*)(b2 + col));
    lds_wait_ge(L, L_FGATE, 1);
    ga = *(const LAS float*)(L + L_GATE + (w & 3) * 256 + lane * 4);
    gb = *(const LAS float*)(L + L_GATE + (4 + (w & 3)) * 256 + lane * 4);
    PIN(ga); PIN(gb); PIN(xres); PIN(bias);
  }
#pragma unroll
  for (int q = 0; q < 8; ++q) { wait_landed(L, offV + q); tw[q] = tfrag(L, (offV + q) & 15, tl); release(L, w, lane, offV + q + 1); }
#pragma unroll
  for (int q = 0; q < VPARK - 8; ++q) { wait_landed(L, offV + 8 + q); tx[q] = tfrag(L, (offV + 8 + q) & 15, tl); release(L, w, lane, offV + 9 + q); }
  lds_wait_ge(L, L_FML, 1);
  {
    const f32x2 ml = *(const LAS f32x2*)(L + L_MLF + (lane & 7) * 8);
    const float f = exp_(mw - ml.x) * ml.y;
#pragma unroll
    for (int i = 0; i < 4; ++i) {
      const v4u p = {pk(sc[i][0] * f, sc[i][1] * f), pk(sc[i][2] * f, sc[i][3] * f), pk(sc[i][4] * f, sc[i][5] * f), pk(sc[i][6] * f, sc[i][7] * f)};
      if ((lane & 15) < 8) *(LAS v4u*)(L + L_P + (w + 8 * i) * 512 + ((lane >> 4) * 8 + (lane & 7)) * 16) = p;
    }
    cbar(L, lane, bar);
  }
  STAMP(10);
  f32x4 oacc = {0.f, 0.f, 0.f, 0.f};
  const int pl = L_P + ((lane >> 4) * 8 + (lane & 7)) * 16;
#pragma unroll
  for (int i = 0; i < VPARK; ++i) oacc = mfma(i < 8 ? tw[i & 7] : tx[i & 7], *(const LAS v4u*)(L + pl + i * 512), oacc);
#pragma unroll 1
  for (int i = VPARK; i < 32; i += 2) {
    const int n = offV + i;
    wait_landed(L, n + 1); wait_landed(L, n);
    const v4u a0 = tfrag(L, n & 15, tl), a1 = tfrag(L, (n + 1) & 15, tl);
    const v4u p0 = *(const LAS v4u*)(L + pl + i * 512), p1 = *(const LAS v4u*)(L + pl + (i + 1) * 512);
    release(L, w, lane, n + 2);
    oacc = mfma(a0, p0, oacc);
    oacc = mfma(a1, p1, oacc);
  }
  if ((lane & 15) < 8) __builtin_amdgcn_raw_buffer_store_b128(asu(oacc), rs, mybox + BX_O + w * 512 + OIDX * 16, 0, AUX_SC1);
  STAMP(11);

  if (isA) {
    v4u g[4]; int off[4];
#pragma unroll
    for (int q = 0; q < 4; ++q) off[q] = box((sg + 8 * q) * 8 + (w >> 2) * 4 + a_hkv) + BX_O + a_dt * 512 + (w & 3) * 128 + piece * 16;
    gather_issue<4>(g, off, rs);
    gather_finish_slow<4, X1_PAUSE>(g, off, rs);
    STAMP(22);
    const float t = tred((asf(g[0]) + asf(g[1])) + (asf(g[2]) + asf(g[3])));
    if (!(lane & 8)) *(LAS u16*)(L + L_TVEC + (w >> 2) * 256 + OKK * 2) = (u16)pk(t, t);
    WGBAR();
    STAMP(12);
    f32x4 a0 = {0.f, 0.f, 0.f, 0.f}, a1 = {0.f, 0.f, 0.f, 0.f};
#pragma unroll
    for (int s = 0; s < 4; ++s) { const v4u bv = vfrag(L, L_TVEC, lane, s); a0 = mfma(wf[s], bv, a0); a1 = mfma(wf[4 + s], bv, a1); }
    publish2(a0, a1, rs, mybox + BX_T1 + w * 128, lane);
    STAMP(13);
  } else if (isC) {
    if (w < 4) {
      const int ct = 4 * ci + w, cg = ct >> 4, ti = ct & 15;
      v4u g[4]; int off[4], off2[4];
#pragma unroll
      for (int q = 0; q < 4; ++q) {
        const int a = (sg + 8 * q) * 4 + cg;
        off[q] = box((a & 15) * 8 + (a >> 4)) + BX_T1 + ti * 128 + piece * 16;
        const int kf = sg + 8 * q, bb = kf * 4 + cg, gg = bb / 14;
        off2[q] = kf < 28 ? box((16 + bb - gg * 14) * 8 + gg) + BX_T2 + ti * 128 + piece * 16 : WS_BYTES;
        PIN(off2[q]);
      }
      gather_issue<4>(g, off, rs);
      gather_finish_slow<4, T1_PAUSE>(g, off, rs);
      STAMP(22);
      const float t = tred((asf(g[0]) + asf(g[1])) + (asf(g[2]) + asf(g[3])));
      const float x1 = rbf(xres + rbf(ga * rbf(t)));
      { const f32x4 x4 = texp(x1); if (lane < 8) __builtin_amdgcn_raw_buffer_store_b128(asu(x4), rs, mybox + BX_X1 + w * 128 + piece * 16, 0, AUX_SC1); }
      STAMP(12);
      gather_issue<4>(g, off2, rs);
      gather_finish_slow<4, T1_PAUSE>(g, off2, rs);
      STAMP(23);
      const float z = tred((asf(g[0]) + asf(g[1])) + (asf(g[2]) + asf(g[3])));
      float o = x1 + rbf(gb * rbf(rbf(z) + bias));
      if ((unsigned)(__builtin_amdgcn_s_memrealtime() - dg0) > PROBE_X * 100u) o += 1000.f;
      if (!(lane & 8)) *(GAS u16*)(out + (piece >> 2) * D + 64 * ci + 16 * w + 4 * (piece & 3) + comp) = (u16)pk(o, o);
      STAMP(13);
    }
  } else {
#pragma unroll
    for (int q = 0; q < 8; ++q) { wait_landed(L, offW + q); tw[q] = tfrag(L, (offW + q) & 15, tl); }
    const int cc = 2 * w + (sg >> 2);
    const int offx[1] = {box((30 + (cc & 1)) * 8 + (cc >> 1)) + BX_X1 + (sg & 3) * 128 + piece * 16};
    const int kf = bi >> 2, bb = (kf >> 1) * 8 + sg, gg = bb / 14;
    int offh[1] = {box((16 + bb - gg * 14) * 8 + gg) + BX_T1 + ((kf & 1) * 8 + w) * 128 + piece * 16};
    PIN(offh[0]);
    v4u g[1];
    if (B_GATE) { wait_landed(L, offW + 14); wait_landed(L, offW + 15); }
    gather_issue<1>(g, offx, rs);
    gather_finish_slow<1, BX_PAUSE>(g, offx, rs);
    STAMP(12);
    const f32x4 x1 = asf(g[0]);
    float m, rden;
    stat_put(L, L_ST1, w, lane, x1);
    WGBAR();
    stat_get(L, L_ST1, lane, m, rden);
    const f32x2 ha = rbf2(rbf2(ln2(f32x2{x1.x, x1.y}, m, rden) * f32x2{cb1.x, cb1.y}) + f32x2{sbv.x, sbv.y});
    const f32x2 hb = rbf2(rbf2(ln2(f32x2{x1.z, x1.w}, m, rden) * f32x2{cb1.z, cb1.w}) + f32x2{sbv.z, sbv.w});
    stat_put(L, L_ST2, w, lane, f32x4{ha.x, ha.y, hb.x, hb.y});
    WGBAR();
    STAMP(24);
    if (w == (bi & 7)) {
      stat_get(L, L_ST2, lane, m, rden);
      const f32x2 ya = rbf2(ha - m) * rden, yb = rbf2(hb - m) * rden;
      *(LAS v2u*)(L + L_TVEC + (piece >> 2) * 256 + (16 * sg + 4 * (piece & 3)) * 2) = v2u{pk(ya.x, ya.y), pk(yb.x, yb.y)};
    }
    WGBAR();
    {
      f32x4 a0 = {0.f, 0.f, 0.f, 0.f}, a1 = {0.f, 0.f, 0.f, 0.f};
#pragma unroll
      for (int s = 0; s < 4; ++s) { const v4u bv = vfrag(L, L_TVEC, lane, s); a0 = mfma(tw[s], bv, a0); a1 = mfma(tw[4 + s], bv, a1); }
      publish2(a0, a1, rs, mybox + BX_T1 + w * 128, lane);
    }
    STAMP(13);
    gather_issue<1>(g, offh, rs);
#pragma unroll
    for (int q = 0; q < 8; ++q) { wait_landed(L, offW + 8 + q); tw[q] = tfrag(L, (offW + 8 + q) & 15, tl); }
    g[0] = gather_pair<X3_PAUSE>(g[0], offh[0], rs);
    STAMP(23);
    {
      const float u = rbf(rbf(tred(asf(g[0]))) + bias);
      const float z = u * (0.7978845608028654f + 0.035677408136300125f * u * u);
      const float hh = u * rcp_(1.f + __builtin_amdgcn_exp2f(-2.8853900817779268f * z));
      if (!(lane & 8)) *(LAS u16*)(L + L_UVEC + (piece >> 2) * 256 + (16 * w + 4 * (piece & 3) + comp) * 2) = (u16)pk(hh, hh);
      WGBAR();
      STAMP(14);
      f32x4 a0 = {0.f, 0.f, 0.f, 0.f}, a1 = {0.f, 0.f, 0.f, 0.f};
#pragma unroll
      for (int s = 0; s < 4; ++s) { const v4u bv = vfrag(L, L_UVEC, lane, s); a0 = mfma(tw[s], bv, a0); a1 = mfma(tw[4 + s], bv, a1); }
      publish2(a0, a1, rs, mybox + BX_T2 + w * 128, lane);
    }
  }
  asm volatile(".Lfb_last:");
  STAMP(15);
}

}

extern "C" void kernel_launch(void* const* d_in, const int* in_sizes, int n_in, void* d_out, int out_size, void* d_ws, size_t ws_size,
                              hipStream_t stream) {
  (void)in_sizes; (void)n_in; (void)out_size; (void)ws_size;
  (void)hipFuncSetAttribute((const void*)fused_block, hipFuncAttributeMaxDynamicSharedMemorySize, LDS_BYTES);
  hipLaunchKernelGGL(fused_block, dim3(256), dim3(NTHR), LDS_BYTES, stream, (const u16*)d_in[0], (const u16*)d_in[1], (const u16*)d_in[2],
                     (const u16*)d_in[3], (const u16*)d_in[4], (const u16*)d_in[6], (const u16*)d_in[7], (const u16*)d_in[8],
                     (const u16*)d_in[9], (const u16*)d_in[10], (const u16*)d_in[11], (u16*)d_out, (unsigned char*)d_ws);
}
```

```cpp
#include <hip/hip_runtime.h>
#include <hip/hip_bf16.h>
#include <stdint.h>

#define LAS __attribute__((address_space(3)))
#define GAS __attribute__((address_space(1)))
typedef unsigned short u16;
typedef unsigned v4u __attribute__((ext_vector_type(4)));
typedef unsigned v2u __attribute__((ext_vector_type(2)));
typedef float f32x4 __attribute__((ext_vector_type(4)));
typedef float f32x2 __attribute__((ext_vector_type(2)));
typedef short s16x4 __attribute__((ext_vector_type(4)));
typedef short bf16x8 __attribute__((ext_vector_type(8)));
typedef __bf16 bf16x2 __attribute__((ext_vector_type(2)));

namespace {

constexpr int D = 1024, SK = 32768;
constexpr int NTHR = 704;
constexpr int W_PRO = 18, W_KV = 12, W_WO = 16, W_MLP = 10;
constexpr int VPARK = 12;

constexpr int WS_EPOCH = 0, WS_ALIVE = 4096, WS_BOX = 65536, BOX = 16384;
constexpr int BX_MC = 0;
constexpr int BX_Q = 1024;
constexpr int BX_ML = 2048;
constexpr int BX_MR = 3072;
constexpr int BX_O = 6144;
constexpr int BX_T1 = 10240;
constexpr int BX_X1 = BX_T1 + 1024;
constexpr int BX_T2 = 12288;
constexpr int WS_BYTES = WS_BOX + 256 * BOX;
constexpr unsigned SENT = 0xFFFFFFFFu;
constexpr unsigned SPIN_CAP = 1u << 15, LDS_CAP = 1u << 20;
constexpr int AUX_SC1 = 16;

constexpr int L_RING = 0, L_P = 131072, L_CTL = L_P + 16384;
constexpr int L_X = L_P, L_C = L_P + 4096, L_STG = L_P + 8192;
constexpr int L_LANDED = L_CTL;
constexpr int L_PROG = L_CTL + 32;
constexpr int L_BAR = L_CTL + 64, L_MLCNT = L_CTL + 68, L_FALIVE = L_CTL + 72, L_FML = L_CTL + 76, L_FAUX = L_CTL + 80, L_FGATE = L_CTL + 88;
constexpr int L_SVEC = L_CTL + 256;
constexpr int L_HVEC = L_SVEC + 4096;
constexpr int L_QVEC = L_HVEC + 512;
constexpr int L_ST1 = L_QVEC + 2048;
constexpr int L_ST2 = L_ST1 + 128;
constexpr int L_MLW = L_ST2 + 128;
constexpr int L_MLF = L_MLW + 512;
constexpr int L_TVEC = L_MLF + 64;
constexpr int L_UVEC = L_TVEC + 512;
constexpr int L_SBST = L_TVEC;
constexpr int L_GATE = L_UVEC + 512;
constexpr int LDS_BYTES = L_GATE + 2048;
static_assert(LDS_BYTES <= 160 * 1024 && LDS_BYTES > 80 * 1024, "one workgroup per CU");

typedef LAS unsigned char* lptr;

__device__ __forceinline__ float lo2f(unsigned w) { return __uint_as_float(w << 16); }
__device__ __forceinline__ float hi2f(unsigned w) { return __uint_as_float(w & 0xffff0000u); }
__device__ __forceinline__ unsigned pk(float a, float b) {
  bf16x2 v = {(__bf16)a, (__bf16)b};
  return __builtin_bit_cast(unsigned, v);
}
__device__ __forceinline__ float rbf(float a) { return hi2f(pk(a, a)); }
__device__ __forceinline__ f32x2 rbf2(f32x2 a) {
  unsigned p = pk(a.x, a.y);
  asm("" : "+v"(p));
  return f32x2{lo2f(p), hi2f(p)};
}
__device__ __forceinline__ f32x4 mfma(v4u a, v4u b, f32x4 c) {
  return __builtin_amdgcn_mfma_f32_16x16x32_bf16(__builtin_bit_cast(bf16x8, a), __builtin_bit_cast(bf16x8, b), c, 0, 0, 0);
}
template <int CTRL> __device__ __forceinline__ float dpp(float x) {
  return __builtin_bit_cast(float, __builtin_amdgcn_mov_dpp(__builtin_bit_cast(int, x), CTRL, 0xf, 0xf, true));
}
constexpr int XOR1 = 0xB1, XOR2 = 0x4E, ROR4 = 0x124, ROR8 = 0x128;
__device__ __forceinline__ f32x2 swap32(float a, float b) {
  auto s = __builtin_amdgcn_permlane32_swap(__float_as_uint(a), __float_as_uint(b), false, false);
  return f32x2{__uint_as_float(s[0]), __uint_as_float(s[1])};
}
__device__ __forceinline__ f32x2 swap16(float a, float b) {
  auto s = __builtin_amdgcn_permlane16_swap(__float_as_uint(a), __float_as_uint(b), false, false);
  return f32x2{__uint_as_float(s[0]), __uint_as_float(s[1])};
}
__device__ __forceinline__ float sum16(float x) { const f32x2 s = swap16(x, x); return s.x + s.y; }
__device__ __forceinline__ float sum32(float x) { const f32x2 s = swap32(x, x); return s.x + s.y; }
__device__ __forceinline__ float max16(float x) { const f32x2 s = swap16(x, x); return fmaxf(s.x, s.y); }
__device__ __forceinline__ float max32(float x) { const f32x2 s = swap32(x, x); return fmaxf(s.x, s.y); }
__device__ __forceinline__ float tred(f32x4 v) {
  const f32x2 a = swap32(v.x, v.y), b = swap32(v.z, v.w);
  const f32x2 c = swap16(a.x + a.y, b.x + b.y);
  const float t = c.x + c.y;
  return t + dpp<ROR8>(t);
}
__device__ __forceinline__ int tcomp(int lane) { return ((lane >> 4) & 1) * 2 + (lane >> 5); }
__device__ __forceinline__ f32x4 texp(float t) {
  const f32x2 a = swap16(t, t);
  const f32x2 b = swap32(a.x, a.x), c = swap32(a.y, a.y);
  return f32x4{b.x, b.y, c.x, c.y};
}
__device__ __forceinline__ f32x4 asf(v4u v) { return __builtin_bit_cast(f32x4, v); }
__device__ __forceinline__ v4u asu(f32x4 v) { return __builtin_bit_cast(v4u, v); }
__device__ __forceinline__ unsigned umax4(v4u v) { return max(max(max(v.x, v.y), v.z), v.w); }
__device__ __forceinline__ float exp_(float x) { return __builtin_amdgcn_exp2f(x * 1.4426950408889634f); }
__device__ __forceinline__ float rcp_(float x) { return __builtin_amdgcn_rcpf(x); }

#define LGKM0() asm volatile("s_waitcnt lgkmcnt(0)" ::: "memory")
#define VM0() asm volatile("s_waitcnt vmcnt(0)" ::: "memory")
#define CFENCE() asm volatile("" ::: "memory")
#define PIN(x) asm volatile("" : "+v"(x))
#define WGBAR() do { LGKM0(); __builtin_amdgcn_s_barrier(); CFENCE(); } while (0)

__device__ __forceinline__ void dma16(unsigned voff, const unsigned char* sbase, unsigned ldsaddr) {
  unsigned keep;
  asm volatile("s_mov_b32 %0, m0\n\ts_mov_b32 m0, %3\n\ts_nop 4\n\tglobal_load_lds_dwordx4 %1, %2 nt\n\ts_mov_b32 m0, %0"
               : "=&s"(keep) : "v"(voff), "s"(sbase), "s"(ldsaddr) : "memory");
}
__device__ __forceinline__ void lds_set(lptr L, int off, int lane, unsigned v) {
  if (lane == 0) *(volatile LAS unsigned*)(L + off) = v;
}
__device__ __forceinline__ void lds_wait_ge(lptr L, int off, unsigned need) {
#pragma nounroll
  for (unsigned sp = 0; (unsigned)__builtin_amdgcn_readfirstlane(*(volatile LAS unsigned*)(L + off)) < need && sp < LDS_CAP; ++sp) { }
  CFENCE();
}
__device__ __forceinline__ void wait_landed(lptr L, int n) { lds_wait_ge(L, L_LANDED + 4 * (n & 1), (unsigned)(n >> 1) + 1u); }
__device__ __forceinline__ void release(lptr L, int w, int lane, int n) { LGKM0(); lds_set(L, L_PROG + 4 * w, lane, (unsigned)n); }
__device__ __forceinline__ void lds_inc(lptr L, int off, int lane) {
  LGKM0();
  if (lane == 0) __hip_atomic_fetch_add((LAS unsigned*)(L + off), 1u, __ATOMIC_RELAXED, __HIP_MEMORY_SCOPE_WORKGROUP);
}
__device__ __forceinline__ void cbar(lptr L, int lane, unsigned& target) {
  target += 8;
  lds_inc(L, L_BAR, lane);
  lds_wait_ge(L, L_BAR, target);
}
__device__ __forceinline__ v4u tfrag(lptr L, int slot, int tl) {
  const s16x4 a = __builtin_amdgcn_ds_read_tr16_b64_v4i16((LAS s16x4*)(L + L_RING + slot * 8192 + tl));
  const s16x4 b = __builtin_amdgcn_ds_read_tr16_b64_v4i16((LAS s16x4*)(L + L_RING + slot * 8192 + tl + 1024));
  const v2u x = __builtin_bit_cast(v2u, a), y = __builtin_bit_cast(v2u, b);
  return v4u{x.x, x.y, y.x, y.y};
}
__device__ __forceinline__ void kfrags(lptr L, int slot, int kl, v4u (&f)[8]) {
#pragma unroll
  for (int q = 0; q < 8; ++q) f[q] = *(const LAS v4u*)(L + L_RING + slot * 8192 + (q >> 2) * 1024 + (kl ^ ((q & 3) << 6)));
}
__device__ __forceinline__ v4u sfrag(lptr L, int lane, int k0) {
  return *(const LAS v4u*)(L + L_SVEC + (lane & 1) * 2048 + k0 * 2 + (lane >> 4) * 16);
}
__device__ __forceinline__ v4u vfrag(lptr L, int off, int lane, int s) {
  return *(const LAS v4u*)(L + off + (lane & 1) * 256 + s * 64 + (lane >> 4) * 16);
}
template <int N>
__device__ __forceinline__ void gather_issue(v4u (&v)[N], const int (&off)[N], __amdgpu_buffer_rsrc_t rs) {
#pragma unroll
  for (int q = 0; q < N; ++q) v[q] = __builtin_amdgcn_raw_buffer_load_b128(rs, off[q], 0, AUX_SC1);
}
template <int N, int PAUSE>
__device__ __forceinline__ void gather_finish(v4u (&v)[N], const int (&off)[N], __amdgpu_buffer_rsrc_t rs) {
#pragma nounroll
  for (unsigned sp = 0; sp < SPIN_CAP; ++sp) {
    unsigned mx = umax4(v[0]);
#pragma unroll
    for (int q = 1; q < N; ++q) mx = max(mx, umax4(v[q]));
    if (__builtin_amdgcn_ballot_w64(mx == SENT) == 0) break;
    if (PAUSE) __builtin_amdgcn_s_sleep(PAUSE);
    CFENCE();
    if (mx == SENT) gather_issue<N>(v, off, rs);
  }
}
template <int N, int PAUSE = 0>
__device__ __forceinline__ void gather(v4u (&v)[N], const int (&off)[N], __amdgpu_buffer_rsrc_t rs) {
  gather_issue<N>(v, off, rs);
  gather_finish<N, PAUSE>(v, off, rs);
}
__device__ __forceinline__ f32x4 sum4(const v4u (&g)[4]) { return (asf(g[0]) + asf(g[1])) + (asf(g[2]) + asf(g[3])); }
__device__ __forceinline__ void tile_pair(lptr L, int vec, const v4u* f0, const v4u* f1, __amdgpu_buffer_rsrc_t rs, int off, int lane) {
  f32x4 a0 = {0.f, 0.f, 0.f, 0.f}, a1 = {0.f, 0.f, 0.f, 0.f};
#pragma unroll
  for (int s = 0; s < 4; ++s) { const v4u bv = vfrag(L, vec, lane, s); a0 = mfma(f0[s], bv, a0); a1 = mfma(f1[s], bv, a1); }
  if ((lane & 15) < 2) {
    __builtin_amdgcn_raw_buffer_store_b128(asu(a0), rs, off + (lane & 1) * 64 + (lane >> 4) * 16, 0, AUX_SC1);
    __builtin_amdgcn_raw_buffer_store_b128(asu(a1), rs, off + 1024 + (lane & 1) * 64 + (lane >> 4) * 16, 0, AUX_SC1);
  }
}
__device__ __forceinline__ int box(int j) { return WS_BOX + j * BOX; }
__device__ __forceinline__ void ln_consts(float s, float q, float& m, float& rden) {
  const float mean = s * (1.f / D);
  const float var = fmaxf(q * (1.f / D) - mean * mean, 0.f);
  m = rbf(mean);
  rden = rcp_(rbf(__builtin_amdgcn_sqrtf(rbf(rbf(var) + 1e-5f))));
}
__device__ __forceinline__ f32x2 ln2(f32x2 x, float m, float rden) { return rbf2(rbf2(x - m) * rden); }
__device__ __forceinline__ void stat_put(lptr L, int off, int w, int lane, f32x4 v) {
  const float s = (v.x + v.y) + (v.z + v.w), q = (v.x * v.x + v.y * v.y) + (v.z * v.z + v.w * v.w);
  const f32x2 a = swap32(s, q);
  float r = sum16(a.x + a.y);
  r += dpp<ROR8>(r); r += dpp<XOR2>(r); r += dpp<XOR1>(r);
  if ((lane & 0x1B) == 0) *(LAS float*)(L + off + w * 16 + (lane & 4) * 2 + (lane >> 5) * 4) = r;
}
__device__ __forceinline__ void stat_get(lptr L, int off, int lane, float& m, float& rden) {
  const f32x2 p = *(const LAS f32x2*)(L + off + (lane >> 3) * 16 + (lane & 4) * 2);
  const f32x2 a = swap32(p.x, p.y);
  float r = sum16(a.x + a.y);
  r += dpp<ROR8>(r);
  const f32x2 t = swap32(r, r);
  ln_consts(t.x, t.y, m, rden);
}

__device__ __forceinline__ int read_minprog(lptr L, int lane) {
  unsigned m = *(volatile LAS unsigned*)(L + L_PROG + 4 * (lane & 7));
  m = min(m, (unsigned)__builtin_amdgcn_mov_dpp((int)m, XOR1, 0xf, 0xf, true));
  m = min(m, (unsigned)__builtin_amdgcn_mov_dpp((int)m, XOR2, 0xf, 0xf, true));
  m = min(m, (unsigned)__builtin_amdgcn_mov_dpp((int)m, ROR4, 0xf, 0xf, true));
  return __builtin_amdgcn_readfirstlane((int)m);
}
template <int W>
__device__ __forceinline__ void load_block(lptr L, int ld, int lane, int n, const unsigned char* bp, int ldb, int hi, int& minprog) {
  if (minprog < n - 15) {
    minprog = read_minprog(L, lane);
    if (minprog < n - 15) {
      VM0();
      lds_set(L, L_LANDED + 4 * ld, lane, (unsigned)(n >> 1));
      for (unsigned sp = 0; sp < LDS_CAP; ++sp) {
        minprog = read_minprog(L, lane);
        if (minprog >= n - 15) break;
      }
    }
  }
  const unsigned voff = (unsigned)((lane >> 4) * ldb + (((lane & 15) ^ ((lane >> 4) << 2)) << 4));
  const unsigned dst = (unsigned)(size_t)L + L_RING + (unsigned)(n & 15) * 8192u;
#pragma unroll
  for (int p = 0; p < 8; ++p) {
    dma16(voff, bp + (size_t)((p & 3) * 4) * ldb + (size_t)(p >> 2) * hi, dst + p * 1024);
    asm volatile("s_waitcnt vmcnt(%0)" ::"i"(W - 1) : "memory");
    if (((p + 2 - W) & 7) == 0) {
      const int done = (n >> 1) + (p + 2 - W) / 8;
      if (done > 0) lds_set(L, L_LANDED + 4 * ld, lane, (unsigned)done);
    }
  }
}

__global__ void __launch_bounds__(NTHR) fused_block(const u16* __restrict__ x, const u16* __restrict__ kc, const u16* __restrict__ vc,
                                                     const u16* __restrict__ wq, const u16* __restrict__ wo, const u16* __restrict__ w1,
                                                     const u16* __restrict__ b1, const u16* __restrict__ w2, const u16* __restrict__ b2,
                                                     const u16* __restrict__ cvec, const u16* __restrict__ wmod, u16* __restrict__ out,
                                                     unsigned char* ws) {
  extern __shared__ __attribute__((aligned(16))) unsigned char smem[];
  lptr L = (lptr)smem;
  const int tid = threadIdx.x, lane = tid & 63, w = __builtin_amdgcn_readfirstlane(tid >> 6);
  const int j = blockIdx.x, grp = j & 7, split = j >> 3, batt = grp >> 2, hkv = grp & 3;
  const bool isA = split < 16, isC = split >= 30, isB = !isA && !isC;
  const int kq = split & 7, g32 = grp * 4 + (split >> 3), g16 = grp * 2 + ((split >> 3) & 1);
  const int mu0 = isA ? 2 * (grp * 16 + split) : 256 + 2 * (grp * 16 + split - 16);
  const int offQ = isA ? 0 : 4, offMR = offQ + 4;
  constexpr int offWO = 8, offK = 12, offV = offK + 32, offW = offV + 32;
  const int ai = grp * 16 + split, a_hkv = ai >> 5, a_dt = (ai >> 2) & 7, a_cg = ai & 3;
  const int bi = grp * 14 + (split - 16), ci = grp * 2 + (split - 30);

  if (tid < 64) *(volatile LAS unsigned*)(L + L_CTL + 4 * tid) = 0u;
  WGBAR();
  unsigned long long epoch = 0, tag = 0;
  if (w == 10) {
    const __amdgpu_buffer_rsrc_t rsh = __builtin_amdgcn_make_buffer_rsrc((void*)ws, 0, WS_BYTES, 0x00020000);
    const v2u ep = __builtin_amdgcn_raw_buffer_load_b64(rsh, WS_EPOCH, 0, AUX_SC1);
    const v4u s4 = {SENT, SENT, SENT, SENT};
    const unsigned used = isA ? 0x0FDEu : (isB ? 0x3FDFu : 0x0FDFu);
#pragma unroll 1
    for (int u = 0; u < 14; ++u)
      if ((used >> u) & 1) __builtin_amdgcn_raw_buffer_store_b128(s4, rsh, box(j) + u * 1024 + lane * 16, 0, AUX_SC1);
    VM0();
    epoch = ((unsigned long long)(unsigned)__builtin_amdgcn_readfirstlane((int)ep.y) << 32) | (unsigned)__builtin_amdgcn_readfirstlane((int)ep.x);
    tag = (epoch ^ 0x9E3779B97F4A7C15ull) * 0xD6E8FEB86659FD93ull + 1ull;
    if (lane == 0) __hip_atomic_store((GAS unsigned long long*)(ws + WS_ALIVE + j * 16), tag, __ATOMIC_RELAXED, __HIP_MEMORY_SCOPE_AGENT);
  }
  if (__builtin_expect(w == 8 || w == 9, 1)) {
    const int ld = w - 8;
    __builtin_amdgcn_s_setprio(3);
    int minprog = 0;
    const unsigned char* aux = (const unsigned char*)(ld ? cvec : x);
#pragma unroll
    for (int p = 0; p < 4; ++p) dma16((unsigned)lane * 16u, aux + p * 1024, (unsigned)(size_t)L + L_X + ld * 4096 + p * 1024);
    const size_t kvoff = ((size_t)grp * SK + split * 1024) * 256;
    const unsigned char* wo_t = (const unsigned char*)wo + ((size_t)(a_hkv * 8) * 128 + 16 * a_dt) * 2048 + a_cg * 512;
#pragma unroll 1
    for (int n = ld; n < offW; n += 2) {
      if (n < offK) {
        const unsigned char* bp; int ldb, hi;
        if (isA && n >= offWO) { bp = wo_t + (size_t)(n - offWO) * 256 * 2048; ldb = 2048; hi = 128 * 2048; }
        else {
          if (n < offQ) { bp = (const unsigned char*)wmod + (size_t)kq * 128 * 12288 + g16 * 256; ldb = 12288; }
          else if (n < offMR) { bp = (const unsigned char*)wq + (size_t)kq * 128 * 8192 + g32 * 256; ldb = 8192; }
          else { const int u = mu0 + ((n - offMR) >> 1); bp = (const unsigned char*)wmod + (size_t)(u & 15) * 64 * 12288 + 4096 + (u >> 4) * 256; ldb = 12288; }
          bp += (size_t)((n < offMR ? n & 3 : (n - offMR) & 1) * 32) * ldb; hi = 16 * ldb;
        }
        load_block<W_PRO>(L, ld, lane, n, bp, ldb, hi, minprog);
        if (n < 2) {
          asm volatile("s_waitcnt vmcnt(8)" ::: "memory");
          lds_set(L, L_FAUX + 4 * ld, lane, 1u);
        }
      } else {
        const unsigned char* bp = n < offV ? (const unsigned char*)kc + kvoff + (size_t)(n - offK) * 8192 : (const unsigned char*)vc + kvoff + (size_t)(n - offV) * 8192;
        load_block<W_KV>(L, ld, lane, n, bp, 256, 4096, minprog);
      }
    }
    int nend = offW;
    if (isA) {
      nend = offW + 4;
#pragma unroll 1
      for (int n = offW + ld; n < nend; n += 2) load_block<W_WO>(L, ld, lane, n, wo_t + (size_t)(n - offW) * 256 * 2048 + 256, 2048, 128 * 2048, minprog);
    }
    if (isB) {
      nend = offW + 16;
#pragma unroll 1
      for (int n = offW + ld; n < nend; n += 2) {
        const int q = n - offW;
        const unsigned char* bp; int ldb;
        if (q < 8) { bp = (const unsigned char*)w1 + (size_t)(bi & 7) * 128 * 7168 + (bi >> 3) * 512; ldb = 7168; }
        else { bp = (const unsigned char*)w2 + (size_t)(bi >> 2) * 128 * 2048 + (bi & 3) * 512; ldb = 2048; }
        load_block<W_MLP>(L, ld, lane, n, bp + (size_t)((q & 3) * 32) * ldb + ((q >> 2) & 1) * 256, ldb, 16 * ldb, minprog);
      }
    }
    VM0();
    lds_set(L, L_LANDED + 4 * ld, lane, (unsigned)(nend >> 1));
    return;
  }

  const __amdgpu_buffer_rsrc_t rs = __builtin_amdgcn_make_buffer_rsrc((void*)ws, 0, WS_BYTES, 0x00020000);
  const int mybox = box(j);
  const int piece = lane & 7, sg = lane >> 3, comp = tcomp(lane);

  if (w == 10) {
    for (int i = 0; i < 3; ++i) __builtin_amdgcn_s_sleep(24);
    for (unsigned sp = 0; sp < SPIN_CAP; ++sp) {
      bool ok = true;
#pragma unroll
      for (int q = 0; q < 4; ++q) {
        const v2u t = __builtin_amdgcn_raw_buffer_load_b64(rs, WS_ALIVE + (q * 64 + lane) * 16, 0, AUX_SC1);
        ok = ok && t.x == (unsigned)tag && t.y == (unsigned)(tag >> 32);
      }
      if (__all(ok)) break;
      __builtin_amdgcn_s_sleep(4);
      CFENCE();
    }
    lds_set(L, L_FALIVE, lane, 1u);
    if (j == 0 && lane == 0) __hip_atomic_store((GAS unsigned long long*)(ws + WS_EPOCH), epoch + 1ull, __ATOMIC_RELAXED, __HIP_MEMORY_SCOPE_AGENT);
    if (isC) {
      lds_wait_ge(L, L_LANDED, (unsigned)(offK + 8) >> 1);
      auto mr = [&](int cg, int kh, int line) {
        const int u = cg * 16 + kh;
        const int ia = (u >> 1) & 127, jj = ((u < 256 ? 0 : 16) + (ia & 15)) * 8 + (ia >> 4);
        return box(jj) + BX_MR + (u & 1) * 1024 + line * 128 + piece * 16;
      };
#pragma unroll 1
      for (int h4 = 0; h4 < 4; ++h4) {
        const int cg = h4 < 2 ? 8 + ci : (h4 == 2 ? 0 : 24) + (ci >> 1), l0 = h4 < 2 ? 4 * h4 : (ci & 1) * 4;
        const int o0 = mr(cg, sg, l0), o1 = mr(cg, sg + 8, l0);
        v4u g[8];
        const int off[8] = {o0, o1, o0 + 128, o1 + 128, o0 + 256, o1 + 256, o0 + 384, o1 + 384};
        gather<8, 8>(g, off, rs);
#pragma unroll
        for (int q = 0; q < 4; ++q) {
          const float t = rbf(tred(asf(g[2 * q]) + asf(g[2 * q + 1])));
          if (h4 < 2) *(LAS float*)(L + L_SBST + (l0 + q) * 128 + piece * 16 + comp * 4) = t;
          else *(LAS float*)(L + L_GATE + ((h4 - 2) * 4 + q) * 256 + lane * 4) = t;
        }
      }
      LGKM0();
      __builtin_amdgcn_raw_buffer_store_b128(*(const LAS v4u*)(L + L_SBST + lane * 16), rs, mybox + BX_T1 + lane * 16, 0, AUX_SC1);
      lds_set(L, L_FGATE, lane, 1u);
    }
    lds_wait_ge(L, L_MLCNT, 8);
    const f32x2 r = *(const LAS f32x2*)(L + L_MLW + sg * 64 + piece * 8);
    float M = r.x;
    M = fmaxf(M, dpp<ROR8>(M)); M = max32(max16(M));
    float Ls = r.y * exp_(r.x - M);
    Ls += dpp<ROR8>(Ls); Ls = sum32(sum16(Ls));
    const float M1 = dpp<XOR1>(M), L1 = dpp<XOR1>(Ls);
    if (lane < 8 && !(lane & 1)) __builtin_amdgcn_raw_buffer_store_b128(asu(f32x4{M, Ls, M1, L1}), rs, mybox + BX_ML + lane * 8, 0, AUX_SC1);
    for (int i = 0; i < 2; ++i) __builtin_amdgcn_s_sleep(32);
    v4u g[2];
    const int off[2] = {box((lane >> 2) * 8 + grp) + BX_ML + (lane & 3) * 16, box(((lane >> 2) + 16) * 8 + grp) + BX_ML + (lane & 3) * 16};
    gather<2, 16>(g, off, rs);
    const f32x4 u = asf(g[0]), v = asf(g[1]);
    float M0 = fmaxf(u.x, v.x), Mb = fmaxf(u.z, v.z);
    M0 = fmaxf(M0, dpp<ROR4>(M0)); M0 = fmaxf(M0, dpp<ROR8>(M0)); M0 = max32(max16(M0));
    Mb = fmaxf(Mb, dpp<ROR4>(Mb)); Mb = fmaxf(Mb, dpp<ROR8>(Mb)); Mb = max32(max16(Mb));
    float L0 = u.y * exp_(u.x - M0) + v.y * exp_(v.x - M0), Lb = u.w * exp_(u.z - Mb) + v.w * exp_(v.z - Mb);
    L0 += dpp<ROR4>(L0); L0 += dpp<ROR8>(L0); L0 = sum32(sum16(L0));
    Lb += dpp<ROR4>(Lb); Lb += dpp<ROR8>(Lb); Lb = sum32(sum16(Lb));
    if (lane < 4) *(LAS f32x4*)(L + L_MLF + lane * 16) = f32x4{M0, 1.f / L0, Mb, 1.f / Lb};
    LGKM0();
    lds_set(L, L_FML, lane, 1u);
    return;
  }

  unsigned warm = 0;
  if (w == 0) {
    const unsigned char* p = (const unsigned char*)ws; size_t o = 0;
    const size_t kv0 = ((size_t)grp * SK + split * 1024) * 256;
    const int l = lane;
    if (l < 2) { p = (const unsigned char*)kc; o = kv0 + l * 131072; }
    else if (l < 4) { p = (const unsigned char*)vc; o = kv0 + (l - 2) * 131072; }
    else if (l < 8) { p = (const unsigned char*)wq; o = (size_t)kq * 128 * 8192 + (l - 4) * 262144 + g32 * 256; }
    else if (l < 14) { p = (const unsigned char*)wmod; o = (size_t)kq * 128 * 12288 + (l - 8) * 262144; }
    else if (l < 20) { const int u = mu0 + (l - 14) / 3; p = (const unsigned char*)wmod; o = (size_t)(u & 15) * 64 * 12288 + ((l - 14) % 3) * 262144 + 4096; }
    else if (l < 28) {
      if (isA) { p = (const unsigned char*)wo; o = (size_t)(a_hkv * 8) * 128 * 2048 + (l - 20) * 262144; }
      else if (isB && l < 24) { p = (const unsigned char*)w1; o = (size_t)(bi & 7) * 128 * 7168 + (l - 20) * 229376; }
      else if (isB) { p = (const unsigned char*)w2; o = (size_t)(bi >> 2) * 128 * 2048; }
    }
    else if (l < 45) o = (size_t)(l - 28) * 262144;
    else if (l == 45) p = (const unsigned char*)b1;
    else if (l == 46) p = (const unsigned char*)b2;
    asm volatile("global_load_dword %0, %1, off" : "=&v"(warm) : "v"(p + o) : "memory");
  }
  unsigned bar = 0;
  lds_wait_ge(L, L_FAUX, 1); lds_wait_ge(L, L_FAUX + 4, 1);
  asm volatile("s_waitcnt vmcnt(0)" : "+v"(warm) :: "memory");
  const unsigned xh = *(const LAS u16*)(L + L_X + ((piece >> 2) * D + kq * 128 + 16 * w + 4 * (piece & 3) + comp) * 2);
  {
    const v2u cr = *(const LAS v2u*)(L + L_C + tid * 8);
    float s[4] = {lo2f(cr.x), hi2f(cr.x), lo2f(cr.y), hi2f(cr.y)};
#pragma unroll
    for (int i = 0; i < 4; ++i) s[i] = s[i] * rcp_(1.f + exp_(-s[i]));
    *(LAS v2u*)(L + L_SVEC + tid * 8) = v2u{pk(s[0], s[1]), pk(s[2], s[3])};
  }
  {
    const v2u xr = *(const LAS v2u*)(L + L_X + tid * 8);
    const float a0 = lo2f(xr.x), a1 = hi2f(xr.x), a2 = lo2f(xr.y), a3 = hi2f(xr.y);
    const f32x2 sq = swap32((a0 + a1) + (a2 + a3), (a0 * a0 + a1 * a1) + (a2 * a2 + a3 * a3));
    float r = sum16(sq.x + sq.y);
    r += dpp<ROR8>(r); r += dpp<ROR4>(r); r += dpp<XOR2>(r); r += dpp<XOR1>(r);
    if ((lane & 31) == 0) *(LAS float*)(L + L_ST1 + w * 8 + (lane >> 5) * 4) = r;
  }
  cbar(L, lane, bar);
  float lnx;
  {
    const int b = piece >> 2;
    const f32x4 r0 = *(const LAS f32x4*)(L + L_ST1 + b * 32), r1 = *(const LAS f32x4*)(L + L_ST1 + b * 32 + 16);
    float m, rden;
    ln_consts((r0.x + r0.z) + (r1.x + r1.z), (r0.y + r0.w) + (r1.y + r1.w), m, rden);
    lnx = rbf(rbf(lo2f(xh) - m) * rden);
    PIN(lnx);
  }
  const int tl = (8 * (lane >> 4) + ((lane >> 2) & 3)) * 256 + (((2 * w + ((lane & 3) >> 1)) ^ (((lane >> 2) & 3) << 2)) << 4) + (lane & 1) * 8;
  const int pubo = w * 128 + (lane & 15) * 64 + (lane >> 4) * 16;

  if (!isA) {
    f32x4 acc = {0.f, 0.f, 0.f, 0.f};
#pragma unroll
    for (int s = 0; s < 4; ++s) { wait_landed(L, s); acc = mfma(tfrag(L, s, tl), sfrag(L, lane, kq * 128 + 32 * s), acc); }
    release(L, w, lane, 4);
    if ((lane & 15) < 2) *(LAS f32x4*)(L + L_STG + pubo) = acc;
    cbar(L, lane, bar);
    if (w == 0) __builtin_amdgcn_raw_buffer_store_b128(*(const LAS v4u*)(L + L_STG + lane * 16), rs, mybox + BX_MC + lane * 16, 0, AUX_SC1);
  }
  v4u qw[4];
#pragma unroll
  for (int s = 0; s < 4; ++s) { wait_landed(L, offQ + s); qw[s] = tfrag(L, (offQ + s) & 15, tl); }
  release(L, w, lane, offMR);
#pragma unroll 1
  for (int u = 0; u < 2; ++u) {
    const int n = offMR + 2 * u, k0 = ((mu0 + u) & 15) * 64;
    wait_landed(L, n); wait_landed(L, n + 1);
    f32x4 acc = mfma(tfrag(L, n & 15, tl), sfrag(L, lane, k0), f32x4{0.f, 0.f, 0.f, 0.f});
    acc = mfma(tfrag(L, (n + 1) & 15, tl), sfrag(L, lane, k0 + 32), acc);
    release(L, w, lane, n + 2 < offK ? n + 2 : offK + w);
    if ((lane & 15) < 2) *(LAS f32x4*)(L + L_STG + 2048 + u * 1024 + pubo) = acc;
  }
  v4u wf[4];
  if (isA) {
#pragma unroll
    for (int q = 0; q < 4; ++q) { wait_landed(L, offWO + q); wf[q] = tfrag(L, offWO + q, tl); }
    release(L, w, lane, offK + w);
  }
  {
    lds_wait_ge(L, L_FALIVE, 1);
    v4u g[2];
    const int js = (16 + (kq & 1) * 8 + sg) * 8 + (kq >> 1);
    const int off[2] = {box(js) + BX_MC + w * 128 + piece * 16, box(js + 4) + BX_MC + w * 128 + piece * 16};
    gather<2>(g, off, rs);
    const float sa = rbf(tred(asf(g[0]))), ca = rbf(tred(asf(g[1])));
    const float h = rbf(lnx * rbf(1.f + ca)) + sa;
    if (!(lane & 8)) *(LAS u16*)(L + L_HVEC + (piece >> 2) * 256 + (16 * w + 4 * (piece & 3) + comp) * 2) = (u16)pk(h, h);
    cbar(L, lane, bar);
    if (w >= 1 && w <= 2)
      __builtin_amdgcn_raw_buffer_store_b128(*(const LAS v4u*)(L + L_STG + 1024 + w * 1024 + lane * 16), rs, mybox + BX_MR + (w - 1) * 1024 + lane * 16, 0, AUX_SC1);
    f32x4 acc = {0.f, 0.f, 0.f, 0.f};
#pragma unroll
    for (int s = 0; s < 4; ++s) acc = mfma(qw[s], vfrag(L, L_HVEC, lane, s), acc);
    if ((lane & 15) < 2) *(LAS f32x4*)(L + L_STG + 1024 + (lane & 15) * 512 + (16 * w + 4 * (lane >> 4)) * 4) = acc;
    cbar(L, lane, bar);
    if (w == 0) __builtin_amdgcn_raw_buffer_store_b128(*(const LAS v4u*)(L + L_STG + 1024 + lane * 16), rs, mybox + BX_Q + lane * 16, 0, AUX_SC1);
  }
  v4u qf[4], tw[8], tx[8];
  const int kl = (8 * ((lane & 15) >> 2) + (lane & 3)) * 256 + ((lane & 3) << 6) + (lane >> 4) * 16;
  {
    wait_landed(L, offK + w);
    kfrags(L, (offK + w) & 15, kl, tw);
    release(L, w, lane, offK + w + 8);
    wait_landed(L, offK + w + 8);
    kfrags(L, (offK + w + 8) & 15, kl, tx);
    release(L, w, lane, offK + w + 16);
    v4u g[4]; int off[4];
    const int head = hkv * 8 + w;
#pragma unroll
    for (int q = 0; q < 4; ++q) {
      const int jq = ((head & 3) * 8 + 2 * q + (lane >> 5)) * 8 + (head >> 2);
      off[q] = box(jq) + BX_Q + batt * 512 + (lane & 31) * 16;
    }
    gather<4, 8>(g, off, rs);
    const f32x4 t = sum4(g);
    const f32x2 a = swap32(t.x, t.y), b = swap32(t.z, t.w);
    const unsigned p = pk(a.x + a.y, b.x + b.y);
    LAS u16* qv = (LAS u16*)(L + L_QVEC + w * 256 + (lane & 31) * 8 + (lane >> 5) * 2);
    qv[0] = (u16)p; qv[2] = (u16)(p >> 16);
    cbar(L, lane, bar);
#pragma unroll
    for (int s = 0; s < 4; ++s) qf[s] = *(const LAS v4u*)(L + L_QVEC + (lane & 7) * 256 + s * 64 + (lane >> 4) * 16);
  }
  float sc[4][8];
#pragma unroll
  for (int i = 0; i < 4; ++i) {
    v4u kf[8];
    if (i < 2) {
#pragma unroll
      for (int q = 0; q < 8; ++q) kf[q] = i == 0 ? tw[q] : tx[q];
    } else {
      const int n = offK + w + 8 * i;
      wait_landed(L, n);
      kfrags(L, n & 15, kl, kf);
      release(L, w, lane, i < 3 ? n + 8 : offV);
    }
#pragma unroll
    for (int T = 0; T < 2; ++T) {
      f32x4 a = {0.f, 0.f, 0.f, 0.f};
#pragma unroll
      for (int s = 0; s < 4; ++s) a = mfma(kf[T * 4 + s], qf[s], a);
      const f32x2 lo = rbf2(rbf2(f32x2{a.x, a.y}) * 0.08838834764831845f), hi = rbf2(rbf2(f32x2{a.z, a.w}) * 0.08838834764831845f);
      sc[i][T * 4] = lo.x; sc[i][T * 4 + 1] = lo.y; sc[i][T * 4 + 2] = hi.x; sc[i][T * 4 + 3] = hi.y;
    }
  }
  float mw = sc[0][0];
  {
#pragma unroll
    for (int i = 0; i < 4; ++i)
#pragma unroll
      for (int r = 0; r < 8; ++r) mw = fmaxf(mw, sc[i][r]);
    mw = max32(max16(mw));
    float lw = 0.f;
#pragma unroll
    for (int i = 0; i < 4; ++i)
#pragma unroll
      for (int r = 0; r < 8; ++r) { sc[i][r] = exp_(sc[i][r] - mw); lw += sc[i][r]; }
    lw = sum32(sum16(lw));
    if (lane < 8) *(LAS f32x2*)(L + L_MLW + w * 64 + lane * 8) = f32x2{mw, lw};
    lds_inc(L, L_MLCNT, lane);
  }
  float ga = 0.f, gb = 0.f, xres = 0.f, bias = 0.f;
  f32x4 sbv = {0.f, 0.f, 0.f, 0.f}, cb1 = {0.f, 0.f, 0.f, 0.f};
  if (isB) {
    v4u g[2];
    const int off[2] = {box(30 * 8 + (w >> 1) + (w & 1) * 8) + BX_T1 + lane * 16, box(30 * 8 + 4 + (w >> 1) + (w & 1) * 8) + BX_T1 + lane * 16};
    gather<2>(g, off, rs);
    sbv = asf(g[0]);
    const f32x4 cb = asf(g[1]);
    const f32x2 c0 = rbf2(f32x2{cb.x, cb.y} + 1.f), c1 = rbf2(f32x2{cb.z, cb.w} + 1.f);
    cb1 = f32x4{c0.x, c0.y, c1.x, c1.y};
    bias = lo2f(*(const GAS u16*)(b1 + (bi >> 2) * 128 + 16 * w + 4 * (piece & 3) + comp));
    PIN(bias); PIN(sbv); PIN(cb1);
  }
  if (isC) {
    const int col = 64 * ci + 16 * (w & 3) + 4 * (piece & 3) + comp;
    xres = lo2f(*(const GAS u16*)(x + (piece >> 2) * D + col));
    bias = lo2f(*(const GAS u16*)(b2 + col));
    lds_wait_ge(L, L_FGATE, 1);
    ga = *(const LAS float*)(L + L_GATE + (w & 3) * 256 + lane * 4);
    gb = *(const LAS float*)(L + L_GATE + (4 + (w & 3)) * 256 + lane * 4);
    PIN(ga); PIN(gb); PIN(xres); PIN(bias);
  }
#pragma unroll
  for (int q = 0; q < VPARK; ++q) {
    wait_landed(L, offV + q);
    if (q < 8) tw[q & 7] = tfrag(L, (offV + q) & 15, tl); else tx[q & 7] = tfrag(L, (offV + q) & 15, tl);
    release(L, w, lane, offV + q + 1);
  }
  lds_wait_ge(L, L_FML, 1);
  {
    const f32x2 ml = *(const LAS f32x2*)(L + L_MLF + (lane & 7) * 8);
    const float f = exp_(mw - ml.x) * ml.y;
#pragma unroll
    for (int i = 0; i < 4; ++i) {
      const v4u p = {pk(sc[i][0] * f, sc[i][1] * f), pk(sc[i][2] * f, sc[i][3] * f), pk(sc[i][4] * f, sc[i][5] * f), pk(sc[i][6] * f, sc[i][7] * f)};
      if ((lane & 15) < 8) *(LAS v4u*)(L + L_P + (w + 8 * i) * 512 + ((lane >> 4) * 8 + (lane & 7)) * 16) = p;
    }
    cbar(L, lane, bar);
  }
  f32x4 oacc = {0.f, 0.f, 0.f, 0.f};
  const int pl = L_P + ((lane >> 4) * 8 + (lane & 7)) * 16;
#pragma unroll
  for (int i = 0; i < VPARK; ++i) oacc = mfma(i < 8 ? tw[i & 7] : tx[i & 7], *(const LAS v4u*)(L + pl + i * 512), oacc);
#pragma unroll 1
  for (int i = VPARK; i < 32; i += 2) {
    const int n = offV + i;
    wait_landed(L, n + 1); wait_landed(L, n);
    const v4u a0 = tfrag(L, n & 15, tl), a1 = tfrag(L, (n + 1) & 15, tl);
    const v4u p0 = *(const LAS v4u*)(L + pl + i * 512), p1 = *(const LAS v4u*)(L + pl + (i + 1) * 512);
    release(L, w, lane, n + 2);
    oacc = mfma(a0, p0, oacc);
    oacc = mfma(a1, p1, oacc);
  }
  if ((lane & 15) < 8) __builtin_amdgcn_raw_buffer_store_b128(asu(oacc), rs, mybox + BX_O + w * 512 + ((lane >> 4) * 8 + (lane & 15)) * 16, 0, AUX_SC1);

  if (isA) {
    v4u g[4]; int off[4];
#pragma unroll
    for (int q = 0; q < 4; ++q) off[q] = box((sg + 8 * q) * 8 + (w >> 2) * 4 + a_hkv) + BX_O + a_dt * 512 + (w & 3) * 128 + piece * 16;
    gather_issue<4>(g, off, rs);
#pragma unroll
    for (int q = 0; q < 4; ++q) { wait_landed(L, offW + q); tw[q] = tfrag(L, (offW + q) & 15, tl); }
    gather_finish<4, 0>(g, off, rs);
    const float t = tred(sum4(g));
    if (!(lane & 8)) *(LAS u16*)(L + L_TVEC + (w >> 2) * 256 + (16 * piece + 4 * (w & 3) + comp) * 2) = (u16)pk(t, t);
    WGBAR();
    tile_pair(L, L_TVEC, wf, tw, rs, mybox + BX_T1 + w * 128, lane);
  } else if (isC) {
    if (w < 4) {
      const int ct = 4 * ci + w, cg = ct >> 4, ti = ct & 15;
      v4u g[4]; int off[4], off2[4];
#pragma unroll
      for (int q = 0; q < 4; ++q) {
        const int a = (sg + 8 * q) * 4 + cg;
        off[q] = box((a & 15) * 8 + (a >> 4)) + BX_T1 + ti * 128 + piece * 16;
        const int kf = sg + 8 * q, bb = kf * 4 + cg, gg = bb / 14;
        off2[q] = kf < 28 ? box((16 + bb - gg * 14) * 8 + gg) + BX_T2 + ti * 128 + piece * 16 : WS_BYTES;
        PIN(off2[q]);
      }
      gather<4>(g, off, rs);
      const float x1 = rbf(xres + rbf(ga * rbf(tred(sum4(g)))));
      const f32x4 x4 = texp(x1);
      if (lane < 8) __builtin_amdgcn_raw_buffer_store_b128(asu(x4), rs, mybox + BX_X1 + w * 128 + piece * 16, 0, AUX_SC1);
      gather<4>(g, off2, rs);
      const float o = x1 + rbf(gb * rbf(rbf(tred(sum4(g))) + bias));
      if (!(lane & 8)) *(GAS u16*)(out + (piece >> 2) * D + 64 * ci + 16 * w + 4 * (piece & 3) + comp) = (u16)pk(o, o);
    }
  } else {
#pragma unroll
    for (int q = 0; q < 8; ++q) { wait_landed(L, offW + q); tw[q] = tfrag(L, (offW + q) & 15, tl); }
    const int cc = 2 * w + (sg >> 2);
    const int offx[1] = {box((30 + (cc & 1)) * 8 + (cc >> 1)) + BX_X1 + (sg & 3) * 128 + piece * 16};
    const int kf = bi >> 2, bb = (kf >> 1) * 8 + sg, gg = bb / 14;
    int offh[1] = {box((16 + bb - gg * 14) * 8 + gg) + BX_T1 + ((kf & 1) * 8 + w) * 128 + piece * 16};
    PIN(offh[0]);
    wait_landed(L, offW + 14); wait_landed(L, offW + 15);
    v4u g[1];
    gather<1>(g, offx, rs);
    const f32x4 x1 = asf(g[0]);
    float m, rden;
    stat_put(L, L_ST1, w, lane, x1);
    WGBAR();
    stat_get(L, L_ST1, lane, m, rden);
    const f32x2 ha = rbf2(rbf2(ln2(f32x2{x1.x, x1.y}, m, rden) * f32x2{cb1.x, cb1.y}) + f32x2{sbv.x, sbv.y});
    const f32x2 hb = rbf2(rbf2(ln2(f32x2{x1.z, x1.w}, m, rden) * f32x2{cb1.z, cb1.w}) + f32x2{sbv.z, sbv.w});
    stat_put(L, L_ST2, w, lane, f32x4{ha.x, ha.y, hb.x, hb.y});
    WGBAR();
    if (w == (bi & 7)) {
      stat_get(L, L_ST2, lane, m, rden);
      const f32x2 ya = rbf2(ha - m) * rden, yb = rbf2(hb - m) * rden;
      *(LAS v2u*)(L + L_TVEC + (piece >> 2) * 256 + (16 * sg + 4 * (piece & 3)) * 2) = v2u{pk(ya.x, ya.y), pk(yb.x, yb.y)};
    }
    WGBAR();
    tile_pair(L, L_TVEC, tw, tw + 4, rs, mybox + BX_T1 + w * 128, lane);
    gather_issue<1>(g, offh, rs);
#pragma unroll
    for (int q = 0; q < 8; ++q) { wait_landed(L, offW + 8 + q); tw[q] = tfrag(L, (offW + 8 + q) & 15, tl); }
    gather_finish<1, 0>(g, offh, rs);
    const float u = rbf(rbf(tred(asf(g[0]))) + bias);
    const float z = u * (0.7978845608028654f + 0.035677408136300125f * u * u);
    const float hh = u * rcp_(1.f + __builtin_amdgcn_exp2f(-2.8853900817779268f * z));
    if (!(lane & 8)) *(LAS u16*)(L + L_UVEC + (piece >> 2) * 256 + (16 * w + 4 * (piece & 3) + comp) * 2) = (u16)pk(hh, hh);
    WGBAR();
    tile_pair(L, L_UVEC, tw, tw + 4, rs, mybox + BX_T2 + w * 128, lane);
  }
}

}

extern "C" void kernel_launch(void* const* d_in, const int* in_sizes, int n_in, void* d_out, int out_size, void* d_ws, size_t ws_size,
                              hipStream_t stream) {
  (void)in_sizes; (void)n_in; (void)out_size; (void)ws_size;
  (void)hipFuncSetAttribute((const void*)fused_block, hipFuncAttributeMaxDynamicSharedMemorySize, LDS_BYTES);
  hipLaunchKernelGGL(fused_block, dim3(256), dim3(NTHR), LDS_BYTES, stream, (const u16*)d_in[0], (const u16*)d_in[1], (const u16*)d_in[2],
                     (const u16*)d_in[3], (const u16*)d_in[4], (const u16*)d_in[6], (const u16*)d_in[7], (const u16*)d_in[8],
                     (const u16*)d_in[9], (const u16*)d_in[10], (const u16*)d_in[11], (u16*)d_out, (unsigned char*)d_ws);
}
```

```cpp
#include <hip/hip_runtime.h>
#include <hip/hip_bf16.h>
#include <stdint.h>

#define LAS __attribute__((address_space(3)))
#define GAS __attribute__((address_space(1)))
typedef unsigned short u16;
typedef unsigned v4u __attribute__((ext_vector_type(4)));
typedef unsigned v2u __attribute__((ext_vector_type(2)));
typedef float f32x4 __attribute__((ext_vector_type(4)));
typedef float f32x2 __attribute__((ext_vector_type(2)));
typedef short s16x4 __attribute__((ext_vector_type(4)));
typedef short bf16x8 __attribute__((ext_vector_type(8)));
typedef __bf16 bf16x2 __attribute__((ext_vector_type(2)));

namespace {

constexpr int D = 1024, SK = 32768;
constexpr int NTHR = 704;
constexpr int W_PRO = 18, W_KV = 12, W_WO = 16, W_MLP = 10;
constexpr int VPARK = 12;

constexpr int WS_EPOCH = 0, WS_ALIVE = 4096, WS_BOX = 65536, BOX = 16384;
constexpr int BX_MC = 0;
constexpr int BX_Q = 1024;
constexpr int BX_ML = 2048;
constexpr int BX_MR = 3072;
constexpr int BX_O = 6144;
constexpr int BX_T1 = 10240;
constexpr int BX_X1 = BX_T1 + 1024;
constexpr int BX_T2 = 12288;
constexpr int WS_BYTES = WS_BOX + 256 * BOX;
constexpr unsigned SENT = 0xFFFFFFFFu;
constexpr unsigned SPIN_CAP = 1u << 15, LDS_CAP = 1u << 20;
constexpr int AUX_SC1 = 16;

constexpr int L_RING = 0, L_P = 131072, L_CTL = L_P + 16384;
constexpr int L_X = L_P, L_C = L_P + 4096, L_STG = L_P + 8192;
constexpr int L_LANDED = L_CTL;
constexpr int L_PROG = L_CTL + 32;
constexpr int L_BAR = L_CTL + 64, L_MLCNT = L_CTL + 68, L_FALIVE = L_CTL + 72, L_FML = L_CTL + 76, L_FAUX = L_CTL + 80, L_FGATE = L_CTL + 88;
constexpr int L_SVEC = L_CTL + 256;
constexpr int VROW = 320;
constexpr int L_HVEC = L_SVEC + 4096;
constexpr int L_QVEC = L_HVEC + 640;
constexpr int L_ST1 = L_QVEC + 2048;
constexpr int L_ST2 = L_ST1 + 128;
constexpr int L_MLW = L_ST2 + 128;
constexpr int L_MLF = L_MLW + 512;
constexpr int L_TVEC = L_MLF + 64;
constexpr int L_UVEC = L_TVEC + 640;
constexpr int L_SBST = L_TVEC;
constexpr int L_GATE = L_UVEC + 640;
constexpr int LDS_BYTES = L_GATE + 2048;
static_assert(LDS_BYTES <= 160 * 1024 && LDS_BYTES > 80 * 1024, "one workgroup per CU");

typedef LAS unsigned char* lptr;

__device__ __forceinline__ float lo2f(unsigned w) { return __uint_as_float(w << 16); }
__device__ __forceinline__ float hi2f(unsigned w) { return __uint_as_float(w & 0xffff0000u); }
__device__ __forceinline__ unsigned pk(float a, float b) {
  bf16x2 v = {(__bf16)a, (__bf16)b};
  return __builtin_bit_cast(unsigned, v);
}
__device__ __forceinline__ float rbf(float a) { return hi2f(pk(a, a)); }
__device__ __forceinline__ f32x2 rbf2(f32x2 a) {
  unsigned p = pk(a.x, a.y);
  asm("" : "+v"(p));
  return f32x2{lo2f(p), hi2f(p)};
}
__device__ __forceinline__ f32x4 mfma(v4u a, v4u b, f32x4 c) {
  return __builtin_amdgcn_mfma_f32_16x16x32_bf16(__builtin_bit_cast(bf16x8, a), __builtin_bit_cast(bf16x8, b), c, 0, 0, 0);
}
template <int CTRL> __device__ __forceinline__ float dpp(float x) {
  return __builtin_bit_cast(float, __builtin_amdgcn_mov_dpp(__builtin_bit_cast(int, x), CTRL, 0xf, 0xf, true));
}
constexpr int XOR1 = 0xB1, XOR2 = 0x4E, ROR4 = 0x124, ROR8 = 0x128;
__device__ __forceinline__ f32x2 swap32(float a, float b) {
  auto s = __builtin_amdgcn_permlane32_swap(__float_as_uint(a), __float_as_uint(b), false, false);
  return f32x2{__uint_as_float(s[0]), __uint_as_float(s[1])};
}
__device__ __forceinline__ f32x2 swap16(float a, float b) {
  auto s = __builtin_amdgcn_permlane16_swap(__float_as_uint(a), __float_as_uint(b), false, false);
  return f32x2{__uint_as_float(s[0]), __uint_as_float(s[1])};
}
__device__ __forceinline__ float sum16(float x) { const f32x2 s = swap16(x, x); return s.x + s.y; }
__device__ __forceinline__ float sum32(float x) { const f32x2 s = swap32(x, x); return s.x + s.y; }
__device__ __forceinline__ float max16(float x) { const f32x2 s = swap16(x, x); return fmaxf(s.x, s.y); }
__device__ __forceinline__ float max32(float x) { const f32x2 s = swap32(x, x); return fmaxf(s.x, s.y); }
__device__ __forceinline__ float tred(f32x4 v) {
  const f32x2 a = swap32(v.x, v.y), b = swap32(v.z, v.w);
  const f32x2 c = swap16(a.x + a.y, b.x + b.y);
  const float t = c.x + c.y;
  return t + dpp<ROR8>(t);
}
__device__ __forceinline__ int tcomp(int lane) { return ((lane >> 4) & 1) * 2 + (lane >> 5); }
__device__ __forceinline__ f32x4 texp(float t) {
  const f32x2 a = swap16(t, t);
  const f32x2 b = swap32(a.x, a.x), c = swap32(a.y, a.y);
  return f32x4{b.x, b.y, c.x, c.y};
}
__device__ __forceinline__ f32x4 asf(v4u v) { return __builtin_bit_cast(f32x4, v); }
__device__ __forceinline__ v4u asu(f32x4 v) { return __builtin_bit_cast(v4u, v); }
__device__ __forceinline__ unsigned umax4(v4u v) { return max(max(max(v.x, v.y), v.z), v.w); }
__device__ __forceinline__ float exp_(float x) { return __builtin_amdgcn_exp2f(x * 1.4426950408889634f); }
__device__ __forceinline__ float rcp_(float x) { return __builtin_amdgcn_rcpf(x); }

#define LGKM0() asm volatile("s_waitcnt lgkmcnt(0)" ::: "memory")
#define VM0() asm volatile("s_waitcnt vmcnt(0)" ::: "memory")
#define CFENCE() asm volatile("" ::: "memory")
#define PIN(x) asm volatile("" : "+v"(x))
#define WGBAR() do { LGKM0(); __builtin_amdgcn_s_barrier(); CFENCE(); } while (0)

__device__ __forceinline__ void dma16(unsigned voff, const unsigned char* sbase, unsigned ldsaddr) {
  unsigned keep;
  asm volatile("s_mov_b32 %0, m0\n\ts_mov_b32 m0, %3\n\ts_nop 4\n\tglobal_load_lds_dwordx4 %1, %2 nt\n\ts_mov_b32 m0, %0"
               : "=&s"(keep) : "v"(voff), "s"(sbase), "s"(ldsaddr) : "memory");
}
__device__ __forceinline__ void lds_set(lptr L, int off, int lane, unsigned v) {
  if (lane == 0) *(volatile LAS unsigned*)(L + off) = v;
}
__device__ __forceinline__ void lds_wait_ge(lptr L, int off, unsigned need) {
#pragma nounroll
  for (unsigned sp = 0; (unsigned)__builtin_amdgcn_readfirstlane(*(volatile LAS unsigned*)(L + off)) < need && sp < LDS_CAP; ++sp) { }
  CFENCE();
}
__device__ __forceinline__ void wait_landed(lptr L, int n) { lds_wait_ge(L, L_LANDED + 4 * (n & 1), (unsigned)(n >> 1) + 1u); }
__device__ __forceinline__ void release(lptr L, int w, int lane, int n) { LGKM0(); lds_set(L, L_PROG + 4 * w, lane, (unsigned)n); }
__device__ __forceinline__ void lds_inc(lptr L, int off, int lane) {
  LGKM0();
  if (lane == 0) __hip_atomic_fetch_add((LAS unsigned*)(L + off), 1u, __ATOMIC_RELAXED, __HIP_MEMORY_SCOPE_WORKGROUP);
}
__device__ __forceinline__ void cbar(lptr L, int lane, unsigned& target) {
  target += 8;
  lds_inc(L, L_BAR, lane);
  lds_wait_ge(L, L_BAR, target);
}
__device__ __forceinline__ v4u tfrag(lptr L, int slot, int tl) {
  const s16x4 a = __builtin_amdgcn_ds_read_tr16_b64_v4i16((LAS s16x4*)(L + L_RING + slot * 8192 + tl));
  const s16x4 b = __builtin_amdgcn_ds_read_tr16_b64_v4i16((LAS s16x4*)(L + L_RING + slot * 8192 + tl + 1024));
  const v2u x = __builtin_bit_cast(v2u, a), y = __builtin_bit_cast(v2u, b);
  return v4u{x.x, x.y, y.x, y.y};
}
__device__ __forceinline__ void kfrags(lptr L, int slot, int kl, v4u (&f)[8]) {
#pragma unroll
  for (int q = 0; q < 8; ++q) f[q] = *(const LAS v4u*)(L + L_RING + slot * 8192 + (q >> 2) * 1024 + (kl ^ ((q & 3) << 6)));
}
__device__ __forceinline__ v4u sfrag(lptr L, int lane, int k0) {
  return *(const LAS v4u*)(L + L_SVEC + (lane & 1) * 2048 + k0 * 2 + (lane >> 4) * 16);
}
__device__ __forceinline__ v4u vfrag(lptr L, int off, int lane, int s) {
  return *(const LAS v4u*)(L + off + (lane & 1) * VROW + s * 64 + (lane >> 4) * 16);
}
template <int N>
__device__ __forceinline__ void gather_issue(v4u (&v)[N], const int (&off)[N], __amdgpu_buffer_rsrc_t rs) {
#pragma unroll
  for (int q = 0; q < N; ++q) v[q] = __builtin_amdgcn_raw_buffer_load_b128(rs, off[q], 0, AUX_SC1);
}
template <int N, int PAUSE>
__device__ __forceinline__ void gather_finish(v4u (&v)[N], const int (&off)[N], __amdgpu_buffer_rsrc_t rs) {
#pragma nounroll
  for (unsigned sp = 0; sp < SPIN_CAP; ++sp) {
    unsigned mx = umax4(v[0]);
#pragma unroll
    for (int q = 1; q < N; ++q) mx = max(mx, umax4(v[q]));
    if (__builtin_amdgcn_ballot_w64(mx == SENT) == 0) break;
    if (PAUSE) __builtin_amdgcn_s_sleep(PAUSE);
    CFENCE();
    if (mx == SENT) gather_issue<N>(v, off, rs);
  }
}
template <int N, int PAUSE = 0>
__device__ __forceinline__ void gather(v4u (&v)[N], const int (&off)[N], __amdgpu_buffer_rsrc_t rs) {
  gather_issue<N>(v, off, rs);
  gather_finish<N, PAUSE>(v, off, rs);
}
__device__ __forceinline__ f32x4 sum4(const v4u (&g)[4]) { return (asf(g[0]) + asf(g[1])) + (asf(g[2]) + asf(g[3])); }
__device__ __forceinline__ void tile_pair(lptr L, int vec, const v4u* f0, const v4u* f1, __amdgpu_buffer_rsrc_t rs, int off, int lane) {
  f32x4 a0 = {0.f, 0.f, 0.f, 0.f}, a1 = {0.f, 0.f, 0.f, 0.f};
#pragma unroll
  for (int s = 0; s < 4; ++s) { const v4u bv = vfrag(L, vec, lane, s); a0 = mfma(f0[s], bv, a0); a1 = mfma(f1[s], bv, a1); }
  if ((lane & 15) < 2) {
    __builtin_amdgcn_raw_buffer_store_b128(asu(a0), rs, off + (lane & 1) * 64 + (lane >> 4) * 16, 0, AUX_SC1);
    __builtin_amdgcn_raw_buffer_store_b128(asu(a1), rs, off + 1024 + (lane & 1) * 64 + (lane >> 4) * 16, 0, AUX_SC1);
  }
}
__device__ __forceinline__ int box(int j) { return WS_BOX + j * BOX; }
__device__ __forceinline__ void ln_consts(float s, float q, float& m, float& rden) {
  const float mean = s * (1.f / D);
  const float var = fmaxf(q * (1.f / D) - mean * mean, 0.f);
  m = rbf(mean);
  rden = rcp_(rbf(__builtin_amdgcn_sqrtf(rbf(rbf(var) + 1e-5f))));
}
__device__ __forceinline__ f32x2 ln2(f32x2 x, float m, float rden) { return rbf2(rbf2(x - m) * rden); }
__device__ __forceinline__ void stat_put(lptr L, int off, int w, int lane, f32x4 v) {
  const float s = (v.x + v.y) + (v.z + v.w), q = (v.x * v.x + v.y * v.y) + (v.z * v.z + v.w * v.w);
  const f32x2 a = swap32(s, q);
  float r = sum16(a.x + a.y);
  r += dpp<ROR8>(r); r += dpp<XOR2>(r); r += dpp<XOR1>(r);
  if ((lane & 0x1B) == 0) *(LAS float*)(L + off + w * 16 + (lane & 4) * 2 + (lane >> 5) * 4) = r;
}
__device__ __forceinline__ void stat_get(lptr L, int off, int lane, float& m, float& rden) {
  const f32x2 p = *(const LAS f32x2*)(L + off + (lane >> 3) * 16 + (lane & 4) * 2);
  const f32x2 a = swap32(p.x, p.y);
  float r = sum16(a.x + a.y);
  r += dpp<ROR8>(r);
  const f32x2 t = swap32(r, r);
  ln_consts(t.x, t.y, m, rden);
}

__device__ __forceinline__ int read_minprog(lptr L, int lane) {
  unsigned m = *(volatile LAS unsigned*)(L + L_PROG + 4 * (lane & 7));
  m = min(m, (unsigned)__builtin_amdgcn_mov_dpp((int)m, XOR1, 0xf, 0xf, true));
  m = min(m, (unsigned)__builtin_amdgcn_mov_dpp((int)m, XOR2, 0xf, 0xf, true));
  m = min(m, (unsigned)__builtin_amdgcn_mov_dpp((int)m, ROR4, 0xf, 0xf, true));
  return __builtin_amdgcn_readfirstlane((int)m);
}
template <int W>
__device__ __forceinline__ void load_block(lptr L, int ld, int lane, int n, const unsigned char* bp, int ldb, int hi, int& minprog) {
  if (minprog < n - 15) {
    minprog = read_minprog(L, lane);
    if (minprog < n - 15) {
      VM0();
      lds_set(L, L_LANDED + 4 * ld, lane, (unsigned)(n >> 1));
      for (unsigned sp = 0; sp < LDS_CAP; ++sp) {
        minprog = read_minprog(L, lane);
        if (minprog >= n - 15) break;
      }
    }
  }
  const unsigned voff = (unsigned)((lane >> 4) * ldb + (((lane & 15) ^ ((lane >> 4) << 2)) << 4));
  const unsigned dst = (unsigned)(size_t)L + L_RING + (unsigned)(n & 15) * 8192u;
#pragma unroll
  for (int p = 0; p < 8; ++p) {
    dma16(voff, bp + (size_t)((p & 3) * 4) * ldb + (size_t)(p >> 2) * hi, dst + p * 1024);
    asm volatile("s_waitcnt vmcnt(%0)" ::"i"(W - 1) : "memory");
    if (((p + 2 - W) & 7) == 0) {
      const int done = (n >> 1) + (p + 2 - W) / 8;
      if (done > 0) lds_set(L, L_LANDED + 4 * ld, lane, (unsigned)done);
    }
  }
}

__global__ void __launch_bounds__(NTHR) fused_block(const u16* __restrict__ x, const u16* __restrict__ kc, const u16* __restrict__ vc,
                                                     const u16* __restrict__ wq, const u16* __restrict__ wo, const u16* __restrict__ w1,
                                                     const u16* __restrict__ b1, const u16* __restrict__ w2, const u16* __restrict__ b2,
                                                     const u16* __restrict__ cvec, const u16* __restrict__ wmod, u16* __restrict__ out,
                                                     unsigned char* ws) {
  extern __shared__ __attribute__((aligned(16))) unsigned char smem[];
  lptr L = (lptr)smem;
  const int tid = threadIdx.x, lane = tid & 63, w = __builtin_amdgcn_readfirstlane(tid >> 6);
  const int j = blockIdx.x, grp = j & 7, split = j >> 3, batt = grp >> 2, hkv = grp & 3;
  const bool isA = split < 16, isC = split >= 30, isB = !isA && !isC;
  const int kq = split & 7, g32 = grp * 4 + (split >> 3), g16 = grp * 2 + ((split >> 3) & 1);
  const int mu0 = isA ? 2 * (grp * 16 + split) : 256 + 2 * (grp * 16 + split - 16);
  const int offQ = isA ? 0 : 4, offMR = offQ + 4;
  constexpr int offWO = 8, offK = 12, offV = offK + 32, offW = offV + 32;
  const int ai = grp * 16 + split, a_hkv = ai >> 5, a_dt = (ai >> 2) & 7, a_cg = ai & 3;
  const int bi = grp * 14 + (split - 16), ci = grp * 2 + (split - 30);

  if (tid < 64) *(volatile LAS unsigned*)(L + L_CTL + 4 * tid) = 0u;
  WGBAR();
  unsigned long long epoch = 0, tag = 0;
  if (w == 10) {
    const __amdgpu_buffer_rsrc_t rsh = __builtin_amdgcn_make_buffer_rsrc((void*)ws, 0, WS_BYTES, 0x00020000);
    const v2u ep = __builtin_amdgcn_raw_buffer_load_b64(rsh, WS_EPOCH, 0, AUX_SC1);
    const v4u s4 = {SENT, SENT, SENT, SENT};
    const unsigned used = isA ? 0x0FDEu : (isB ? 0x3FDFu : 0x7FDFu);
#pragma unroll 1
    for (int u = 0; u < 15; ++u)
      if ((used >> u) & 1) __builtin_amdgcn_raw_buffer_store_b128(s4, rsh, box(j) + u * 1024 + lane * 16, 0, AUX_SC1);
    VM0();
    epoch = ((unsigned long long)(unsigned)__builtin_amdgcn_readfirstlane((int)ep.y) << 32) | (unsigned)__builtin_amdgcn_readfirstlane((int)ep.x);
    tag = (epoch ^ 0x9E3779B97F4A7C15ull) * 0xD6E8FEB86659FD93ull + 1ull;
    if (lane == 0) __hip_atomic_store((GAS unsigned long long*)(ws + WS_ALIVE + j * 16), tag, __ATOMIC_RELAXED, __HIP_MEMORY_SCOPE_AGENT);
  }
  if (__builtin_expect(w == 8 || w == 9, 1)) {
    const int ld = w - 8;
    __builtin_amdgcn_s_setprio(3);
    int minprog = 0;
    const unsigned char* aux = (const unsigned char*)(ld ? cvec : x);
#pragma unroll
    for (int p = 0; p < 4; ++p) dma16((unsigned)lane * 16u, aux + p * 1024, (unsigned)(size_t)L + L_X + ld * 4096 + p * 1024);
    const size_t kvoff = ((size_t)grp * SK + split * 1024) * 256;
    const unsigned char* wo_t = (const unsigned char*)wo + ((size_t)(a_hkv * 8) * 128 + 16 * a_dt) * 2048 + a_cg * 512;
#pragma unroll 1
    for (int n = ld; n < offW; n += 2) {
      if (n < offK) {
        const unsigned char* bp; int ldb, hi;
        if (isA && n >= offWO) { bp = wo_t + (size_t)(n - offWO) * 256 * 2048; ldb = 2048; hi = 128 * 2048; }
        else {
          if (n < offQ) { bp = (const unsigned char*)wmod + (size_t)kq * 128 * 12288 + g16 * 256; ldb = 12288; }
          else if (n < offMR) { bp = (const unsigned char*)wq + (size_t)kq * 128 * 8192 + g32 * 256; ldb = 8192; }
          else { const int u = mu0 + ((n - offMR) >> 1); bp = (const unsigned char*)wmod + (size_t)(u & 15) * 64 * 12288 + 4096 + (u >> 4) * 256; ldb = 12288; }
          bp += (size_t)((n < offMR ? n & 3 : (n - offMR) & 1) * 32) * ldb; hi = 16 * ldb;
        }
        load_block<W_PRO>(L, ld, lane, n, bp, ldb, hi, minprog);
        if (n < 2) {
          asm volatile("s_waitcnt vmcnt(8)" ::: "memory");
          lds_set(L, L_FAUX + 4 * ld, lane, 1u);
        }
      } else {
        const unsigned char* bp = n < offV ? (const unsigned char*)kc + kvoff + (size_t)(n - offK) * 8192 : (const unsigned char*)vc + kvoff + (size_t)(n - offV) * 8192;
        load_block<W_KV>(L, ld, lane, n, bp, 256, 4096, minprog);
      }
    }
    int nend = offW;
    if (isA) {
      nend = offW + 4;
#pragma unroll 1
      for (int n = offW + ld; n < nend; n += 2) load_block<W_WO>(L, ld, lane, n, wo_t + (size_t)(n - offW) * 256 * 2048 + 256, 2048, 128 * 2048, minprog);
    }
    if (isB) {
      nend = offW + 16;
#pragma unroll 1
      for (int n = offW + ld; n < nend; n += 2) {
        const int q = n - offW;
        const unsigned char* bp; int ldb;
        if (q < 8) { bp = (const unsigned char*)w1 + (size_t)(bi & 7) * 128 * 7168 + (bi >> 3) * 512; ldb = 7168; }
        else { bp = (const unsigned char*)w2 + (size_t)(bi >> 2) * 128 * 2048 + (bi & 3) * 512; ldb = 2048; }
        load_block<W_MLP>(L, ld, lane, n, bp + (size_t)((q & 3) * 32) * ldb + ((q >> 2) & 1) * 256, ldb, 16 * ldb, minprog);
      }
    }
    VM0();
    lds_set(L, L_LANDED + 4 * ld, lane, (unsigned)(nend >> 1));
    return;
  }

  const __amdgpu_buffer_rsrc_t rs = __builtin_amdgcn_make_buffer_rsrc((void*)ws, 0, WS_BYTES, 0x00020000);
  const int mybox = box(j);
  const int piece = lane & 7, sg = lane >> 3, comp = tcomp(lane);

  if (w == 10) {
    for (int i = 0; i < 3; ++i) __builtin_amdgcn_s_sleep(24);
    for (unsigned sp = 0; sp < SPIN_CAP; ++sp) {
      bool ok = true;
#pragma unroll
      for (int q = 0; q < 4; ++q) {
        const v2u t = __builtin_amdgcn_raw_buffer_load_b64(rs, WS_ALIVE + (q * 64 + lane) * 16, 0, AUX_SC1);
        ok = ok && t.x == (unsigned)tag && t.y == (unsigned)(tag >> 32);
      }
      if (__all(ok)) break;
      __builtin_amdgcn_s_sleep(4);
      CFENCE();
    }
    lds_set(L, L_FALIVE, lane, 1u);
    if (j == 0 && lane == 0) __hip_atomic_store((GAS unsigned long long*)(ws + WS_EPOCH), epoch + 1ull, __ATOMIC_RELAXED, __HIP_MEMORY_SCOPE_AGENT);
    if (isC) {
      lds_wait_ge(L, L_LANDED, (unsigned)(offK + 8) >> 1);
      auto mr = [&](int cg, int kh, int line) {
        const int u = cg * 16 + kh;
        const int ia = (u >> 1) & 127, jj = ((u < 256 ? 0 : 16) + (ia & 15)) * 8 + (ia >> 4);
        return box(jj) + BX_MR + (u & 1) * 1024 + line * 128 + piece * 16;
      };
#pragma unroll 1
      for (int h4 = 0; h4 < 4; ++h4) {
        const int cg = h4 < 2 ? 8 + ci : (h4 == 2 ? 0 : 24) + (ci >> 1), l0 = h4 < 2 ? 4 * h4 : (ci & 1) * 4;
        const int o0 = mr(cg, sg, l0), o1 = mr(cg, sg + 8, l0);
        v4u g[8];
        const int off[8] = {o0, o1, o0 + 128, o1 + 128, o0 + 256, o1 + 256, o0 + 384, o1 + 384};
        gather<8, 8>(g, off, rs);
#pragma unroll
        for (int q = 0; q < 4; ++q) {
          const float t = rbf(tred(asf(g[2 * q]) + asf(g[2 * q + 1])));
          if (h4 < 2) *(LAS float*)(L + L_SBST + (l0 + q) * 128 + piece * 16 + comp * 4) = t;
          else *(LAS float*)(L + L_GATE + ((h4 - 2) * 4 + q) * 256 + lane * 4) = t;
        }
      }
      LGKM0();
      __builtin_amdgcn_raw_buffer_store_b128(*(const LAS v4u*)(L + L_SBST + lane * 16), rs, mybox + BX_T1 + lane * 16, 0, AUX_SC1);
      lds_set(L, L_FGATE, lane, 1u);
    }
    lds_wait_ge(L, L_MLCNT, 8);
    const f32x2 r = *(const LAS f32x2*)(L + L_MLW + sg * 64 + piece * 8);
    float M = r.x;
    M = fmaxf(M, dpp<ROR8>(M)); M = max32(max16(M));
    float Ls = r.y * exp_(r.x - M);
    Ls += dpp<ROR8>(Ls); Ls = sum32(sum16(Ls));
    const float M1 = dpp<XOR1>(M), L1 = dpp<XOR1>(Ls);
    if (lane < 8 && !(lane & 1)) __builtin_amdgcn_raw_buffer_store_b128(asu(f32x4{M, Ls, M1, L1}), rs, mybox + BX_ML + lane * 8, 0, AUX_SC1);
    for (int i = 0; i < 2; ++i) __builtin_amdgcn_s_sleep(32);
    v4u g[2];
    const int off[2] = {box((lane >> 2) * 8 + grp) + BX_ML + (lane & 3) * 16, box(((lane >> 2) + 16) * 8 + grp) + BX_ML + (lane & 3) * 16};
    gather<2, 16>(g, off, rs);
    const f32x4 u = asf(g[0]), v = asf(g[1]);
    float M0 = fmaxf(u.x, v.x), Mb = fmaxf(u.z, v.z);
    M0 = fmaxf(M0, dpp<ROR4>(M0)); M0 = fmaxf(M0, dpp<ROR8>(M0)); M0 = max32(max16(M0));
    Mb = fmaxf(Mb, dpp<ROR4>(Mb)); Mb = fmaxf(Mb, dpp<ROR8>(Mb)); Mb = max32(max16(Mb));
    float L0 = u.y * exp_(u.x - M0) + v.y * exp_(v.x - M0), Lb = u.w * exp_(u.z - Mb) + v.w * exp_(v.z - Mb);
    L0 += dpp<ROR4>(L0); L0 += dpp<ROR8>(L0); L0 = sum32(sum16(L0));
    Lb += dpp<ROR4>(Lb); Lb += dpp<ROR8>(Lb); Lb = sum32(sum16(Lb));
    if (lane < 4) *(LAS f32x4*)(L + L_MLF + lane * 16) = f32x4{M0, 1.f / L0, Mb, 1.f / Lb};
    LGKM0();
    lds_set(L, L_FML, lane, 1u);
    return;
  }

  unsigned warm = 0;
  if (w == 0) {
    const unsigned char* p = (const unsigned char*)ws; size_t o = 0;
    const size_t kv0 = ((size_t)grp * SK + split * 1024) * 256;
    const int l = lane;
    if (l < 2) { p = (const unsigned char*)kc; o = kv0 + l * 131072; }
    else if (l < 4) { p = (const unsigned char*)vc; o = kv0 + (l - 2) * 131072; }
    else if (l < 8) { p = (const unsigned char*)wq; o = (size_t)kq * 128 * 8192 + (l - 4) * 262144 + g32 * 256; }
    else if (l < 14) { p = (const unsigned char*)wmod; o = (size_t)kq * 128 * 12288 + (l - 8) * 262144; }
    else if (l < 20) { const int u = mu0 + (l - 14) / 3; p = (const unsigned char*)wmod; o = (size_t)(u & 15) * 64 * 12288 + ((l - 14) % 3) * 262144 + 4096; }
    else if (l < 28) {
      if (isA) { p = (const unsigned char*)wo; o = (size_t)(a_hkv * 8) * 128 * 2048 + (l - 20) * 262144; }
      else if (isB && l < 24) { p = (const unsigned char*)w1; o = (size_t)(bi & 7) * 128 * 7168 + (l - 20) * 229376; }
      else if (isB) { p = (const unsigned char*)w2; o = (size_t)(bi >> 2) * 128 * 2048; }
    }
    else if (l < 45) o = (size_t)(l - 28) * 262144;
    else if (l == 45) p = (const unsigned char*)b1;
    else if (l == 46) p = (const unsigned char*)b2;
    asm volatile("global_load_dword %0, %1, off" : "=&v"(warm) : "v"(p + o) : "memory");
  }
  unsigned bar = 0;
  lds_wait_ge(L, L_FAUX, 1); lds_wait_ge(L, L_FAUX + 4, 1);
  asm volatile("s_waitcnt vmcnt(0)" : "+v"(warm) :: "memory");
  const unsigned xh = *(const LAS u16*)(L + L_X + ((piece >> 2) * D + kq * 128 + 16 * w + 4 * (piece & 3) + comp) * 2);
  {
    const v2u cr = *(const LAS v2u*)(L + L_C + tid * 8);
    float s[4] = {lo2f(cr.x), hi2f(cr.x), lo2f(cr.y), hi2f(cr.y)};
#pragma unroll
    for (int i = 0; i < 4; ++i) s[i] = s[i] * rcp_(1.f + exp_(-s[i]));
    *(LAS v2u*)(L + L_SVEC + tid * 8) = v2u{pk(s[0], s[1]), pk(s[2], s[3])};
  }
  {
    const v2u xr = *(const LAS v2u*)(L + L_X + tid * 8);
    const float a0 = lo2f(xr.x), a1 = hi2f(xr.x), a2 = lo2f(xr.y), a3 = hi2f(xr.y);
    const f32x2 sq = swap32((a0 + a1) + (a2 + a3), (a0 * a0 + a1 * a1) + (a2 * a2 + a3 * a3));
    float r = sum16(sq.x + sq.y);
    r += dpp<ROR8>(r); r += dpp<ROR4>(r); r += dpp<XOR2>(r); r += dpp<XOR1>(r);
    if ((lane & 31) == 0) *(LAS float*)(L + L_ST1 + w * 8 + (lane >> 5) * 4) = r;
  }
  cbar(L, lane, bar);
  float lnx;
  {
    const int b = piece >> 2;
    const f32x4 r0 = *(const LAS f32x4*)(L + L_ST1 + b * 32), r1 = *(const LAS f32x4*)(L + L_ST1 + b * 32 + 16);
    float m, rden;
    ln_consts((r0.x + r0.z) + (r1.x + r1.z), (r0.y + r0.w) + (r1.y + r1.w), m, rden);
    lnx = rbf(rbf(lo2f(xh) - m) * rden);
    PIN(lnx);
  }
  const int tl = (8 * (lane >> 4) + ((lane >> 2) & 3)) * 256 + (((2 * w + ((lane & 3) >> 1)) ^ (((lane >> 2) & 3) << 2)) << 4) + (lane & 1) * 8;
  const int pubo = w * 128 + (lane & 15) * 64 + (lane >> 4) * 16;

  if (!isA) {
    f32x4 acc = {0.f, 0.f, 0.f, 0.f};
#pragma unroll
    for (int s = 0; s < 4; ++s) { wait_landed(L, s); acc = mfma(tfrag(L, s, tl), sfrag(L, lane, kq * 128 + 32 * s), acc); }
    release(L, w, lane, 4);
    if ((lane & 15) < 2) *(LAS f32x4*)(L + L_STG + pubo) = acc;
    cbar(L, lane, bar);
    if (w == 0) __builtin_amdgcn_raw_buffer_store_b128(*(const LAS v4u*)(L + L_STG + lane * 16), rs, mybox + BX_MC + lane * 16, 0, AUX_SC1);
  }
  v4u qw[4];
#pragma unroll
  for (int s = 0; s < 4; ++s) { wait_landed(L, offQ + s); qw[s] = tfrag(L, (offQ + s) & 15, tl); }
  release(L, w, lane, offMR);
#pragma unroll 1
  for (int u = 0; u < 2; ++u) {
    const int n = offMR + 2 * u, k0 = ((mu0 + u) & 15) * 64;
    wait_landed(L, n); wait_landed(L, n + 1);
    f32x4 acc = mfma(tfrag(L, n & 15, tl), sfrag(L, lane, k0), f32x4{0.f, 0.f, 0.f, 0.f});
    acc = mfma(tfrag(L, (n + 1) & 15, tl), sfrag(L, lane, k0 + 32), acc);
    release(L, w, lane, n + 2 < offK ? n + 2 : offK + w);
    if ((lane & 15) < 2) *(LAS f32x4*)(L + L_STG + 2048 + u * 1024 + pubo) = acc;
  }
  v4u wf[4];
  if (isA) {
#pragma unroll
    for (int q = 0; q < 4; ++q) { wait_landed(L, offWO + q); wf[q] = tfrag(L, offWO + q, tl); }
    release(L, w, lane, offK + w);
  }
  {
    lds_wait_ge(L, L_FALIVE, 1);
    v4u g[2];
    const int js = (16 + (kq & 1) * 8 + sg) * 8 + (kq >> 1);
    const int off[2] = {box(js) + BX_MC + w * 128 + piece * 16, box(js + 4) + BX_MC + w * 128 + piece * 16};
    gather<2>(g, off, rs);
    const float sa = rbf(tred(asf(g[0]))), ca = rbf(tred(asf(g[1])));
    const float h = rbf(lnx * rbf(1.f + ca)) + sa;
    if (!(lane & 8)) *(LAS u16*)(L + L_HVEC + (piece >> 2) * VROW + (16 * w + 4 * (piece & 3) + comp) * 2) = (u16)pk(h, h);
    cbar(L, lane, bar);
    if (w >= 1 && w <= 2)
      __builtin_amdgcn_raw_buffer_store_b128(*(const LAS v4u*)(L + L_STG + 1024 + w * 1024 + lane * 16), rs, mybox + BX_MR + (w - 1) * 1024 + lane * 16, 0, AUX_SC1);
    f32x4 acc = {0.f, 0.f, 0.f, 0.f};
#pragma unroll
    for (int s = 0; s < 4; ++s) acc = mfma(qw[s], vfrag(L, L_HVEC, lane, s), acc);
    if ((lane & 15) < 2) *(LAS f32x4*)(L + L_STG + 1024 + (lane & 15) * 512 + (16 * w + 4 * (lane >> 4)) * 4) = acc;
    cbar(L, lane, bar);
    if (w == 0) __builtin_amdgcn_raw_buffer_store_b128(*(const LAS v4u*)(L + L_STG + 1024 + lane * 16), rs, mybox + BX_Q + lane * 16, 0, AUX_SC1);
  }
  v4u qf[4], tw[8], tx[8];
  const int kl = (8 * ((lane & 15) >> 2) + (lane & 3)) * 256 + ((lane & 3) << 6) + (lane >> 4) * 16;
  {
    wait_landed(L, offK + w);
    kfrags(L, (offK + w) & 15, kl, tw);
    release(L, w, lane, offK + w + 8);
    wait_landed(L, offK + w + 8);
    kfrags(L, (offK + w + 8) & 15, kl, tx);
    release(L, w, lane, offK + w + 16);
    v4u g[4]; int off[4];
    const int head = hkv * 8 + w;
#pragma unroll
    for (int q = 0; q < 4; ++q) {
      const int jq = ((head & 3) * 8 + 2 * q + (lane >> 5)) * 8 + (head >> 2);
      off[q] = box(jq) + BX_Q + batt * 512 + (lane & 31) * 16;
    }
    gather<4, 8>(g, off, rs);
    const f32x4 t = sum4(g);
    const f32x2 a = swap32(t.x, t.y), b = swap32(t.z, t.w);
    const unsigned p = pk(a.x + a.y, b.x + b.y);
    LAS u16* qv = (LAS u16*)(L + L_QVEC + w * 256 + (lane & 31) * 8 + (lane >> 5) * 2);
    qv[0] = (u16)p; qv[2] = (u16)(p >> 16);
    cbar(L, lane, bar);
#pragma unroll
    for (int s = 0; s < 4; ++s) qf[s] = *(const LAS v4u*)(L + L_QVEC + (lane & 7) * 256 + s * 64 + (lane >> 4) * 16);
  }
  float sc[4][8];
#pragma unroll
  for (int i = 0; i < 4; ++i) {
    v4u kf[8];
    if (i < 2) {
#pragma unroll
      for (int q = 0; q < 8; ++q) kf[q] = i == 0 ? tw[q] : tx[q];
    } else {
      const int n = offK + w + 8 * i;
      wait_landed(L, n);
      kfrags(L, n & 15, kl, kf);
      release(L, w, lane, i < 3 ? n + 8 : offV);
    }
#pragma unroll
    for (int T = 0; T < 2; ++T) {
      f32x4 a = {0.f, 0.f, 0.f, 0.f};
#pragma unroll
      for (int s = 0; s < 4; ++s) a = mfma(kf[T * 4 + s], qf[s], a);
      const f32x2 lo = rbf2(rbf2(f32x2{a.x, a.y}) * 0.08838834764831845f), hi = rbf2(rbf2(f32x2{a.z, a.w}) * 0.08838834764831845f);
      sc[i][T * 4] = lo.x; sc[i][T * 4 + 1] = lo.y; sc[i][T * 4 + 2] = hi.x; sc[i][T * 4 + 3] = hi.y;
    }
  }
  float mw = sc[0][0];
  {
#pragma unroll
    for (int i = 0; i < 4; ++i)
#pragma unroll
      for (int r = 0; r < 8; ++r) mw = fmaxf(mw, sc[i][r]);
    mw = max32(max16(mw));
    float lw = 0.f;
#pragma unroll
    for (int i = 0; i < 4; ++i)
#pragma unroll
      for (int r = 0; r < 8; ++r) { sc[i][r] = exp_(sc[i][r] - mw); lw += sc[i][r]; }
    lw = sum32(sum16(lw));
    if (lane < 8) *(LAS f32x2*)(L + L_MLW + w * 64 + lane * 8) = f32x2{mw, lw};
    lds_inc(L, L_MLCNT, lane);
  }
  float ga = 0.f, gb = 0.f, xres = 0.f, bias = 0.f;
  f32x4 sbv = {0.f, 0.f, 0.f, 0.f}, cb1 = {0.f, 0.f, 0.f, 0.f};
  if (isB) {
    v4u g[2];
    const int off[2] = {box(30 * 8 + (w >> 1) + (w & 1) * 8) + BX_T1 + lane * 16, box(30 * 8 + 4 + (w >> 1) + (w & 1) * 8) + BX_T1 + lane * 16};
    gather<2>(g, off, rs);
    sbv = asf(g[0]);
    const f32x4 cb = asf(g[1]);
    const f32x2 c0 = rbf2(f32x2{cb.x, cb.y} + 1.f), c1 = rbf2(f32x2{cb.z, cb.w} + 1.f);
    cb1 = f32x4{c0.x, c0.y, c1.x, c1.y};
    bias = lo2f(*(const GAS u16*)(b1 + (bi >> 2) * 128 + 16 * w + 4 * (piece & 3) + comp));
    PIN(bias); PIN(sbv); PIN(cb1);
  }
  if (isC) {
    const int col = 64 * ci + 16 * (w & 3) + 4 * (piece & 3) + comp;
    xres = lo2f(*(const GAS u16*)(x + (piece >> 2) * D + col));
    bias = lo2f(*(const GAS u16*)(b2 + col));
    lds_wait_ge(L, L_FGATE, 1);
    ga = *(const LAS float*)(L + L_GATE + (w & 3) * 256 + lane * 4);
    gb = *(const LAS float*)(L + L_GATE + (4 + (w & 3)) * 256 + lane * 4);
    PIN(ga); PIN(gb); PIN(xres); PIN(bias);
  }
#pragma unroll
  for (int q = 0; q < VPARK; ++q) {
    wait_landed(L, offV + q);
    if (q < 8) tw[q & 7] = tfrag(L, (offV + q) & 15, tl); else tx[q & 7] = tfrag(L, (offV + q) & 15, tl);
    release(L, w, lane, offV + q + 1);
  }
  lds_wait_ge(L, L_FML, 1);
  {
    const f32x2 ml = *(const LAS f32x2*)(L + L_MLF + (lane & 7) * 8);
    const float f = exp_(mw - ml.x) * ml.y;
#pragma unroll
    for (int i = 0; i < 4; ++i) {
      const v4u p = {pk(sc[i][0] * f, sc[i][1] * f), pk(sc[i][2] * f, sc[i][3] * f), pk(sc[i][4] * f, sc[i][5] * f), pk(sc[i][6] * f, sc[i][7] * f)};
      if ((lane & 15) < 8) *(LAS v4u*)(L + L_P + (w + 8 * i) * 512 + ((lane >> 4) * 8 + (lane & 7)) * 16) = p;
    }
    cbar(L, lane, bar);
  }
  f32x4 oacc = {0.f, 0.f, 0.f, 0.f};
  const int pl = L_P + ((lane >> 4) * 8 + (lane & 7)) * 16;
#pragma unroll
  for (int i = 0; i < VPARK; ++i) oacc = mfma(i < 8 ? tw[i & 7] : tx[i & 7], *(const LAS v4u*)(L + pl + i * 512), oacc);
#pragma unroll 1
  for (int i = VPARK; i < 32; i += 2) {
    const int n = offV + i;
    wait_landed(L, n + 1); wait_landed(L, n);
    const v4u a0 = tfrag(L, n & 15, tl), a1 = tfrag(L, (n + 1) & 15, tl);
    const v4u p0 = *(const LAS v4u*)(L + pl + i * 512), p1 = *(const LAS v4u*)(L + pl + (i + 1) * 512);
    release(L, w, lane, n + 2);
    oacc = mfma(a0, p0, oacc);
    oacc = mfma(a1, p1, oacc);
  }
  if ((lane & 15) < 8) __builtin_amdgcn_raw_buffer_store_b128(asu(oacc), rs, mybox + BX_O + w * 512 + ((lane >> 4) * 8 + (lane & 15)) * 16, 0, AUX_SC1);

  if (isA) {
    v4u g[4]; int off[4];
#pragma unroll
    for (int q = 0; q < 4; ++q) off[q] = box((sg + 8 * q) * 8 + (w >> 2) * 4 + a_hkv) + BX_O + a_dt * 512 + (w & 3) * 128 + piece * 16;
#pragma unroll
    for (int q = 0; q < 4; ++q) { wait_landed(L, offW + q); tw[q] = tfrag(L, (offW + q) & 15, tl); }
    __builtin_amdgcn_s_sleep(4);
    gather<4>(g, off, rs);
    const float t = tred(sum4(g));
    *(LAS u16*)(L + L_TVEC + (w >> 2) * VROW + (16 * piece + 4 * (w & 3) + comp) * 2) = (u16)pk(t, t);
    WGBAR();
    tile_pair(L, L_TVEC, wf, tw, rs, mybox + BX_T1 + w * 128, lane);
  } else if (isC) {
    if (w < 4) {
      const int ct = 4 * ci + w, cg = ct >> 4, ti = ct & 15;
      v4u g[4]; int off[4], off2[4];
#pragma unroll
      for (int q = 0; q < 4; ++q) {
        const int a = (sg + 8 * q) * 4 + cg;
        off[q] = box((a & 15) * 8 + (a >> 4)) + BX_T1 + ti * 128 + piece * 16;
        const int kf = sg + 8 * q, bb = kf * 4 + cg, gg = bb / 14;
        off2[q] = kf < 28 ? box((16 + bb - gg * 14) * 8 + gg) + BX_T2 + ti * 128 + piece * 16 : WS_BYTES;
        PIN(off2[q]);
      }
      gather<4>(g, off, rs);
      const float x1 = rbf(xres + rbf(ga * rbf(tred(sum4(g)))));
      const f32x4 x4 = texp(x1);
      __builtin_amdgcn_raw_buffer_store_b128(asu(x4), rs, mybox + BX_X1 + sg * 512 + w * 128 + piece * 16, 0, AUX_SC1);
      gather<4>(g, off2, rs);
      const float o = x1 + rbf(gb * rbf(rbf(tred(sum4(g))) + bias));
      if (!(lane & 8)) *(GAS u16*)(out + (piece >> 2) * D + 64 * ci + 16 * w + 4 * (piece & 3) + comp) = (u16)pk(o, o);
    }
  } else {
#pragma unroll
    for (int q = 0; q < 8; ++q) { wait_landed(L, offW + q); tw[q] = tfrag(L, (offW + q) & 15, tl); }
    const int cc = 2 * w + (sg >> 2);
    const int offx[1] = {box((30 + (cc & 1)) * 8 + (cc >> 1)) + BX_X1 + grp * 512 + (sg & 3) * 128 + piece * 16};
    const int kf = bi >> 2, bb = (kf >> 1) * 8 + sg, gg = bb / 14;
    int offh[1] = {box((16 + bb - gg * 14) * 8 + gg) + BX_T1 + ((kf & 1) * 8 + w) * 128 + piece * 16};
    PIN(offh[0]);
    wait_landed(L, offW + 14); wait_landed(L, offW + 15);
    v4u g[1];
    gather<1>(g, offx, rs);
    const f32x4 x1 = asf(g[0]);
    float m, rden;
    stat_put(L, L_ST1, w, lane, x1);
    WGBAR();
    stat_get(L, L_ST1, lane, m, rden);
    const f32x2 ha = rbf2(rbf2(ln2(f32x2{x1.x, x1.y}, m, rden) * f32x2{cb1.x, cb1.y}) + f32x2{sbv.x, sbv.y});
    const f32x2 hb = rbf2(rbf2(ln2(f32x2{x1.z, x1.w}, m, rden) * f32x2{cb1.z, cb1.w}) + f32x2{sbv.z, sbv.w});
    stat_put(L, L_ST2, w, lane, f32x4{ha.x, ha.y, hb.x, hb.y});
    WGBAR();
    if (w == (bi & 7)) {
      stat_get(L, L_ST2, lane, m, rden);
      const f32x2 ya = rbf2(ha - m) * rden, yb = rbf2(hb - m) * rden;
      *(LAS v2u*)(L + L_TVEC + (piece >> 2) * VROW + (16 * sg + 4 * (piece & 3)) * 2) = v2u{pk(ya.x, ya.y), pk(yb.x, yb.y)};
    }
    WGBAR();
    tile_pair(L, L_TVEC, tw, tw + 4, rs, mybox + BX_T1 + w * 128, lane);
#pragma unroll
    for (int q = 0; q < 8; ++q) { wait_landed(L, offW + 8 + q); tw[q] = tfrag(L, (offW + 8 + q) & 15, tl); }
    __builtin_amdgcn_s_sleep(5);
    gather<1>(g, offh, rs);
    const float u = rbf(rbf(tred(asf(g[0]))) + bias);
    const float z = u * (0.7978845608028654f + 0.035677408136300125f * u * u);
    const float hh = u * rcp_(1.f + __builtin_amdgcn_exp2f(-2.8853900817779268f * z));
    *(LAS u16*)(L + L_UVEC + (piece >> 2) * VROW + (16 * w + 4 * (piece & 3) + comp) * 2) = (u16)pk(hh, hh);
    WGBAR();
    tile_pair(L, L_UVEC, tw, tw + 4, rs, mybox + BX_T2 + w * 128, lane);
  }
}

}

extern "C" void kernel_launch(void* const* d_in, const int* in_sizes, int n_in, void* d_out, int out_size, void* d_ws, size_t ws_size,
                              hipStream_t stream) {
  (void)in_sizes; (void)n_in; (void)out_size; (void)ws_size;
  (void)hipFuncSetAttribute((const void*)fused_block, hipFuncAttributeMaxDynamicSharedMemorySize, LDS_BYTES);
  hipLaunchKernelGGL(fused_block, dim3(256), dim3(NTHR), LDS_BYTES, stream, (const u16*)d_in[0], (const u16*)d_in[1], (const u16*)d_in[2],
                     (const u16*)d_in[3], (const u16*)d_in[4], (const u16*)d_in[6], (const u16*)d_in[7], (const u16*)d_in[8],
                     (const u16*)d_in[9], (const u16*)d_in[10], (const u16*)d_in[11], (u16*)d_out, (unsigned char*)d_ws);
}
```

```cpp
#include <hip/hip_runtime.h>
#include <hip/hip_bf16.h>
#include <stdint.h>

#define LAS __attribute__((address_space(3)))
#define GAS __attribute__((address_space(1)))
typedef unsigned short u16;
typedef unsigned v4u __attribute__((ext_vector_type(4)));
typedef unsigned v2u __attribute__((ext_vector_type(2)));
typedef float f32x4 __attribute__((ext_vector_type(4)));
typedef float f32x2 __attribute__((ext_vector_type(2)));
typedef short s16x4 __attribute__((ext_vector_type(4)));
typedef short bf16x8 __attribute__((ext_vector_type(8)));
typedef __bf16 bf16x2 __attribute__((ext_vector_type(2)));

namespace {

constexpr int D = 1024, SK = 32768;
constexpr int NTHR = 704;
constexpr int W_PRO = 18, W_KV = 11, W_WO = 16, W_MLP = 10;
constexpr int VPARK = 12;

constexpr int WS_EPOCH = 0, WS_ALIVE = 4096, WS_BOX = 65536, BOX = 16384;
constexpr int BX_MC = 0;
constexpr int BX_Q = 1024;
constexpr int BX_ML = 2048;
constexpr int BX_MR = 3072;
constexpr int BX_O = 6144;
constexpr int BX_T1 = 10240;
constexpr int BX_X1 = BX_T1 + 1024;
constexpr int BX_T2 = 12288;
constexpr int WS_BYTES = WS_BOX + 256 * BOX;
constexpr unsigned SENT = 0xFFFFFFFFu;
constexpr unsigned SPIN_CAP = 1u << 15, LDS_CAP = 1u << 20;
constexpr int AUX_SC1 = 16;

constexpr int L_RING = 0, L_P = 131072, L_CTL = L_P + 16384;
constexpr int L_X = L_P, L_C = L_P + 4096, L_STG = L_P + 8192;
constexpr int L_LANDED = L_CTL;
constexpr int L_PROG = L_CTL + 32;
constexpr int L_BAR = L_CTL + 64, L_MLCNT = L_CTL + 68, L_FALIVE = L_CTL + 72, L_FML = L_CTL + 76, L_FAUX = L_CTL + 80, L_FGATE = L_CTL + 88;
constexpr int L_SVEC = L_CTL + 256;
constexpr int VROW = 320;
constexpr int L_HVEC = L_SVEC + 4096;
constexpr int L_QVEC = L_HVEC + 640;
constexpr int L_ST1 = L_QVEC + 2048;
constexpr int L_ST2 = L_ST1 + 128;
constexpr int L_MLW = L_ST2 + 128;
constexpr int L_MLF = L_MLW + 512;
constexpr int L_TVEC = L_MLF + 64;
constexpr int L_UVEC = L_TVEC + 640;
constexpr int L_SBST = L_TVEC;
constexpr int L_GATE = L_UVEC + 640;
constexpr int LDS_BYTES = L_GATE + 2048;
static_assert(LDS_BYTES <= 160 * 1024 && LDS_BYTES > 80 * 1024, "one workgroup per CU");

typedef LAS unsigned char* lptr;

__device__ __forceinline__ float lo2f(unsigned w) { return __uint_as_float(w << 16); }
__device__ __forceinline__ float hi2f(unsigned w) { return __uint_as_float(w & 0xffff0000u); }
__device__ __forceinline__ unsigned pk(float a, float b) {
  bf16x2 v = {(__bf16)a, (__bf16)b};
  return __builtin_bit_cast(unsigned, v);
}
__device__ __forceinline__ float rbf(float a) { return hi2f(pk(a, a)); }
__device__ __forceinline__ f32x2 rbf2(f32x2 a) {
  unsigned p = pk(a.x, a.y);
  asm("" : "+v"(p));
  return f32x2{lo2f(p), hi2f(p)};
}
__device__ __forceinline__ f32x4 mfma(v4u a, v4u b, f32x4 c) {
  return __builtin_amdgcn_mfma_f32_16x16x32_bf16(__builtin_bit_cast(bf16x8, a), __builtin_bit_cast(bf16x8, b), c, 0, 0, 0);
}
template <int CTRL> __device__ __forceinline__ float dpp(float x) {
  return __builtin_bit_cast(float, __builtin_amdgcn_mov_dpp(__builtin_bit_cast(int, x), CTRL, 0xf, 0xf, true));
}
constexpr int XOR1 = 0xB1, XOR2 = 0x4E, ROR4 = 0x124, ROR8 = 0x128;
__device__ __forceinline__ f32x2 swap32(float a, float b) {
  auto s = __builtin_amdgcn_permlane32_swap(__float_as_uint(a), __float_as_uint(b), false, false);
  return f32x2{__uint_as_float(s[0]), __uint_as_float(s[1])};
}
__device__ __forceinline__ f32x2 swap16(float a, float b) {
  auto s = __builtin_amdgcn_permlane16_swap(__float_as_uint(a), __float_as_uint(b), false, false);
  return f32x2{__uint_as_float(s[0]), __uint_as_float(s[1])};
}
__device__ __forceinline__ float sum16(float x) { const f32x2 s = swap16(x, x); return s.x + s.y; }
__device__ __forceinline__ float sum32(float x) { const f32x2 s = swap32(x, x); return s.x + s.y; }
__device__ __forceinline__ float max16(float x) { const f32x2 s = swap16(x, x); return fmaxf(s.x, s.y); }
__device__ __forceinline__ float max32(float x) { const f32x2 s = swap32(x, x); return fmaxf(s.x, s.y); }
__device__ __forceinline__ float tred(f32x4 v) {
  const f32x2 a = swap32(v.x, v.y), b = swap32(v.z, v.w);
  const f32x2 c = swap16(a.x + a.y, b.x + b.y);
  const float t = c.x + c.y;
  return t + dpp<ROR8>(t);
}
__device__ __forceinline__ int tcomp(int lane) { return ((lane >> 4) & 1) * 2 + (lane >> 5); }
__device__ __forceinline__ f32x4 texp(float t) {
  const f32x2 a = swap16(t, t);
  const f32x2 b = swap32(a.x, a.x), c = swap32(a.y, a.y);
  return f32x4{b.x, b.y, c.x, c.y};
}
__device__ __forceinline__ f32x4 asf(v4u v) { return __builtin_bit_cast(f32x4, v); }
__device__ __forceinline__ v4u asu(f32x4 v) { return __builtin_bit_cast(v4u, v); }
__device__ __forceinline__ unsigned umax4(v4u v) { return max(max(max(v.x, v.y), v.z), v.w); }
__device__ __forceinline__ float exp_(float x) { return __builtin_amdgcn_exp2f(x * 1.4426950408889634f); }
__device__ __forceinline__ float rcp_(float x) { return __builtin_amdgcn_rcpf(x); }

#define LGKM0() asm volatile("s_waitcnt lgkmcnt(0)" ::: "memory")
#define VM0() asm volatile("s_waitcnt vmcnt(0)" ::: "memory")
#define CFENCE() asm volatile("" ::: "memory")
#define PIN(x) asm volatile("" : "+v"(x))
#define WGBAR() do { LGKM0(); __builtin_amdgcn_s_barrier(); CFENCE(); } while (0)

__device__ __forceinline__ void dma16(unsigned voff, const unsigned char* sbase, unsigned ldsaddr) {
  unsigned keep;
  asm volatile("s_mov_b32 %0, m0\n\ts_mov_b32 m0, %3\n\ts_nop 4\n\tglobal_load_lds_dwordx4 %1, %2 nt\n\ts_mov_b32 m0, %0"
               : "=&s"(keep) : "v"(voff), "s"(sbase), "s"(ldsaddr) : "memory");
}
__device__ __forceinline__ void lds_set(lptr L, int off, int lane, unsigned v) {
  if (lane == 0) *(volatile LAS unsigned*)(L + off) = v;
}
__device__ __forceinline__ void lds_wait_ge(lptr L, int off, unsigned need) {
#pragma nounroll
  for (unsigned sp = 0; (unsigned)__builtin_amdgcn_readfirstlane(*(volatile LAS unsigned*)(L + off)) < need && sp < LDS_CAP; ++sp) { }
  CFENCE();
}
__device__ __forceinline__ void wait_landed(lptr L, int n) { lds_wait_ge(L, L_LANDED + 4 * (n & 1), (unsigned)(n >> 1) + 1u); }
__device__ __forceinline__ void release(lptr L, int w, int lane, int n) { LGKM0(); lds_set(L, L_PROG + 4 * w, lane, (unsigned)n); }
__device__ __forceinline__ void lds_inc(lptr L, int off, int lane) {
  LGKM0();
  if (lane == 0) __hip_atomic_fetch_add((LAS unsigned*)(L + off), 1u, __ATOMIC_RELAXED, __HIP_MEMORY_SCOPE_WORKGROUP);
}
__device__ __forceinline__ void cbar(lptr L, int lane, unsigned& target) {
  target += 8;
  lds_inc(L, L_BAR, lane);
  lds_wait_ge(L, L_BAR, target);
}
__device__ __forceinline__ v4u tfrag(lptr L, int slot, int tl) {
  const s16x4 a = __builtin_amdgcn_ds_read_tr16_b64_v4i16((LAS s16x4*)(L + L_RING + slot * 8192 + tl));
  const s16x4 b = __builtin_amdgcn_ds_read_tr16_b64_v4i16((LAS s16x4*)(L + L_RING + slot * 8192 + tl + 1024));
  const v2u x = __builtin_bit_cast(v2u, a), y = __builtin_bit_cast(v2u, b);
  return v4u{x.x, x.y, y.x, y.y};
}
__device__ __forceinline__ void kfrags(lptr L, int slot, int kl, v4u (&f)[8]) {
#pragma unroll
  for (int q = 0; q < 8; ++q) f[q] = *(const LAS v4u*)(L + L_RING + slot * 8192 + (q >> 2) * 1024 + (kl ^ ((q & 3) << 6)));
}
__device__ __forceinline__ v4u sfrag(lptr L, int lane, int k0) {
  return *(const LAS v4u*)(L + L_SVEC + (lane & 1) * 2048 + k0 * 2 + (lane >> 4) * 16);
}
__device__ __forceinline__ v4u vfrag(lptr L, int off, int lane, int s) {
  return *(const LAS v4u*)(L + off + (lane & 1) * VROW + s * 64 + (lane >> 4) * 16);
}
template <int N>
__device__ __forceinline__ void gather_issue(v4u (&v)[N], const int (&off)[N], __amdgpu_buffer_rsrc_t rs) {
#pragma unroll
  for (int q = 0; q < N; ++q) v[q] = __builtin_amdgcn_raw_buffer_load_b128(rs, off[q], 0, AUX_SC1);
}
template <int N, int PAUSE>
__device__ __forceinline__ void gather_finish(v4u (&v)[N], const int (&off)[N], __amdgpu_buffer_rsrc_t rs) {
#pragma nounroll
  for (unsigned sp = 0; sp < SPIN_CAP; ++sp) {
    unsigned mx = umax4(v[0]);
#pragma unroll
    for (int q = 1; q < N; ++q) mx = max(mx, umax4(v[q]));
    if (__builtin_amdgcn_ballot_w64(mx == SENT) == 0) break;
    if (PAUSE) __builtin_amdgcn_s_sleep(PAUSE);
    CFENCE();
    if (mx == SENT) gather_issue<N>(v, off, rs);
  }
}
template <int N, int PAUSE = 0>
__device__ __forceinline__ void gather(v4u (&v)[N], const int (&off)[N], __amdgpu_buffer_rsrc_t rs) {
  gather_issue<N>(v, off, rs);
  gather_finish<N, PAUSE>(v, off, rs);
}
__device__ __forceinline__ f32x4 sum4(const v4u (&g)[4]) { return (asf(g[0]) + asf(g[1])) + (asf(g[2]) + asf(g[3])); }
__device__ __forceinline__ void tile_pair(lptr L, int vec, const v4u* f0, const v4u* f1, __amdgpu_buffer_rsrc_t rs, int off, int lane) {
  f32x4 a0 = {0.f, 0.f, 0.f, 0.f}, a1 = {0.f, 0.f, 0.f, 0.f};
#pragma unroll
  for (int s = 0; s < 4; ++s) { const v4u bv = vfrag(L, vec, lane, s); a0 = mfma(f0[s], bv, a0); a1 = mfma(f1[s], bv, a1); }
  if ((lane & 15) < 2) {
    __builtin_amdgcn_raw_buffer_store_b128(asu(a0), rs, off + (lane & 1) * 64 + (lane >> 4) * 16, 0, AUX_SC1);
    __builtin_amdgcn_raw_buffer_store_b128(asu(a1), rs, off + 1024 + (lane & 1) * 64 + (lane >> 4) * 16, 0, AUX_SC1);
  }
}
__device__ __forceinline__ int box(int j) { return WS_BOX + j * BOX; }
__device__ __forceinline__ void ln_consts(float s, float q, float& m, float& rden) {
  const float mean = s * (1.f / D);
  const float var = fmaxf(q * (1.f / D) - mean * mean, 0.f);
  m = rbf(mean);
  rden = rcp_(rbf(__builtin_amdgcn_sqrtf(rbf(rbf(var) + 1e-5f))));
}
__device__ __forceinline__ f32x2 ln2(f32x2 x, float m, float rden) { return rbf2(rbf2(x - m) * rden); }
__device__ __forceinline__ void stat_put(lptr L, int off, int w, int lane, f32x4 v) {
  const float s = (v.x + v.y) + (v.z + v.w), q = (v.x * v.x + v.y * v.y) + (v.z * v.z + v.w * v.w);
  const f32x2 a = swap32(s, q);
  float r = sum16(a.x + a.y);
  r += dpp<ROR8>(r); r += dpp<XOR2>(r); r += dpp<XOR1>(r);
  if ((lane & 0x1B) == 0) *(LAS float*)(L + off + w * 16 + (lane & 4) * 2 + (lane >> 5) * 4) = r;
}
__device__ __forceinline__ void stat_get(lptr L, int off, int lane, float& m, float& rden) {
  const f32x2 p = *(const LAS f32x2*)(L + off + (lane >> 3) * 16 + (lane & 4) * 2);
  const f32x2 a = swap32(p.x, p.y);
  float r = sum16(a.x + a.y);
  r += dpp<ROR8>(r);
  const f32x2 t = swap32(r, r);
  ln_consts(t.x, t.y, m, rden);
}

__device__ __forceinline__ int read_minprog(lptr L, int lane) {
  unsigned m = *(volatile LAS unsigned*)(L + L_PROG + 4 * (lane & 7));
  m = min(m, (unsigned)__builtin_amdgcn_mov_dpp((int)m, XOR1, 0xf, 0xf, true));
  m = min(m, (unsigned)__builtin_amdgcn_mov_dpp((int)m, XOR2, 0xf, 0xf, true));
  m = min(m, (unsigned)__builtin_amdgcn_mov_dpp((int)m, ROR4, 0xf, 0xf, true));
  return __builtin_amdgcn_readfirstlane((int)m);
}
template <int W>
__device__ __forceinline__ void load_block(lptr L, int ld, int lane, int n, const unsigned char* bp, int ldb, int hi, int& minprog) {
  if (minprog < n - 15) {
    minprog = read_minprog(L, lane);
    if (minprog < n - 15) {
      VM0();
      lds_set(L, L_LANDED + 4 * ld, lane, (unsigned)(n >> 1));
      for (unsigned sp = 0; sp < LDS_CAP; ++sp) {
        minprog = read_minprog(L, lane);
        if (minprog >= n - 15) break;
      }
    }
  }
  const unsigned voff = (unsigned)((lane >> 4) * ldb + (((lane & 15) ^ ((lane >> 4) << 2)) << 4));
  const unsigned dst = (unsigned)(size_t)L + L_RING + (unsigned)(n & 15) * 8192u;
#pragma unroll
  for (int p = 0; p < 8; ++p) {
    dma16(voff, bp + (size_t)((p & 3) * 4) * ldb + (size_t)(p >> 2) * hi, dst + p * 1024);
    asm volatile("s_waitcnt vmcnt(%0)" ::"i"(W - 1) : "memory");
    if (((p + 2 - W) & 7) == 0) {
      const int done = (n >> 1) + (p + 2 - W) / 8;
      if (done > 0) lds_set(L, L_LANDED + 4 * ld, lane, (unsigned)done);
    }
  }
}

__global__ void __launch_bounds__(NTHR) fused_block(const u16* __restrict__ x, const u16* __restrict__ kc, const u16* __restrict__ vc,
                                                     const u16* __restrict__ wq, const u16* __restrict__ wo, const u16* __restrict__ w1,
                                                     const u16* __restrict__ b1, const u16* __restrict__ w2, const u16* __restrict__ b2,
                                                     const u16* __restrict__ cvec, const u16* __restrict__ wmod, u16* __restrict__ out,
                                                     unsigned char* ws) {
  extern __shared__ __attribute__((aligned(16))) unsigned char smem[];
  lptr L = (lptr)smem;
  const int tid = threadIdx.x, lane = tid & 63, w = __builtin_amdgcn_readfirstlane(tid >> 6);
  const int j = blockIdx.x, grp = j & 7, split = j >> 3, batt = grp >> 2, hkv = grp & 3;
  const bool isA = split < 16, isC = split >= 30, isB = !isA && !isC;
  const int kq = split & 7, g32 = grp * 4 + (split >> 3), g16 = grp * 2 + ((split >> 3) & 1);
  const int mu0 = isA ? 2 * (grp * 16 + split) : 256 + 2 * (grp * 16 + split - 16);
  const int offQ = isA ? 0 : 4, offMR = offQ + 4;
  constexpr int offWO = 8, offK = 12, offV = offK + 32, offW = offV + 32;
  const int ai = grp * 16 + split, a_hkv = ai >> 5, a_dt = (ai >> 2) & 7, a_cg = ai & 3;
  const int bi = grp * 14 + (split - 16), ci = grp * 2 + (split - 30);

  if (tid < 64) *(volatile LAS unsigned*)(L + L_CTL + 4 * tid) = 0u;
  WGBAR();
  unsigned long long epoch = 0, tag = 0;
  if (w == 10) {
    const __amdgpu_buffer_rsrc_t rsh = __builtin_amdgcn_make_buffer_rsrc((void*)ws, 0, WS_BYTES, 0x00020000);
    const v2u ep = __builtin_amdgcn_raw_buffer_load_b64(rsh, WS_EPOCH, 0, AUX_SC1);
    const v4u s4 = {SENT, SENT, SENT, SENT};
    const unsigned used = isA ? 0x0FDEu : (isB ? 0x3FDFu : 0x7FDFu);
#pragma unroll 1
    for (int u = 0; u < 15; ++u)
      if ((used >> u) & 1) __builtin_amdgcn_raw_buffer_store_b128(s4, rsh, box(j) + u * 1024 + lane * 16, 0, AUX_SC1);
    VM0();
    epoch = ((unsigned long long)(unsigned)__builtin_amdgcn_readfirstlane((int)ep.y) << 32) | (unsigned)__builtin_amdgcn_readfirstlane((int)ep.x);
    tag = (epoch ^ 0x9E3779B97F4A7C15ull) * 0xD6E8FEB86659FD93ull + 1ull;
    if (lane == 0) __hip_atomic_store((GAS unsigned long long*)(ws + WS_ALIVE + j * 16), tag, __ATOMIC_RELAXED, __HIP_MEMORY_SCOPE_AGENT);
  }
  if (__builtin_expect(w == 8 || w == 9, 1)) {
    const int ld = w - 8;
    __builtin_amdgcn_s_setprio(3);
    int minprog = 0;
    const unsigned char* aux = (const unsigned char*)(ld ? cvec : x);
#pragma unroll
    for (int p = 0; p < 4; ++p) dma16((unsigned)lane * 16u, aux + p * 1024, (unsigned)(size_t)L + L_X + ld * 4096 + p * 1024);
    const size_t kvoff = ((size_t)grp * SK + split * 1024) * 256;
    const unsigned char* wo_t = (const unsigned char*)wo + ((size_t)(a_hkv * 8) * 128 + 16 * a_dt) * 2048 + a_cg * 512;
#pragma unroll 1
    for (int n = ld; n < offW; n += 2) {
      if (n < offK) {
        const unsigned char* bp; int ldb, hi;
        if (isA && n >= offWO) { bp = wo_t + (size_t)(n - offWO) * 256 * 2048; ldb = 2048; hi = 128 * 2048; }
        else {
          if (n < offQ) { bp = (const unsigned char*)wmod + (size_t)kq * 128 * 12288 + g16 * 256; ldb = 12288; }
          else if (n < offMR) { bp = (const unsigned char*)wq + (size_t)kq * 128 * 8192 + g32 * 256; ldb = 8192; }
          else { const int u = mu0 + ((n - offMR) >> 1); bp = (const unsigned char*)wmod + (size_t)(u & 15) * 64 * 12288 + 4096 + (u >> 4) * 256; ldb = 12288; }
          bp += (size_t)((n < offMR ? n & 3 : (n - offMR) & 1) * 32) * ldb; hi = 16 * ldb;
        }
        load_block<W_PRO>(L, ld, lane, n, bp, ldb, hi, minprog);
        if (n < 2) {
          asm volatile("s_waitcnt vmcnt(8)" ::: "memory");
          lds_set(L, L_FAUX + 4 * ld, lane, 1u);
        }
      } else {
        const unsigned char* bp = n < offV ? (const unsigned char*)kc + kvoff + (size_t)(n - offK) * 8192 : (const unsigned char*)vc + kvoff + (size_t)(n - offV) * 8192;
        load_block<W_KV>(L, ld, lane, n, bp, 256, 4096, minprog);
      }
    }
    int nend = offW;
    if (isA) {
      nend = offW + 4;
#pragma unroll 1
      for (int n = offW + ld; n < nend; n += 2) load_block<W_WO>(L, ld, lane, n, wo_t + (size_t)(n - offW) * 256 * 2048 + 256, 2048, 128 * 2048, minprog);
    }
    if (isB) {
      nend = offW + 16;
#pragma unroll 1
      for (int n = offW + ld; n < nend; n += 2) {
        const int q = n - offW;
        const unsigned char* bp; int ldb;
        if (q < 8) { bp = (const unsigned char*)w1 + (size_t)(bi & 7) * 128 * 7168 + (bi >> 3) * 512; ldb = 7168; }
        else { bp = (const unsigned char*)w2 + (size_t)(bi >> 2) * 128 * 2048 + (bi & 3) * 512; ldb = 2048; }
        load_block<W_MLP>(L, ld, lane, n, bp + (size_t)((q & 3) * 32) * ldb + ((q >> 2) & 1) * 256, ldb, 16 * ldb, minprog);
      }
    }
    VM0();
    lds_set(L, L_LANDED + 4 * ld, lane, (unsigned)(nend >> 1));
    return;
  }

  const __amdgpu_buffer_rsrc_t rs = __builtin_amdgcn_make_buffer_rsrc((void*)ws, 0, WS_BYTES, 0x00020000);
  const int mybox = box(j);
  const int piece = lane & 7, sg = lane >> 3, comp = tcomp(lane);

  if (w == 10) {
    for (int i = 0; i < 3; ++i) __builtin_amdgcn_s_sleep(24);
    for (unsigned sp = 0; sp < SPIN_CAP; ++sp) {
      bool ok = true;
#pragma unroll
      for (int q = 0; q < 4; ++q) {
        const v2u t = __builtin_amdgcn_raw_buffer_load_b64(rs, WS_ALIVE + (q * 64 + lane) * 16, 0, AUX_SC1);
        ok = ok && t.x == (unsigned)tag && t.y == (unsigned)(tag >> 32);
      }
      if (__all(ok)) break;
      __builtin_amdgcn_s_sleep(4);
      CFENCE();
    }
    lds_set(L, L_FALIVE, lane, 1u);
    if (j == 0 && lane == 0) __hip_atomic_store((GAS unsigned long long*)(ws + WS_EPOCH), epoch + 1ull, __ATOMIC_RELAXED, __HIP_MEMORY_SCOPE_AGENT);
    if (isC) {
      lds_wait_ge(L, L_LANDED, (unsigned)(offK + 8) >> 1);
      auto mr = [&](int cg, int kh, int line) {
        const int u = cg * 16 + kh;
        const int ia = (u >> 1) & 127, jj = ((u < 256 ? 0 : 16) + (ia & 15)) * 8 + (ia >> 4);
        return box(jj) + BX_MR + (u & 1) * 1024 + line * 128 + piece * 16;
      };
#pragma unroll 1
      for (int h4 = 0; h4 < 4; ++h4) {
        const int cg = h4 < 2 ? 8 + ci : (h4 == 2 ? 0 : 24) + (ci >> 1), l0 = h4 < 2 ? 4 * h4 : (ci & 1) * 4;
        const int o0 = mr(cg, sg, l0), o1 = mr(cg, sg + 8, l0);
        v4u g[8];
        const int off[8] = {o0, o1, o0 + 128, o1 + 128, o0 + 256, o1 + 256, o0 + 384, o1 + 384};
        gather<8, 8>(g, off, rs);
#pragma unroll
        for (int q = 0; q < 4; ++q) {
          const float t = rbf(tred(asf(g[2 * q]) + asf(g[2 * q + 1])));
          if (h4 < 2) *(LAS float*)(L + L_SBST + (l0 + q) * 128 + piece * 16 + comp * 4) = t;
          else *(LAS float*)(L + L_GATE + ((h4 - 2) * 4 + q) * 256 + lane * 4) = t;
        }
      }
      LGKM0();
      __builtin_amdgcn_raw_buffer_store_b128(*(const LAS v4u*)(L + L_SBST + lane * 16), rs, mybox + BX_T1 + lane * 16, 0, AUX_SC1);
      lds_set(L, L_FGATE, lane, 1u);
    }
    lds_wait_ge(L, L_MLCNT, 8);
    const f32x2 r = *(const LAS f32x2*)(L + L_MLW + sg * 64 + piece * 8);
    float M = r.x;
    M = fmaxf(M, dpp<ROR8>(M)); M = max32(max16(M));
    float Ls = r.y * exp_(r.x - M);
    Ls += dpp<ROR8>(Ls); Ls = sum32(sum16(Ls));
    const float M1 = dpp<XOR1>(M), L1 = dpp<XOR1>(Ls);
    if (lane < 8 && !(lane & 1)) __builtin_amdgcn_raw_buffer_store_b128(asu(f32x4{M, Ls, M1, L1}), rs, mybox + BX_ML + lane * 8, 0, AUX_SC1);
    for (int i = 0; i < 2; ++i) __builtin_amdgcn_s_sleep(32);
    v4u g[2];
    const int off[2] = {box((lane >> 2) * 8 + grp) + BX_ML + (lane & 3) * 16, box(((lane >> 2) + 16) * 8 + grp) + BX_ML + (lane & 3) * 16};
    gather<2, 16>(g, off, rs);
    const f32x4 u = asf(g[0]), v = asf(g[1]);
    float M0 = fmaxf(u.x, v.x), Mb = fmaxf(u.z, v.z);
    M0 = fmaxf(M0, dpp<ROR4>(M0)); M0 = fmaxf(M0, dpp<ROR8>(M0)); M0 = max32(max16(M0));
    Mb = fmaxf(Mb, dpp<ROR4>(Mb)); Mb = fmaxf(Mb, dpp<ROR8>(Mb)); Mb = max32(max16(Mb));
    float L0 = u.y * exp_(u.x - M0) + v.y * exp_(v.x - M0), Lb = u.w * exp_(u.z - Mb) + v.w * exp_(v.z - Mb);
    L0 += dpp<ROR4>(L0); L0 += dpp<ROR8>(L0); L0 = sum32(sum16(L0));
    Lb += dpp<ROR4>(Lb); Lb += dpp<ROR8>(Lb); Lb = sum32(sum16(Lb));
    if (lane < 4) *(LAS f32x4*)(L + L_MLF + lane * 16) = f32x4{M0, 1.f / L0, Mb, 1.f / Lb};
    LGKM0();
    lds_set(L, L_FML, lane, 1u);
    return;
  }

  unsigned warm = 0;
  if (w == 0) {
    const unsigned char* p = (const unsigned char*)ws; size_t o = 0;
    const size_t kv0 = ((size_t)grp * SK + split * 1024) * 256;
    const int l = lane;
    if (l < 2) { p = (const unsigned char*)kc; o = kv0 + l * 131072; }
    else if (l < 4) { p = (const unsigned char*)vc; o = kv0 + (l - 2) * 131072; }
    else if (l < 8) { p = (const unsigned char*)wq; o = (size_t)kq * 128 * 8192 + (l - 4) * 262144 + g32 * 256; }
    else if (l < 14) { p = (const unsigned char*)wmod; o = (size_t)kq * 128 * 12288 + (l - 8) * 262144; }
    else if (l < 20) { const int u = mu0 + (l - 14) / 3; p = (const unsigned char*)wmod; o = (size_t)(u & 15) * 64 * 12288 + ((l - 14) % 3) * 262144 + 4096; }
    else if (l < 28) {
      if (isA) { p = (const unsigned char*)wo; o = (size_t)(a_hkv * 8) * 128 * 2048 + (l - 20) * 262144; }
      else if (isB && l < 24) { p = (const unsigned char*)w1; o = (size_t)(bi & 7) * 128 * 7168 + (l - 20) * 229376; }
      else if (isB) { p = (const unsigned char*)w2; o = (size_t)(bi >> 2) * 128 * 2048; }
    }
    else if (l < 45) o = (size_t)(l - 28) * 262144;
    else if (l == 45) p = (const unsigned char*)b1;
    else if (l == 46) p = (const unsigned char*)b2;
    asm volatile("global_load_dword %0, %1, off" : "=&v"(warm) : "v"(p + o) : "memory");
  }
  unsigned bar = 0;
  lds_wait_ge(L, L_FAUX, 1); lds_wait_ge(L, L_FAUX + 4, 1);
  asm volatile("s_waitcnt vmcnt(0)" : "+v"(warm) :: "memory");
  const unsigned xh = *(const LAS u16*)(L + L_X + ((piece >> 2) * D + kq * 128 + 16 * w + 4 * (piece & 3) + comp) * 2);
  {
    const v2u cr = *(const LAS v2u*)(L + L_C + tid * 8);
    float s[4] = {lo2f(cr.x), hi2f(cr.x), lo2f(cr.y), hi2f(cr.y)};
#pragma unroll
    for (int i = 0; i < 4; ++i) s[i] = s[i] * rcp_(1.f + exp_(-s[i]));
    *(LAS v2u*)(L + L_SVEC + tid * 8) = v2u{pk(s[0], s[1]), pk(s[2], s[3])};
  }
  {
    const v2u xr = *(const LAS v2u*)(L + L_X + tid * 8);
    const float a0 = lo2f(xr.x), a1 = hi2f(xr.x), a2 = lo2f(xr.y), a3 = hi2f(xr.y);
    const f32x2 sq = swap32((a0 + a1) + (a2 + a3), (a0 * a0 + a1 * a1) + (a2 * a2 + a3 * a3));
    float r = sum16(sq.x + sq.y);
    r += dpp<ROR8>(r); r += dpp<ROR4>(r); r += dpp<XOR2>(r); r += dpp<XOR1>(r);
    if ((lane & 31) == 0) *(LAS float*)(L + L_ST1 + w * 8 + (lane >> 5) * 4) = r;
  }
  cbar(L, lane, bar);
  float lnx;
  {
    const int b = piece >> 2;
    const f32x4 r0 = *(const LAS f32x4*)(L + L_ST1 + b * 32), r1 = *(const LAS f32x4*)(L + L_ST1 + b * 32 + 16);
    float m, rden;
    ln_consts((r0.x + r0.z) + (r1.x + r1.z), (r0.y + r0.w) + (r1.y + r1.w), m, rden);
    lnx = rbf(rbf(lo2f(xh) - m) * rden);
    PIN(lnx);
  }
  const int tl = (8 * (lane >> 4) + ((lane >> 2) & 3)) * 256 + (((2 * w + ((lane & 3) >> 1)) ^ (((lane >> 2) & 3) << 2)) << 4) + (lane & 1) * 8;
  const int pubo = w * 128 + (lane & 15) * 64 + (lane >> 4) * 16;

  if (!isA) {
    f32x4 acc = {0.f, 0.f, 0.f, 0.f};
#pragma unroll
    for (int s = 0; s < 4; ++s) { wait_landed(L, s); acc = mfma(tfrag(L, s, tl), sfrag(L, lane, kq * 128 + 32 * s), acc); }
    release(L, w, lane, 4);
    if ((lane & 15) < 2) *(LAS f32x4*)(L + L_STG + pubo) = acc;
    cbar(L, lane, bar);
    if (w == 0) __builtin_amdgcn_raw_buffer_store_b128(*(const LAS v4u*)(L + L_STG + lane * 16), rs, mybox + BX_MC + lane * 16, 0, AUX_SC1);
  }
  v4u qw[4];
#pragma unroll
  for (int s = 0; s < 4; ++s) { wait_landed(L, offQ + s); qw[s] = tfrag(L, (offQ + s) & 15, tl); }
  release(L, w, lane, offMR);
#pragma unroll 1
  for (int u = 0; u < 2; ++u) {
    const int n = offMR + 2 * u, k0 = ((mu0 + u) & 15) * 64;
    wait_landed(L, n); wait_landed(L, n + 1);
    f32x4 acc = mfma(tfrag(L, n & 15, tl), sfrag(L, lane, k0), f32x4{0.f, 0.f, 0.f, 0.f});
    acc = mfma(tfrag(L, (n + 1) & 15, tl), sfrag(L, lane, k0 + 32), acc);
    release(L, w, lane, n + 2 < offK ? n + 2 : offK + w);
    if ((lane & 15) < 2) *(LAS f32x4*)(L + L_STG + 2048 + u * 1024 + pubo) = acc;
  }
  v4u wf[4];
  if (isA) {
#pragma unroll
    for (int q = 0; q < 4; ++q) { wait_landed(L, offWO + q); wf[q] = tfrag(L, offWO + q, tl); }
    release(L, w, lane, offK + w);
  }
  {
    lds_wait_ge(L, L_FALIVE, 1);
    v4u g[2];
    const int js = (16 + (kq & 1) * 8 + sg) * 8 + (kq >> 1);
    const int off[2] = {box(js) + BX_MC + w * 128 + piece * 16, box(js + 4) + BX_MC + w * 128 + piece * 16};
    gather<2>(g, off, rs);
    const float sa = rbf(tred(asf(g[0]))), ca = rbf(tred(asf(g[1])));
    const float h = rbf(lnx * rbf(1.f + ca)) + sa;
    if (!(lane & 8)) *(LAS u16*)(L + L_HVEC + (piece >> 2) * VROW + (16 * w + 4 * (piece & 3) + comp) * 2) = (u16)pk(h, h);
    cbar(L, lane, bar);
    if (w >= 1 && w <= 2)
      __builtin_amdgcn_raw_buffer_store_b128(*(const LAS v4u*)(L + L_STG + 1024 + w * 1024 + lane * 16), rs, mybox + BX_MR + (w - 1) * 1024 + lane * 16, 0, AUX_SC1);
    f32x4 acc = {0.f, 0.f, 0.f, 0.f};
#pragma unroll
    for (int s = 0; s < 4; ++s) acc = mfma(qw[s], vfrag(L, L_HVEC, lane, s), acc);
    if ((lane & 15) < 2) *(LAS f32x4*)(L + L_STG + 1024 + (lane & 15) * 512 + (16 * w + 4 * (lane >> 4)) * 4) = acc;
    cbar(L, lane, bar);
    if (w == 0) __builtin_amdgcn_raw_buffer_store_b128(*(const LAS v4u*)(L + L_STG + 1024 + lane * 16), rs, mybox + BX_Q + lane * 16, 0, AUX_SC1);
  }
  v4u qf[4], tw[8], tx[8];
  const int kl = (8 * ((lane & 15) >> 2) + (lane & 3)) * 256 + ((lane & 3) << 6) + (lane >> 4) * 16;
  {
    wait_landed(L, offK + w);
    kfrags(L, (offK + w) & 15, kl, tw);
    release(L, w, lane, offK + w + 8);
    wait_landed(L, offK + w + 8);
    kfrags(L, (offK + w + 8) & 15, kl, tx);
    release(L, w, lane, offK + w + 16);
    v4u g[4]; int off[4];
    const int head = hkv * 8 + w;
#pragma unroll
    for (int q = 0; q < 4; ++q) {
      const int jq = ((head & 3) * 8 + 2 * q + (lane >> 5)) * 8 + (head >> 2);
      off[q] = box(jq) + BX_Q + batt * 512 + (lane & 31) * 16;
    }
    gather<4, 8>(g, off, rs);
    const f32x4 t = sum4(g);
    const f32x2 a = swap32(t.x, t.y), b = swap32(t.z, t.w);
    const unsigned p = pk(a.x + a.y, b.x + b.y);
    LAS u16* qv = (LAS u16*)(L + L_QVEC + w * 256 + (lane & 31) * 8 + (lane >> 5) * 2);
    qv[0] = (u16)p; qv[2] = (u16)(p >> 16);
    cbar(L, lane, bar);
#pragma unroll
    for (int s = 0; s < 4; ++s) qf[s] = *(const LAS v4u*)(L + L_QVEC + (lane & 7) * 256 + s * 64 + (lane >> 4) * 16);
  }
  float sc[4][8];
#pragma unroll
  for (int i = 0; i < 4; ++i) {
    v4u kf[8];
    if (i < 2) {
#pragma unroll
      for (int q = 0; q < 8; ++q) kf[q] = i == 0 ? tw[q] : tx[q];
    } else {
      const int n = offK + w + 8 * i;
      wait_landed(L, n);
      kfrags(L, n & 15, kl, kf);
      release(L, w, lane, i < 3 ? n + 8 : offV);
    }
#pragma unroll
    for (int T = 0; T < 2; ++T) {
      f32x4 a = {0.f, 0.f, 0.f, 0.f};
#pragma unroll
      for (int s = 0; s < 4; ++s) a = mfma(kf[T * 4 + s], qf[s], a);
      const f32x2 lo = rbf2(rbf2(f32x2{a.x, a.y}) * 0.08838834764831845f), hi = rbf2(rbf2(f32x2{a.z, a.w}) * 0.08838834764831845f);
      sc[i][T * 4] = lo.x; sc[i][T * 4 + 1] = lo.y; sc[i][T * 4 + 2] = hi.x; sc[i][T * 4 + 3] = hi.y;
    }
  }
  float mw = sc[0][0];
  {
#pragma unroll
    for (int i = 0; i < 4; ++i)
#pragma unroll
      for (int r = 0; r < 8; ++r) mw = fmaxf(mw, sc[i][r]);
    mw = max32(max16(mw));
    float lw = 0.f;
#pragma unroll
    for (int i = 0; i < 4; ++i)
#pragma unroll
      for (int r = 0; r < 8; ++r) { sc[i][r] = exp_(sc[i][r] - mw); lw += sc[i][r]; }
    lw = sum32(sum16(lw));
    if (lane < 8) *(LAS f32x2*)(L + L_MLW + w * 64 + lane * 8) = f32x2{mw, lw};
    lds_inc(L, L_MLCNT, lane);
  }
  float ga = 0.f, gb = 0.f, xres = 0.f, bias = 0.f;
  if (isC) {
    const int col = 64 * ci + 16 * (w & 3) + 4 * (piece & 3) + comp;
    xres = lo2f(*(const GAS u16*)(x + (piece >> 2) * D + col));
    bias = lo2f(*(const GAS u16*)(b2 + col));
    lds_wait_ge(L, L_FGATE, 1);
    ga = *(const LAS float*)(L + L_GATE + (w & 3) * 256 + lane * 4);
    gb = *(const LAS float*)(L + L_GATE + (4 + (w & 3)) * 256 + lane * 4);
    PIN(ga); PIN(gb); PIN(xres); PIN(bias);
  }
#pragma unroll
  for (int q = 0; q < VPARK; ++q) {
    wait_landed(L, offV + q);
    if (q < 8) tw[q & 7] = tfrag(L, (offV + q) & 15, tl); else tx[q & 7] = tfrag(L, (offV + q) & 15, tl);
    release(L, w, lane, offV + q + 1);
  }
  lds_wait_ge(L, L_FML, 1);
  {
    const f32x2 ml = *(const LAS f32x2*)(L + L_MLF + (lane & 7) * 8);
    const float f = exp_(mw - ml.x) * ml.y;
#pragma unroll
    for (int i = 0; i < 4; ++i) {
      const v4u p = {pk(sc[i][0] * f, sc[i][1] * f), pk(sc[i][2] * f, sc[i][3] * f), pk(sc[i][4] * f, sc[i][5] * f), pk(sc[i][6] * f, sc[i][7] * f)};
      if ((lane & 15) < 8) *(LAS v4u*)(L + L_P + (w + 8 * i) * 512 + ((lane >> 4) * 8 + (lane & 7)) * 16) = p;
    }
    cbar(L, lane, bar);
  }
  f32x4 oacc = {0.f, 0.f, 0.f, 0.f};
  const int pl = L_P + ((lane >> 4) * 8 + (lane & 7)) * 16;
#pragma unroll
  for (int i = 0; i < VPARK; ++i) oacc = mfma(i < 8 ? tw[i & 7] : tx[i & 7], *(const LAS v4u*)(L + pl + i * 512), oacc);
#pragma unroll 1
  for (int i = VPARK; i < 32; i += 2) {
    const int n = offV + i;
    wait_landed(L, n + 1); wait_landed(L, n);
    const v4u a0 = tfrag(L, n & 15, tl), a1 = tfrag(L, (n + 1) & 15, tl);
    const v4u p0 = *(const LAS v4u*)(L + pl + i * 512), p1 = *(const LAS v4u*)(L + pl + (i + 1) * 512);
    release(L, w, lane, n + 2);
    oacc = mfma(a0, p0, oacc);
    oacc = mfma(a1, p1, oacc);
  }
  if ((lane & 15) < 8) __builtin_amdgcn_raw_buffer_store_b128(asu(oacc), rs, mybox + BX_O + w * 512 + ((lane >> 4) * 8 + (lane & 15)) * 16, 0, AUX_SC1);

  if (isA) {
    v4u g[4]; int off[4];
#pragma unroll
    for (int q = 0; q < 4; ++q) off[q] = box((sg + 8 * q) * 8 + (w >> 2) * 4 + a_hkv) + BX_O + a_dt * 512 + (w & 3) * 128 + piece * 16;
#pragma unroll
    for (int q = 0; q < 4; ++q) { wait_landed(L, offW + q); tw[q] = tfrag(L, (offW + q) & 15, tl); }
    __builtin_amdgcn_s_sleep(4);
    gather<4>(g, off, rs);
    const float t = tred(sum4(g));
    *(LAS u16*)(L + L_TVEC + (w >> 2) * VROW + (16 * piece + 4 * (w & 3) + comp) * 2) = (u16)pk(t, t);
    WGBAR();
    tile_pair(L, L_TVEC, wf, tw, rs, mybox + BX_T1 + w * 128, lane);
  } else if (isC) {
    if (w < 4) {
      const int ct = 4 * ci + w, cg = ct >> 4, ti = ct & 15;
      v4u g[4]; int off[4], off2[4];
#pragma unroll
      for (int q = 0; q < 4; ++q) {
        const int a = (sg + 8 * q) * 4 + cg;
        off[q] = box((a & 15) * 8 + (a >> 4)) + BX_T1 + ti * 128 + piece * 16;
        const int kf = sg + 8 * q, bb = kf * 4 + cg, gg = bb / 14;
        off2[q] = kf < 28 ? box((16 + bb - gg * 14) * 8 + gg) + BX_T2 + ti * 128 + piece * 16 : WS_BYTES;
        PIN(off2[q]);
      }
      gather<4>(g, off, rs);
      const float x1 = rbf(xres + rbf(ga * rbf(tred(sum4(g)))));
      const f32x4 x4 = texp(x1);
      __builtin_amdgcn_raw_buffer_store_b128(asu(x4), rs, mybox + BX_X1 + sg * 512 + w * 128 + piece * 16, 0, AUX_SC1);
      gather<4>(g, off2, rs);
      const float o = x1 + rbf(gb * rbf(rbf(tred(sum4(g))) + bias));
      if (!(lane & 8)) *(GAS u16*)(out + (piece >> 2) * D + 64 * ci + 16 * w + 4 * (piece & 3) + comp) = (u16)pk(o, o);
    }
  } else {
    f32x4 sbv, cb1;
    {
      v4u g2[2];
      const int off[2] = {box(30 * 8 + (w >> 1) + (w & 1) * 8) + BX_T1 + lane * 16, box(30 * 8 + 4 + (w >> 1) + (w & 1) * 8) + BX_T1 + lane * 16};
      gather<2>(g2, off, rs);
      sbv = asf(g2[0]);
      const f32x4 cb = asf(g2[1]);
      const f32x2 c0 = rbf2(f32x2{cb.x, cb.y} + 1.f), c1 = rbf2(f32x2{cb.z, cb.w} + 1.f);
      cb1 = f32x4{c0.x, c0.y, c1.x, c1.y};
      bias = lo2f(*(const GAS u16*)(b1 + (bi >> 2) * 128 + 16 * w + 4 * (piece & 3) + comp));
      PIN(bias); PIN(sbv); PIN(cb1);
    }
#pragma unroll
    for (int q = 0; q < 8; ++q) { wait_landed(L, offW + q); tw[q] = tfrag(L, (offW + q) & 15, tl); }
    const int cc = 2 * w + (sg >> 2);
    const int offx[1] = {box((30 + (cc & 1)) * 8 + (cc >> 1)) + BX_X1 + grp * 512 + (sg & 3) * 128 + piece * 16};
    const int kf = bi >> 2, bb = (kf >> 1) * 8 + sg, gg = bb / 14;
    int offh[1] = {box((16 + bb - gg * 14) * 8 + gg) + BX_T1 + ((kf & 1) * 8 + w) * 128 + piece * 16};
    PIN(offh[0]);
    wait_landed(L, offW + 14); wait_landed(L, offW + 15);
    v4u g[1];
    gather<1>(g, offx, rs);
    const f32x4 x1 = asf(g[0]);
    float m, rden;
    stat_put(L, L_ST1, w, lane, x1);
    WGBAR();
    stat_get(L, L_ST1, lane, m, rden);
    const f32x2 ha = rbf2(rbf2(ln2(f32x2{x1.x, x1.y}, m, rden) * f32x2{cb1.x, cb1.y}) + f32x2{sbv.x, sbv.y});
    const f32x2 hb = rbf2(rbf2(ln2(f32x2{x1.z, x1.w}, m, rden) * f32x2{cb1.z, cb1.w}) + f32x2{sbv.z, sbv.w});
    stat_put(L, L_ST2, w, lane, f32x4{ha.x, ha.y, hb.x, hb.y});
    WGBAR();
    if (w == (bi & 7)) {
      stat_get(L, L_ST2, lane, m, rden);
      const f32x2 ya = rbf2(ha - m) * rden, yb = rbf2(hb - m) * rden;
      *(LAS v2u*)(L + L_TVEC + (piece >> 2) * VROW + (16 * sg + 4 * (piece & 3)) * 2) = v2u{pk(ya.x, ya.y), pk(yb.x, yb.y)};
    }
    WGBAR();
    tile_pair(L, L_TVEC, tw, tw + 4, rs, mybox + BX_T1 + w * 128, lane);
#pragma unroll
    for (int q = 0; q < 8; ++q) { wait_landed(L, offW + 8 + q); tw[q] = tfrag(L, (offW + 8 + q) & 15, tl); }
    __builtin_amdgcn_s_sleep(5);
    gather<1>(g, offh, rs);
    const float u = rbf(rbf(tred(asf(g[0]))) + bias);
    const float z = u * (0.7978845608028654f + 0.035677408136300125f * u * u);
    const float hh = u * rcp_(1.f + __builtin_amdgcn_exp2f(-2.8853900817779268f * z));
    *(LAS u16*)(L + L_UVEC + (piece >> 2) * VROW + (16 * w + 4 * (piece & 3) + comp) * 2) = (u16)pk(hh, hh);
    WGBAR();
    tile_pair(L, L_UVEC, tw, tw + 4, rs, mybox + BX_T2 + w * 128, lane);
  }
}

}

extern "C" void kernel_launch(void* const* d_in, const int* in_sizes, int n_in, void* d_out, int out_size, void* d_ws, size_t ws_size,
                              hipStream_t stream) {
  (void)in_sizes; (void)n_in; (void)out_size; (void)ws_size;
  (void)hipFuncSetAttribute((const void*)fused_block, hipFuncAttributeMaxDynamicSharedMemorySize, LDS_BYTES);
  hipLaunchKernelGGL(fused_block, dim3(256), dim3(NTHR), LDS_BYTES, stream, (const u16*)d_in[0], (const u16*)d_in[1], (const u16*)d_in[2],
                     (const u16*)d_in[3], (const u16*)d_in[4], (const u16*)d_in[6], (const u16*)d_in[7], (const u16*)d_in[8],
                     (const u16*)d_in[9], (const u16*)d_in[10], (const u16*)d_in[11], (u16*)d_out, (unsigned char*)d_ws);
}
```

```cpp
#include <hip/hip_runtime.h>
#include <hip/hip_bf16.h>
#include <stdint.h>

#define LAS __attribute__((address_space(3)))
#define GAS __attribute__((address_space(1)))
typedef unsigned short u16;
typedef unsigned v4u __attribute__((ext_vector_type(4)));
typedef unsigned v2u __attribute__((ext_vector_type(2)));
typedef float f32x4 __attribute__((ext_vector_type(4)));
typedef float f32x2 __attribute__((ext_vector_type(2)));
typedef short s16x4 __attribute__((ext_vector_type(4)));
typedef short bf16x8 __attribute__((ext_vector_type(8)));
typedef __bf16 bf16x2 __attribute__((ext_vector_type(2)));

namespace {

constexpr int D = 1024, SK = 32768;
constexpr int NTHR = 704;
constexpr int W_PRO = 18, W_KV = 12, W_WO = 16, W_MLP = 10;
constexpr int VPARK = 12;

constexpr int WS_EPOCH = 0, WS_ALIVE = 4096, WS_BOX = 65536, BOX = 16384;
constexpr int BX_MC = 0;
constexpr int BX_Q = 1024;
constexpr int BX_ML = 2048;
constexpr int BX_MR = 3072;
constexpr int BX_O = 6144;
constexpr int BX_T1 = 10240;
constexpr int BX_X1 = BX_T1 + 1024;
constexpr int BX_T2 = 12288;
constexpr int WS_BYTES = WS_BOX + 256 * BOX;
constexpr unsigned SENT = 0xFFFFFFFFu;
constexpr unsigned SPIN_CAP = 1u << 15, LDS_CAP = 1u << 20;
constexpr int AUX_SC1 = 16;

constexpr int L_RING = 0, L_P = 131072, L_CTL = L_P + 16384;
constexpr int L_X = L_P, L_C = L_P + 4096, L_STG = L_P + 8192;
constexpr int L_LANDED = L_CTL;
constexpr int L_PROG = L_CTL + 32;
constexpr int L_BAR = L_CTL + 64, L_MLCNT = L_CTL + 68, L_FALIVE = L_CTL + 72, L_FML = L_CTL + 76, L_FAUX = L_CTL + 80, L_FGATE = L_CTL + 88;
constexpr int L_SVEC = L_CTL + 256;
constexpr int VROW = 320;
constexpr int L_HVEC = L_SVEC + 4096;
constexpr int L_QVEC = L_HVEC + 640;
constexpr int L_ST1 = L_QVEC + 2048;
constexpr int L_ST2 = L_ST1 + 128;
constexpr int L_MLW = L_ST2 + 128;
constexpr int L_MLF = L_MLW + 512;
constexpr int L_TVEC = L_MLF + 64;
constexpr int L_UVEC = L_TVEC + 640;
constexpr int L_SBST = L_TVEC;
constexpr int L_GATE = L_UVEC + 640;
constexpr int LDS_BYTES = L_GATE + 2048;
static_assert(LDS_BYTES <= 160 * 1024 && LDS_BYTES > 80 * 1024, "one workgroup per CU");

typedef LAS unsigned char* lptr;

__device__ __forceinline__ float lo2f(unsigned w) { return __uint_as_float(w << 16); }
__device__ __forceinline__ float hi2f(unsigned w) { return __uint_as_float(w & 0xffff0000u); }
__device__ __forceinline__ unsigned pk(float a, float b) {
  bf16x2 v = {(__bf16)a, (__bf16)b};
  return __builtin_bit_cast(unsigned, v);
}
__device__ __forceinline__ float rbf(float a) { return hi2f(pk(a, a)); }
__device__ __forceinline__ f32x2 rbf2(f32x2 a) {
  unsigned p = pk(a.x, a.y);
  asm("" : "+v"(p));
  return f32x2{lo2f(p), hi2f(p)};
}
__device__ __forceinline__ f32x4 mfma(v4u a, v4u b, f32x4 c) {
  return __builtin_amdgcn_mfma_f32_16x16x32_bf16(__builtin_bit_cast(bf16x8, a), __builtin_bit_cast(bf16x8, b), c, 0, 0, 0);
}
template <int CTRL> __device__ __forceinline__ float dpp(float x) {
  return __builtin_bit_cast(float, __builtin_amdgcn_mov_dpp(__builtin_bit_cast(int, x), CTRL, 0xf, 0xf, true));
}
constexpr int XOR1 = 0xB1, XOR2 = 0x4E, ROR4 = 0x124, ROR8 = 0x128;
__device__ __forceinline__ f32x2 swap32(float a, float b) {
  auto s = __builtin_amdgcn_permlane32_swap(__float_as_uint(a), __float_as_uint(b), false, false);
  return f32x2{__uint_as_float(s[0]), __uint_as_float(s[1])};
}
__device__ __forceinline__ f32x2 swap16(float a, float b) {
  auto s = __builtin_amdgcn_permlane16_swap(__float_as_uint(a), __float_as_uint(b), false, false);
  return f32x2{__uint_as_float(s[0]), __uint_as_float(s[1])};
}
__device__ __forceinline__ float sum16(float x) { const f32x2 s = swap16(x, x); return s.x + s.y; }
__device__ __forceinline__ float sum32(float x) { const f32x2 s = swap32(x, x); return s.x + s.y; }
__device__ __forceinline__ float max16(float x) { const f32x2 s = swap16(x, x); return fmaxf(s.x, s.y); }
__device__ __forceinline__ float max32(float x) { const f32x2 s = swap32(x, x); return fmaxf(s.x, s.y); }
__device__ __forceinline__ float tred(f32x4 v) {
  const f32x2 a = swap32(v.x, v.y), b = swap32(v.z, v.w);
  const f32x2 c = swap16(a.x + a.y, b.x + b.y);
  const float t = c.x + c.y;
  return t + dpp<ROR8>(t);
}
__device__ __forceinline__ int tcomp(int lane) { return ((lane >> 4) & 1) * 2 + (lane >> 5); }
__device__ __forceinline__ f32x4 texp(float t) {
  const f32x2 a = swap16(t, t);
  const f32x2 b = swap32(a.x, a.x), c = swap32(a.y, a.y);
  return f32x4{b.x, b.y, c.x, c.y};
}
__device__ __forceinline__ f32x4 asf(v4u v) { return __builtin_bit_cast(f32x4, v); }
__device__ __forceinline__ v4u asu(f32x4 v) { return __builtin_bit_cast(v4u, v); }
__device__ __forceinline__ unsigned umax4(v4u v) { return max(max(max(v.x, v.y), v.z), v.w); }
__device__ __forceinline__ float exp_(float x) { return __builtin_amdgcn_exp2f(x * 1.4426950408889634f); }
__device__ __forceinline__ float rcp_(float x) { return __builtin_amdgcn_rcpf(x); }

#define LGKM0() asm volatile("s_waitcnt lgkmcnt(0)" ::: "memory")
#define VM0() asm volatile("s_waitcnt vmcnt(0)" ::: "memory")
#define CFENCE() asm volatile("" ::: "memory")
#define PIN(x) asm volatile("" : "+v"(x))
#define WGBAR() do { LGKM0(); __builtin_amdgcn_s_barrier(); CFENCE(); } while (0)

__device__ __forceinline__ void dma16(unsigned voff, const unsigned char* sbase, unsigned ldsaddr) {
  unsigned keep;
  asm volatile("s_mov_b32 %0, m0\n\ts_mov_b32 m0, %3\n\ts_nop 4\n\tglobal_load_lds_dwordx4 %1, %2 nt\n\ts_mov_b32 m0, %0"
               : "=&s"(keep) : "v"(voff), "s"(sbase), "s"(ldsaddr) : "memory");
}
__device__ __forceinline__ void lds_set(lptr L, int off, int lane, unsigned v) {
  if (lane == 0) *(volatile LAS unsigned*)(L + off) = v;
}
__device__ __forceinline__ void lds_wait_ge(lptr L, int off, unsigned need) {
#pragma nounroll
  for (unsigned sp = 0; (unsigned)__builtin_amdgcn_readfirstlane(*(volatile LAS unsigned*)(L + off)) < need && sp < LDS_CAP; ++sp) { }
  CFENCE();
}
__device__ __forceinline__ void wait_landed(lptr L, int n) { lds_wait_ge(L, L_LANDED + 4 * (n & 1), (unsigned)(n >> 1) + 1u); }
__device__ __forceinline__ void release(lptr L, int w, int lane, int n) { LGKM0(); lds_set(L, L_PROG + 4 * w, lane, (unsigned)n); }
__device__ __forceinline__ void lds_inc(lptr L, int off, int lane) {
  LGKM0();
  if (lane == 0) __hip_atomic_fetch_add((LAS unsigned*)(L + off), 1u, __ATOMIC_RELAXED, __HIP_MEMORY_SCOPE_WORKGROUP);
}
__device__ __forceinline__ void cbar(lptr L, int lane, unsigned& target) {
  target += 8;
  lds_inc(L, L_BAR, lane);
  lds_wait_ge(L, L_BAR, target);
}
__device__ __forceinline__ v4u tfrag(lptr L, int slot, int tl) {
  const s16x4 a = __builtin_amdgcn_ds_read_tr16_b64_v4i16((LAS s16x4*)(L + L_RING + slot * 8192 + tl));
  const s16x4 b = __builtin_amdgcn_ds_read_tr16_b64_v4i16((LAS s16x4*)(L + L_RING + slot * 8192 + tl + 1024));
  const v2u x = __builtin_bit_cast(v2u, a), y = __builtin_bit_cast(v2u, b);
  return v4u{x.x, x.y, y.x, y.y};
}
__device__ __forceinline__ void kfrags(lptr L, int slot, int kl, v4u (&f)[8]) {
#pragma unroll
  for (int q = 0; q < 8; ++q) f[q] = *(const LAS v4u*)(L + L_RING + slot * 8192 + (q >> 2) * 1024 + (kl ^ ((q & 3) << 6)));
}
__device__ __forceinline__ v4u sfrag(lptr L, int lane, int k0) {
  return *(const LAS v4u*)(L + L_SVEC + (lane & 1) * 2048 + k0 * 2 + (lane >> 4) * 16);
}
__device__ __forceinline__ v4u vfrag(lptr L, int off, int lane, int s) {
  return *(const LAS v4u*)(L + off + (lane & 1) * VROW + s * 64 + (lane >> 4) * 16);
}
template <int N>
__device__ __forceinline__ void gather_issue(v4u (&v)[N], const int (&off)[N], __amdgpu_buffer_rsrc_t rs) {
#pragma unroll
  for (int q = 0; q < N; ++q) v[q] = __builtin_amdgcn_raw_buffer_load_b128(rs, off[q], 0, AUX_SC1);
}
template <int N, int PAUSE>
__device__ __forceinline__ void gather_finish(v4u (&v)[N], const int (&off)[N], __amdgpu_buffer_rsrc_t rs) {
#pragma nounroll
  for (unsigned sp = 0; sp < SPIN_CAP; ++sp) {
    unsigned mx = umax4(v[0]);
#pragma unroll
    for (int q = 1; q < N; ++q) mx = max(mx, umax4(v[q]));
    if (__builtin_amdgcn_ballot_w64(mx == SENT) == 0) break;
    if (PAUSE) __builtin_amdgcn_s_sleep(PAUSE);
    CFENCE();
    if (mx == SENT) gather_issue<N>(v, off, rs);
  }
}
template <int N, int PAUSE = 0>
__device__ __forceinline__ void gather(v4u (&v)[N], const int (&off)[N], __amdgpu_buffer_rsrc_t rs) {
  gather_issue<N>(v, off, rs);
  gather_finish<N, PAUSE>(v, off, rs);
}
__device__ __forceinline__ f32x4 sum4(const v4u (&g)[4]) { return (asf(g[0]) + asf(g[1])) + (asf(g[2]) + asf(g[3])); }
__device__ __forceinline__ void tile_pair(lptr L, int vec, const v4u* f0, const v4u* f1, __amdgpu_buffer_rsrc_t rs, int off, int lane) {
  f32x4 a0 = {0.f, 0.f, 0.f, 0.f}, a1 = {0.f, 0.f, 0.f, 0.f};
#pragma unroll
  for (int s = 0; s < 4; ++s) { const v4u bv = vfrag(L, vec, lane, s); a0 = mfma(f0[s], bv, a0); a1 = mfma(f1[s], bv, a1); }
  if ((lane & 15) < 2) {
    __builtin_amdgcn_raw_buffer_store_b128(asu(a0), rs, off + (lane & 1) * 64 + (lane >> 4) * 16, 0, AUX_SC1);
    __builtin_amdgcn_raw_buffer_store_b128(asu(a1), rs, off + 1024 + (lane & 1) * 64 + (lane >> 4) * 16, 0, AUX_SC1);
  }
}
__device__ __forceinline__ int box(int j) { return WS_BOX + j * BOX; }
__device__ __forceinline__ void ln_consts(float s, float q, float& m, float& rden) {
  const float mean = s * (1.f / D);
  const float var = fmaxf(q * (1.f / D) - mean * mean, 0.f);
  m = rbf(mean);
  rden = rcp_(rbf(__builtin_amdgcn_sqrtf(rbf(rbf(var) + 1e-5f))));
}
__device__ __forceinline__ f32x2 ln2(f32x2 x, float m, float rden) { return rbf2(rbf2(x - m) * rden); }
__device__ __forceinline__ void stat_put(lptr L, int off, int w, int lane, f32x4 v) {
  const float s = (v.x + v.y) + (v.z + v.w), q = (v.x * v.x + v.y * v.y) + (v.z * v.z + v.w * v.w);
  const f32x2 a = swap32(s, q);
  float r = sum16(a.x + a.y);
  r += dpp<ROR8>(r); r += dpp<XOR2>(r); r += dpp<XOR1>(r);
  if ((lane & 0x1B) == 0) *(LAS float*)(L + off + w * 16 + (lane & 4) * 2 + (lane >> 5) * 4) = r;
}
__device__ __forceinline__ void stat_get(lptr L, int off, int lane, float& m, float& rden) {
  const f32x2 p = *(const LAS f32x2*)(L + off + (lane >> 3) * 16 + (lane & 4) * 2);
  const f32x2 a = swap32(p.x, p.y);
  float r = sum16(a.x + a.y);
  r += dpp<ROR8>(r);
  const f32x2 t = swap32(r, r);
  ln_consts(t.x, t.y, m, rden);
}

__device__ __forceinline__ int read_minprog(lptr L, int lane) {
  unsigned m = *(volatile LAS unsigned*)(L + L_PROG + 4 * (lane & 7));
  m = min(m, (unsigned)__builtin_amdgcn_mov_dpp((int)m, XOR1, 0xf, 0xf, true));
  m = min(m, (unsigned)__builtin_amdgcn_mov_dpp((int)m, XOR2, 0xf, 0xf, true));
  m = min(m, (unsigned)__builtin_amdgcn_mov_dpp((int)m, ROR4, 0xf, 0xf, true));
  return __builtin_amdgcn_readfirstlane((int)m);
}
template <int W>
__device__ __forceinline__ void load_block(lptr L, int ld, int lane, int n, const unsigned char* bp, int ldb, int hi, int& minprog) {
  if (minprog < n - 15) {
    minprog = read_minprog(L, lane);
    if (minprog < n - 15) {
      VM0();
      lds_set(L, L_LANDED + 4 * ld, lane, (unsigned)(n >> 1));
      for (unsigned sp = 0; sp < LDS_CAP; ++sp) {
        minprog = read_minprog(L, lane);
        if (minprog >= n - 15) break;
      }
    }
  }
  const unsigned voff = (unsigned)((lane >> 4) * ldb + (((lane & 15) ^ ((lane >> 4) << 2)) << 4));
  const unsigned dst = (unsigned)(size_t)L + L_RING + (unsigned)(n & 15) * 8192u;
#pragma unroll
  for (int p = 0; p < 8; ++p) {
    dma16(voff, bp + (size_t)((p & 3) * 4) * ldb + (size_t)(p >> 2) * hi, dst + p * 1024);
    asm volatile("s_waitcnt vmcnt(%0)" ::"i"(W - 1) : "memory");
    if (((p + 2 - W) & 7) == 0) {
      const int done = (n >> 1) + (p + 2 - W) / 8;
      if (done > 0) lds_set(L, L_LANDED + 4 * ld, lane, (unsigned)done);
    }
  }
}

__global__ void __launch_bounds__(NTHR) fused_block(const u16* __restrict__ x, const u16* __restrict__ kc, const u16* __restrict__ vc,
                                                     const u16* __restrict__ wq, const u16* __restrict__ wo, const u16* __restrict__ w1,
                                                     const u16* __restrict__ b1, const u16* __restrict__ w2, const u16* __restrict__ b2,
                                                     const u16* __restrict__ cvec, const u16* __restrict__ wmod, u16* __restrict__ out,
                                                     unsigned char* ws) {
  extern __shared__ __attribute__((aligned(16))) unsigned char smem[];
  lptr L = (lptr)smem;
  const int tid = threadIdx.x, lane = tid & 63, w = __builtin_amdgcn_readfirstlane(tid >> 6);
  const int j = blockIdx.x, grp = j & 7, split = j >> 3, batt = grp >> 2, hkv = grp & 3;
  const bool isA = split < 16, isC = split >= 30, isB = !isA && !isC;
  const int kq = split & 7, g32 = grp * 4 + (split >> 3), g16 = grp * 2 + ((split >> 3) & 1);
  const int mu0 = isA ? 2 * (grp * 16 + split) : 256 + 2 * (grp * 16 + split - 16);
  const int offQ = isA ? 0 : 4, offMR = offQ + 4;
  constexpr int offWO = 8, offK = 12, offV = offK + 32, offW = offV + 32;
  const int ai = grp * 16 + split, a_hkv = ai >> 5, a_dt = (ai >> 2) & 7, a_cg = ai & 3;
  const int bi = grp * 14 + (split - 16), ci = grp * 2 + (split - 30);

  if (tid < 64) *(volatile LAS unsigned*)(L + L_CTL + 4 * tid) = 0u;
  WGBAR();
  unsigned long long epoch = 0, tag = 0;
  if (w == 10) {
    const __amdgpu_buffer_rsrc_t rsh = __builtin_amdgcn_make_buffer_rsrc((void*)ws, 0, WS_BYTES, 0x00020000);
    const v2u ep = __builtin_amdgcn_raw_buffer_load_b64(rsh, WS_EPOCH, 0, AUX_SC1);
    const v4u s4 = {SENT, SENT, SENT, SENT};
    const unsigned used = isA ? 0x0FDEu : (isB ? 0x3FDFu : 0x7FDFu);
#pragma unroll 1
    for (int u = 0; u < 15; ++u)
      if ((used >> u) & 1) __builtin_amdgcn_raw_buffer_store_b128(s4, rsh, box(j) + u * 1024 + lane * 16, 0, AUX_SC1);
    VM0();
    epoch = ((unsigned long long)(unsigned)__builtin_amdgcn_readfirstlane((int)ep.y) << 32) | (unsigned)__builtin_amdgcn_readfirstlane((int)ep.x);
    tag = (epoch ^ 0x9E3779B97F4A7C15ull) * 0xD6E8FEB86659FD93ull + 1ull;
    if (lane == 0) __hip_atomic_store((GAS unsigned long long*)(ws + WS_ALIVE + j * 16), tag, __ATOMIC_RELAXED, __HIP_MEMORY_SCOPE_AGENT);
  }
  if (__builtin_expect(w == 8 || w == 9, 1)) {
    const int ld = w - 8;
    __builtin_amdgcn_s_setprio(3);
    int minprog = 0;
    const unsigned char* aux = (const unsigned char*)(ld ? cvec : x);
#pragma unroll
    for (int p = 0; p < 4; ++p) dma16((unsigned)lane * 16u, aux + p * 1024, (unsigned)(size_t)L + L_X + ld * 4096 + p * 1024);
    const size_t kvoff = ((size_t)grp * SK + split * 1024) * 256;
    const unsigned char* wo_t = (const unsigned char*)wo + ((size_t)(a_hkv * 8) * 128 + 16 * a_dt) * 2048 + a_cg * 512;
#pragma unroll 1
    for (int n = ld; n < offW; n += 2) {
      if (n < offK) {
        const unsigned char* bp; int ldb, hi;
        if (isA && n >= offWO) { bp = wo_t + (size_t)(n - offWO) * 256 * 2048; ldb = 2048; hi = 128 * 2048; }
        else {
          if (n < offQ) { bp = (const unsigned char*)wmod + (size_t)kq * 128 * 12288 + g16 * 256; ldb = 12288; }
          else if (n < offMR) { bp = (const unsigned char*)wq + (size_t)kq * 128 * 8192 + g32 * 256; ldb = 8192; }
          else { const int u = mu0 + ((n - offMR) >> 1); bp = (const unsigned char*)wmod + (size_t)(u & 15) * 64 * 12288 + 4096 + (u >> 4) * 256; ldb = 12288; }
          bp += (size_t)((n < offMR ? n & 3 : (n - offMR) & 1) * 32) * ldb; hi = 16 * ldb;
        }
        load_block<W_PRO>(L, ld, lane, n, bp, ldb, hi, minprog);
        if (n < 2) {
          asm volatile("s_waitcnt vmcnt(8)" ::: "memory");
          lds_set(L, L_FAUX + 4 * ld, lane, 1u);
        }
      } else {
        const unsigned char* bp = n < offV ? (const unsigned char*)kc + kvoff + (size_t)(n - offK) * 8192 : (const unsigned char*)vc + kvoff + (size_t)(n - offV) * 8192;
        load_block<W_KV>(L, ld, lane, n, bp, 256, 4096, minprog);
      }
    }
    int nend = offW;
    if (isA) {
      nend = offW + 4;
#pragma unroll 1
      for (int n = offW + ld; n < nend; n += 2) load_block<W_WO>(L, ld, lane, n, wo_t + (size_t)(n - offW) * 256 * 2048 + 256, 2048, 128 * 2048, minprog);
    }
    if (isB) {
      nend = offW + 16;
#pragma unroll 1
      for (int n = offW + ld; n < nend; n += 2) {
        const int q = n - offW;
        const unsigned char* bp; int ldb;
        if (q < 8) { bp = (const unsigned char*)w1 + (size_t)(bi & 7) * 128 * 7168 + (bi >> 3) * 512; ldb = 7168; }
        else { bp = (const unsigned char*)w2 + (size_t)(bi >> 2) * 128 * 2048 + (bi & 3) * 512; ldb = 2048; }
        load_block<W_MLP>(L, ld, lane, n, bp + (size_t)((q & 3) * 32) * ldb + ((q >> 2) & 1) * 256, ldb, 16 * ldb, minprog);
      }
    }
    VM0();
    lds_set(L, L_LANDED + 4 * ld, lane, (unsigned)(nend >> 1));
    return;
  }

  const __amdgpu_buffer_rsrc_t rs = __builtin_amdgcn_make_buffer_rsrc((void*)ws, 0, WS_BYTES, 0x00020000);
  const int mybox = box(j);
  const int piece = lane & 7, sg = lane >> 3, comp = tcomp(lane);

  if (w == 10) {
    for (int i = 0; i < 3; ++i) __builtin_amdgcn_s_sleep(24);
    for (unsigned sp = 0; sp < SPIN_CAP; ++sp) {
      bool ok = true;
#pragma unroll
      for (int q = 0; q < 4; ++q) {
        const v2u t = __builtin_amdgcn_raw_buffer_load_b64(rs, WS_ALIVE + (q * 64 + lane) * 16, 0, AUX_SC1);
        ok = ok && t.x == (unsigned)tag && t.y == (unsigned)(tag >> 32);
      }
      if (__all(ok)) break;
      __builtin_amdgcn_s_sleep(4);
      CFENCE();
    }
    lds_set(L, L_FALIVE, lane, 1u);
    if (j == 0 && lane == 0) __hip_atomic_store((GAS unsigned long long*)(ws + WS_EPOCH), epoch + 1ull, __ATOMIC_RELAXED, __HIP_MEMORY_SCOPE_AGENT);
    if (isC) {
      lds_wait_ge(L, L_LANDED, (unsigned)(offK + 8) >> 1);
      auto mr = [&](int cg, int kh, int line) {
        const int u = cg * 16 + kh;
        const int ia = (u >> 1) & 127, jj = ((u < 256 ? 0 : 16) + (ia & 15)) * 8 + (ia >> 4);
        return box(jj) + BX_MR + (u & 1) * 1024 + line * 128 + piece * 16;
      };
#pragma unroll 1
      for (int h4 = 0; h4 < 4; ++h4) {
        const int cg = h4 < 2 ? 8 + ci : (h4 == 2 ? 0 : 24) + (ci >> 1), l0 = h4 < 2 ? 4 * h4 : (ci & 1) * 4;
        const int o0 = mr(cg, sg, l0), o1 = mr(cg, sg + 8, l0);
        v4u g[8];
        const int off[8] = {o0, o1, o0 + 128, o1 + 128, o0 + 256, o1 + 256, o0 + 384, o1 + 384};
        gather<8, 8>(g, off, rs);
#pragma unroll
        for (int q = 0; q < 4; ++q) {
          const float t = rbf(tred(asf(g[2 * q]) + asf(g[2 * q + 1])));
          if (h4 < 2) *(LAS float*)(L + L_SBST + (l0 + q) * 128 + piece * 16 + comp * 4) = t;
          else *(LAS float*)(L + L_GATE + ((h4 - 2) * 4 + q) * 256 + lane * 4) = t;
        }
      }
      LGKM0();
      __builtin_amdgcn_raw_buffer_store_b128(*(const LAS v4u*)(L + L_SBST + lane * 16), rs, mybox + BX_T1 + lane * 16, 0, AUX_SC1);
      lds_set(L, L_FGATE, lane, 1u);
    }
    lds_wait_ge(L, L_MLCNT, 8);
    const f32x2 r = *(const LAS f32x2*)(L + L_MLW + sg * 64 + piece * 8);
    float M = r.x;
    M = fmaxf(M, dpp<ROR8>(M)); M = max32(max16(M));
    float Ls = r.y * exp_(r.x - M);
    Ls += dpp<ROR8>(Ls); Ls = sum32(sum16(Ls));
    const float M1 = dpp<XOR1>(M), L1 = dpp<XOR1>(Ls);
    if (lane < 8 && !(lane & 1)) __builtin_amdgcn_raw_buffer_store_b128(asu(f32x4{M, Ls, M1, L1}), rs, mybox + BX_ML + lane * 8, 0, AUX_SC1);
    for (int i = 0; i < 2; ++i) __builtin_amdgcn_s_sleep(32);
    v4u g[2];
    const int off[2] = {box((lane >> 2) * 8 + grp) + BX_ML + (lane & 3) * 16, box(((lane >> 2) + 16) * 8 + grp) + BX_ML + (lane & 3) * 16};
    gather<2, 16>(g, off, rs);
    const f32x4 u = asf(g[0]), v = asf(g[1]);
    float M0 = fmaxf(u.x, v.x), Mb = fmaxf(u.z, v.z);
    M0 = fmaxf(M0, dpp<ROR4>(M0)); M0 = fmaxf(M0, dpp<ROR8>(M0)); M0 = max32(max16(M0));
    Mb = fmaxf(Mb, dpp<ROR4>(Mb)); Mb = fmaxf(Mb, dpp<ROR8>(Mb)); Mb = max32(max16(Mb));
    float L0 = u.y * exp_(u.x - M0) + v.y * exp_(v.x - M0), Lb = u.w * exp_(u.z - Mb) + v.w * exp_(v.z - Mb);
    L0 += dpp<ROR4>(L0); L0 += dpp<ROR8>(L0); L0 = sum32(sum16(L0));
    Lb += dpp<ROR4>(Lb); Lb += dpp<ROR8>(Lb); Lb = sum32(sum16(Lb));
    if (lane < 4) *(LAS f32x4*)(L + L_MLF + lane * 16) = f32x4{M0, 1.f / L0, Mb, 1.f / Lb};
    LGKM0();
    lds_set(L, L_FML, lane, 1u);
    return;
  }

  unsigned warm = 0;
  if (w == 0) {
    const unsigned char* p = (const unsigned char*)ws; size_t o = 0;
    const size_t kv0 = ((size_t)grp * SK + split * 1024) * 256;
    const int l = lane;
    if (l < 2) { p = (const unsigned char*)kc; o = kv0 + l * 131072; }
    else if (l < 4) { p = (const unsigned char*)vc; o = kv0 + (l - 2) * 131072; }
    else if (l < 8) { p = (const unsigned char*)wq; o = (size_t)kq * 128 * 8192 + (l - 4) * 262144 + g32 * 256; }
    else if (l < 14) { p = (const unsigned char*)wmod; o = (size_t)kq * 128 * 12288 + (l - 8) * 262144; }
    else if (l < 20) { const int u = mu0 + (l - 14) / 3; p = (const unsigned char*)wmod; o = (size_t)(u & 15) * 64 * 12288 + ((l - 14) % 3) * 262144 + 4096; }
    else if (l < 28) {
      if (isA) { p = (const unsigned char*)wo; o = (size_t)(a_hkv * 8) * 128 * 2048 + (l - 20) * 262144; }
      else if (isB && l < 24) { p = (const unsigned char*)w1; o = (size_t)(bi & 7) * 128 * 7168 + (l - 20) * 229376; }
      else if (isB) { p = (const unsigned char*)w2; o = (size_t)(bi >> 2) * 128 * 2048; }
    }
    else if (l < 45) o = (size_t)(l - 28) * 262144;
    else if (l == 45) p = (const unsigned char*)b1;
    else if (l == 46) p = (const unsigned char*)b2;
    asm volatile("global_load_dword %0, %1, off" : "=&v"(warm) : "v"(p + o) : "memory");
  }
  unsigned bar = 0;
  lds_wait_ge(L, L_FAUX, 1); lds_wait_ge(L, L_FAUX + 4, 1);
  asm volatile("s_waitcnt vmcnt(0)" : "+v"(warm) :: "memory");
  const unsigned xh = *(const LAS u16*)(L + L_X + ((piece >> 2) * D + kq * 128 + 16 * w + 4 * (piece & 3) + comp) * 2);
  {
    const v2u cr = *(const LAS v2u*)(L + L_C + tid * 8);
    float s[4] = {lo2f(cr.x), hi2f(cr.x), lo2f(cr.y), hi2f(cr.y)};
#pragma unroll
    for (int i = 0; i < 4; ++i) s[i] = s[i] * rcp_(1.f + exp_(-s[i]));
    *(LAS v2u*)(L + L_SVEC + tid * 8) = v2u{pk(s[0], s[1]), pk(s[2], s[3])};
  }
  {
    const v2u xr = *(const LAS v2u*)(L + L_X + tid * 8);
    const float a0 = lo2f(xr.x), a1 = hi2f(xr.x), a2 = lo2f(xr.y), a3 = hi2f(xr.y);
    const f32x2 sq = swap32((a0 + a1) + (a2 + a3), (a0 * a0 + a1 * a1) + (a2 * a2 + a3 * a3));
    float r = sum16(sq.x + sq.y);
    r += dpp<ROR8>(r); r += dpp<ROR4>(r); r += dpp<XOR2>(r); r += dpp<XOR1>(r);
    if ((lane & 31) == 0) *(LAS float*)(L + L_ST1 + w * 8 + (lane >> 5) * 4) = r;
  }
  cbar(L, lane, bar);
  float lnx;
  {
    const int b = piece >> 2;
    const f32x4 r0 = *(const LAS f32x4*)(L + L_ST1 + b * 32), r1 = *(const LAS f32x4*)(L + L_ST1 + b * 32 + 16);
    float m, rden;
    ln_consts((r0.x + r0.z) + (r1.x + r1.z), (r0.y + r0.w) + (r1.y + r1.w), m, rden);
    lnx = rbf(rbf(lo2f(xh) - m) * rden);
    PIN(lnx);
  }
  const int tl = (8 * (lane >> 4) + ((lane >> 2) & 3)) * 256 + (((2 * w + ((lane & 3) >> 1)) ^ (((lane >> 2) & 3) << 2)) << 4) + (lane & 1) * 8;
  const int pubo = w * 128 + (lane & 15) * 64 + (lane >> 4) * 16;

  if (!isA) {
    f32x4 acc = {0.f, 0.f, 0.f, 0.f};
#pragma unroll
    for (int s = 0; s < 4; ++s) { wait_landed(L, s); acc = mfma(tfrag(L, s, tl), sfrag(L, lane, kq * 128 + 32 * s), acc); }
    release(L, w, lane, 4);
    if ((lane & 15) < 2) *(LAS f32x4*)(L + L_STG + pubo) = acc;
    cbar(L, lane, bar);
    if (w == 0) __builtin_amdgcn_raw_buffer_store_b128(*(const LAS v4u*)(L + L_STG + lane * 16), rs, mybox + BX_MC + lane * 16, 0, AUX_SC1);
  }
  v4u qw[4];
#pragma unroll
  for (int s = 0; s < 4; ++s) { wait_landed(L, offQ + s); qw[s] = tfrag(L, (offQ + s) & 15, tl); }
  release(L, w, lane, offMR);
#pragma unroll 1
  for (int u = 0; u < 2; ++u) {
    const int n = offMR + 2 * u, k0 = ((mu0 + u) & 15) * 64;
    wait_landed(L, n); wait_landed(L, n + 1);
    f32x4 acc = mfma(tfrag(L, n & 15, tl), sfrag(L, lane, k0), f32x4{0.f, 0.f, 0.f, 0.f});
    acc = mfma(tfrag(L, (n + 1) & 15, tl), sfrag(L, lane, k0 + 32), acc);
    release(L, w, lane, n + 2 < offK ? n + 2 : offK + w);
    if ((lane & 15) < 2) *(LAS f32x4*)(L + L_STG + 2048 + u * 1024 + pubo) = acc;
  }
  v4u wf[4];
  if (isA) {
#pragma unroll
    for (int q = 0; q < 4; ++q) { wait_landed(L, offWO + q); wf[q] = tfrag(L, offWO + q, tl); }
    release(L, w, lane, offK + w);
  }
  {
    lds_wait_ge(L, L_FALIVE, 1);
    v4u g[2];
    const int js = (16 + (kq & 1) * 8 + sg) * 8 + (kq >> 1);
    const int off[2] = {box(js) + BX_MC + w * 128 + piece * 16, box(js + 4) + BX_MC + w * 128 + piece * 16};
    gather<2>(g, off, rs);
    const float sa = rbf(tred(asf(g[0]))), ca = rbf(tred(asf(g[1])));
    const float h = rbf(lnx * rbf(1.f + ca)) + sa;
    if (!(lane & 8)) *(LAS u16*)(L + L_HVEC + (piece >> 2) * VROW + (16 * w + 4 * (piece & 3) + comp) * 2) = (u16)pk(h, h);
    cbar(L, lane, bar);
    if (w >= 1 && w <= 2)
      __builtin_amdgcn_raw_buffer_store_b128(*(const LAS v4u*)(L + L_STG + 1024 + w * 1024 + lane * 16), rs, mybox + BX_MR + (w - 1) * 1024 + lane * 16, 0, AUX_SC1);
    f32x4 acc = {0.f, 0.f, 0.f, 0.f};
#pragma unroll
    for (int s = 0; s < 4; ++s) acc = mfma(qw[s], vfrag(L, L_HVEC, lane, s), acc);
    if ((lane & 15) < 2) *(LAS f32x4*)(L + L_STG + 1024 + (lane & 15) * 512 + (16 * w + 4 * (lane >> 4)) * 4) = acc;
    cbar(L, lane, bar);
    if (w == 0) __builtin_amdgcn_raw_buffer_store_b128(*(const LAS v4u*)(L + L_STG + 1024 + lane * 16), rs, mybox + BX_Q + lane * 16, 0, AUX_SC1);
  }
  v4u qf[4], tw[8], tx[8];
  const int kl = (8 * ((lane & 15) >> 2) + (lane & 3)) * 256 + ((lane & 3) << 6) + (lane >> 4) * 16;
  {
    wait_landed(L, offK + w);
    kfrags(L, (offK + w) & 15, kl, tw);
    release(L, w, lane, offK + w + 8);
    wait_landed(L, offK + w + 8);
    kfrags(L, (offK + w + 8) & 15, kl, tx);
    release(L, w, lane, offK + w + 16);
    v4u g[4]; int off[4];
    const int head = hkv * 8 + w;
#pragma unroll
    for (int q = 0; q < 4; ++q) {
      const int jq = ((head & 3) * 8 + 2 * q + (lane >> 5)) * 8 + (head >> 2);
      off[q] = box(jq) + BX_Q + batt * 512 + (lane & 31) * 16;
    }
    gather<4, 8>(g, off, rs);
    const f32x4 t = sum4(g);
    const f32x2 a = swap32(t.x, t.y), b = swap32(t.z, t.w);
    const unsigned p = pk(a.x + a.y, b.x + b.y);
    LAS u16* qv = (LAS u16*)(L + L_QVEC + w * 256 + (lane & 31) * 8 + (lane >> 5) * 2);
    qv[0] = (u16)p; qv[2] = (u16)(p >> 16);
    cbar(L, lane, bar);
#pragma unroll
    for (int s = 0; s < 4; ++s) qf[s] = *(const LAS v4u*)(L + L_QVEC + (lane & 7) * 256 + s * 64 + (lane >> 4) * 16);
  }
  float sc[4][8];
#pragma unroll
  for (int i = 0; i < 4; ++i) {
    v4u kf[8];
    if (i < 2) {
#pragma unroll
      for (int q = 0; q < 8; ++q) kf[q] = i == 0 ? tw[q] : tx[q];
    } else {
      const int n = offK + w + 8 * i;
      wait_landed(L, n);
      kfrags(L, n & 15, kl, kf);
      release(L, w, lane, i < 3 ? n + 8 : offV);
    }
#pragma unroll
    for (int T = 0; T < 2; ++T) {
      f32x4 a = {0.f, 0.f, 0.f, 0.f};
#pragma unroll
      for (int s = 0; s < 4; ++s) a = mfma(kf[T * 4 + s], qf[s], a);
      const f32x2 lo = rbf2(rbf2(f32x2{a.x, a.y}) * 0.08838834764831845f), hi = rbf2(rbf2(f32x2{a.z, a.w}) * 0.08838834764831845f);
      sc[i][T * 4] = lo.x; sc[i][T * 4 + 1] = lo.y; sc[i][T * 4 + 2] = hi.x; sc[i][T * 4 + 3] = hi.y;
    }
  }
  float mw = sc[0][0];
  {
#pragma unroll
    for (int i = 0; i < 4; ++i)
#pragma unroll
      for (int r = 0; r < 8; ++r) mw = fmaxf(mw, sc[i][r]);
    mw = max32(max16(mw));
    float lw = 0.f;
#pragma unroll
    for (int i = 0; i < 4; ++i)
#pragma unroll
      for (int r = 0; r < 8; ++r) { sc[i][r] = exp_(sc[i][r] - mw); lw += sc[i][r]; }
    lw = sum32(sum16(lw));
    if (lane < 8) *(LAS f32x2*)(L + L_MLW + w * 64 + lane * 8) = f32x2{mw, lw};
    lds_inc(L, L_MLCNT, lane);
  }
#pragma unroll
  for (int q = 0; q < VPARK; ++q) {
    wait_landed(L, offV + q);
    if (q < 8) tw[q & 7] = tfrag(L, (offV + q) & 15, tl); else tx[q & 7] = tfrag(L, (offV + q) & 15, tl);
    release(L, w, lane, offV + q + 1);
  }
  lds_wait_ge(L, L_FML, 1);
  {
    const f32x2 ml = *(const LAS f32x2*)(L + L_MLF + (lane & 7) * 8);
    const float f = exp_(mw - ml.x) * ml.y;
#pragma unroll
    for (int i = 0; i < 4; ++i) {
      const v4u p = {pk(sc[i][0] * f, sc[i][1] * f), pk(sc[i][2] * f, sc[i][3] * f), pk(sc[i][4] * f, sc[i][5] * f), pk(sc[i][6] * f, sc[i][7] * f)};
      if ((lane & 15) < 8) *(LAS v4u*)(L + L_P + (w + 8 * i) * 512 + ((lane >> 4) * 8 + (lane & 7)) * 16) = p;
    }
    cbar(L, lane, bar);
  }
  f32x4 oacc = {0.f, 0.f, 0.f, 0.f};
  const int pl = L_P + ((lane >> 4) * 8 + (lane & 7)) * 16;
#pragma unroll
  for (int i = 0; i < VPARK; ++i) oacc = mfma(i < 8 ? tw[i & 7] : tx[i & 7], *(const LAS v4u*)(L + pl + i * 512), oacc);
#pragma unroll 1
  for (int i = VPARK; i < 32; i += 2) {
    const int n = offV + i;
    wait_landed(L, n + 1); wait_landed(L, n);
    const v4u a0 = tfrag(L, n & 15, tl), a1 = tfrag(L, (n + 1) & 15, tl);
    const v4u p0 = *(const LAS v4u*)(L + pl + i * 512), p1 = *(const LAS v4u*)(L + pl + (i + 1) * 512);
    release(L, w, lane, n + 2);
    oacc = mfma(a0, p0, oacc);
    oacc = mfma(a1, p1, oacc);
  }
  if ((lane & 15) < 8) __builtin_amdgcn_raw_buffer_store_b128(asu(oacc), rs, mybox + BX_O + w * 512 + ((lane >> 4) * 8 + (lane & 15)) * 16, 0, AUX_SC1);

  if (isA) {
    v4u g[4]; int off[4];
#pragma unroll
    for (int q = 0; q < 4; ++q) off[q] = box((sg + 8 * q) * 8 + (w >> 2) * 4 + a_hkv) + BX_O + a_dt * 512 + (w & 3) * 128 + piece * 16;
#pragma unroll
    for (int q = 0; q < 4; ++q) { wait_landed(L, offW + q); tw[q] = tfrag(L, (offW + q) & 15, tl); }
    __builtin_amdgcn_s_sleep(4);
    gather<4>(g, off, rs);
    const float t = tred(sum4(g));
    *(LAS u16*)(L + L_TVEC + (w >> 2) * VROW + (16 * piece + 4 * (w & 3) + comp) * 2) = (u16)pk(t, t);
    WGBAR();
    tile_pair(L, L_TVEC, wf, tw, rs, mybox + BX_T1 + w * 128, lane);
  } else if (isC) {
    if (w < 4) {
      const int col = 64 * ci + 16 * w + 4 * (piece & 3) + comp;
      const float xres = lo2f(*(const GAS u16*)(x + (piece >> 2) * D + col)), bias = lo2f(*(const GAS u16*)(b2 + col));
      lds_wait_ge(L, L_FGATE, 1);
      const float ga = *(const LAS float*)(L + L_GATE + w * 256 + lane * 4), gb = *(const LAS float*)(L + L_GATE + (4 + w) * 256 + lane * 4);
      const int ct = 4 * ci + w, cg = ct >> 4, ti = ct & 15;
      v4u g[4]; int off[4], off2[4];
#pragma unroll
      for (int q = 0; q < 4; ++q) {
        const int a = (sg + 8 * q) * 4 + cg;
        off[q] = box((a & 15) * 8 + (a >> 4)) + BX_T1 + ti * 128 + piece * 16;
        const int kf = sg + 8 * q, bb = kf * 4 + cg, gg = bb / 14;
        off2[q] = kf < 28 ? box((16 + bb - gg * 14) * 8 + gg) + BX_T2 + ti * 128 + piece * 16 : WS_BYTES;
        PIN(off2[q]);
      }
      gather<4>(g, off, rs);
      const float x1 = rbf(xres + rbf(ga * rbf(tred(sum4(g)))));
      const f32x4 x4 = texp(x1);
      __builtin_amdgcn_raw_buffer_store_b128(asu(x4), rs, mybox + BX_X1 + sg * 512 + w * 128 + piece * 16, 0, AUX_SC1);
      gather<4>(g, off2, rs);
      const float o = x1 + rbf(gb * rbf(rbf(tred(sum4(g))) + bias));
      if (!(lane & 8)) *(GAS u16*)(out + (piece >> 2) * D + col) = (u16)pk(o, o);
    }
  } else {
    f32x4 sbv, cb1;
    float bias;
    {
      v4u g2[2];
      const int off[2] = {box(30 * 8 + (w >> 1) + (w & 1) * 8) + BX_T1 + lane * 16, box(30 * 8 + 4 + (w >> 1) + (w & 1) * 8) + BX_T1 + lane * 16};
      gather<2>(g2, off, rs);
      sbv = asf(g2[0]);
      const f32x4 cb = asf(g2[1]);
      const f32x2 c0 = rbf2(f32x2{cb.x, cb.y} + 1.f), c1 = rbf2(f32x2{cb.z, cb.w} + 1.f);
      cb1 = f32x4{c0.x, c0.y, c1.x, c1.y};
      bias = lo2f(*(const GAS u16*)(b1 + (bi >> 2) * 128 + 16 * w + 4 * (piece & 3) + comp));
      PIN(bias); PIN(sbv); PIN(cb1);
    }
#pragma unroll
    for (int q = 0; q < 8; ++q) { wait_landed(L, offW + q); tw[q] = tfrag(L, (offW + q) & 15, tl); }
    const int cc = 2 * w + (sg >> 2);
    const int offx[1] = {box((30 + (cc & 1)) * 8 + (cc >> 1)) + BX_X1 + grp * 512 + (sg & 3) * 128 + piece * 16};
    const int kf = bi >> 2, bb = (kf >> 1) * 8 + sg, gg = bb / 14;
    int offh[1] = {box((16 + bb - gg * 14) * 8 + gg) + BX_T1 + ((kf & 1) * 8 + w) * 128 + piece * 16};
    PIN(offh[0]);
    wait_landed(L, offW + 14); wait_landed(L, offW + 15);
    v4u g[1];
    gather<1>(g, offx, rs);
    const f32x4 x1 = asf(g[0]);
    float m, rden;
    stat_put(L, L_ST1, w, lane, x1);
    WGBAR();
    stat_get(L, L_ST1, lane, m, rden);
    const f32x2 ha = rbf2(rbf2(ln2(f32x2{x1.x, x1.y}, m, rden) * f32x2{cb1.x, cb1.y}) + f32x2{sbv.x, sbv.y});
    const f32x2 hb = rbf2(rbf2(ln2(f32x2{x1.z, x1.w}, m, rden) * f32x2{cb1.z, cb1.w}) + f32x2{sbv.z, sbv.w});
    stat_put(L, L_ST2, w, lane, f32x4{ha.x, ha.y, hb.x, hb.y});
    WGBAR();
    if (w == (bi & 7)) {
      stat_get(L, L_ST2, lane, m, rden);
      const f32x2 ya = rbf2(ha - m) * rden, yb = rbf2(hb - m) * rden;
      *(LAS v2u*)(L + L_TVEC + (piece >> 2) * VROW + (16 * sg + 4 * (piece & 3)) * 2) = v2u{pk(ya.x, ya.y), pk(yb.x, yb.y)};
    }
    WGBAR();
    tile_pair(L, L_TVEC, tw, tw + 4, rs, mybox + BX_T1 + w * 128, lane);
#pragma unroll
    for (int q = 0; q < 8; ++q) { wait_landed(L, offW + 8 + q); tw[q] = tfrag(L, (offW + 8 + q) & 15, tl); }
    __builtin_amdgcn_s_sleep(5);
    gather<1>(g, offh, rs);
    const float u = rbf(rbf(tred(asf(g[0]))) + bias);
    const float z = u * (0.7978845608028654f + 0.035677408136300125f * u * u);
    const float hh = u * rcp_(1.f + __builtin_amdgcn_exp2f(-2.8853900817779268f * z));
    *(LAS u16*)(L + L_UVEC + (piece >> 2) * VROW + (16 * w + 4 * (piece & 3) + comp) * 2) = (u16)pk(hh, hh);
    WGBAR();
    tile_pair(L, L_UVEC, tw, tw + 4, rs, mybox + BX_T2 + w * 128, lane);
  }
}

}

extern "C" void kernel_launch(void* const* d_in, const int* in_sizes, int n_in, void* d_out, int out_size, void* d_ws, size_t ws_size,
                              hipStream_t stream) {
  (void)in_sizes; (void)n_in; (void)out_size; (void)ws_size;
  (void)hipFuncSetAttribute((const void*)fused_block, hipFuncAttributeMaxDynamicSharedMemorySize, LDS_BYTES);
  hipLaunchKernelGGL(fused_block, dim3(256), dim3(NTHR), LDS_BYTES, stream, (const u16*)d_in[0], (const u16*)d_in[1], (const u16*)d_in[2],
                     (const u16*)d_in[3], (const u16*)d_in[4], (const u16*)d_in[6], (const u16*)d_in[7], (const u16*)d_in[8],
                     (const u16*)d_in[9], (const u16*)d_in[10], (const u16*)d_in[11], (u16*)d_out, (unsigned char*)d_ws);
}
```

```cpp
#include <hip/hip_runtime.h>
#include <hip/hip_bf16.h>
#include <stdint.h>

#define LAS __attribute__((address_space(3)))
#define GAS __attribute__((address_space(1)))
typedef unsigned short u16;
typedef unsigned v4u __attribute__((ext_vector_type(4)));
typedef unsigned v2u __attribute__((ext_vector_type(2)));
typedef float f32x4 __attribute__((ext_vector_type(4)));
typedef float f32x2 __attribute__((ext_vector_type(2)));
typedef short s16x4 __attribute__((ext_vector_type(4)));
typedef short bf16x8 __attribute__((ext_vector_type(8)));
typedef __bf16 bf16x2 __attribute__((ext_vector_type(2)));

namespace {

constexpr int D = 1024, SK = 32768;
constexpr int NTHR = 704;
constexpr int W_PRO = 18, W_KV = 12, W_WO = 16, W_MLP = 10;
constexpr int VPARK = 12;

constexpr int WS_EPOCH = 0, WS_ALIVE = 4096, WS_BOX = 65536, BOX = 16384;
constexpr int BX_MC = 0;
constexpr int BX_Q = 1024;
constexpr int BX_ML = 2048;
constexpr int BX_MR = 3072;
constexpr int BX_O = 6144;
constexpr int BX_T1 = 10240;
constexpr int BX_X1 = BX_T1 + 1024;
constexpr int BX_T2 = 12288;
constexpr int WS_BYTES = WS_BOX + 256 * BOX;
constexpr unsigned SENT = 0xFFFFFFFFu;
constexpr unsigned SPIN_CAP = 1u << 15, LDS_CAP = 1u << 20;
constexpr int AUX_SC1 = 16;

constexpr int L_RING = 0, L_P = 131072, L_CTL = L_P + 16384;
constexpr int L_X = L_P, L_C = L_P + 4096, L_STG = L_P + 8192;
constexpr int L_LANDED = L_CTL;
constexpr int L_PROG = L_CTL + 32;
constexpr int L_BAR = L_CTL + 64, L_MLCNT = L_CTL + 68, L_FALIVE = L_CTL + 72, L_FML = L_CTL + 76, L_FAUX = L_CTL + 80, L_FGATE = L_CTL + 88, L_FCLR = L_CTL + 92;
constexpr int L_SVEC = L_CTL + 256;
constexpr int VROW = 320;
constexpr int L_HVEC = L_SVEC + 4096;
constexpr int L_QVEC = L_HVEC + 640;
constexpr int L_ST1 = L_QVEC + 2048;
constexpr int L_ST2 = L_ST1 + 128;
constexpr int L_MLW = L_ST2 + 128;
constexpr int L_MLF = L_MLW + 512;
constexpr int L_TVEC = L_MLF + 64;
constexpr int L_UVEC = L_TVEC + 640;
constexpr int L_SBST = L_TVEC;
constexpr int L_GATE = L_UVEC + 640;
constexpr int LDS_BYTES = L_GATE + 2048;
static_assert(LDS_BYTES <= 160 * 1024 && LDS_BYTES > 80 * 1024, "one workgroup per CU");

typedef LAS unsigned char* lptr;

__device__ __forceinline__ float lo2f(unsigned w) { return __uint_as_float(w << 16); }
__device__ __forceinline__ float hi2f(unsigned w) { return __uint_as_float(w & 0xffff0000u); }
__device__ __forceinline__ unsigned pk(float a, float b) {
  bf16x2 v = {(__bf16)a, (__bf16)b};
  return __builtin_bit_cast(unsigned, v);
}
__device__ __forceinline__ float rbf(float a) { return hi2f(pk(a, a)); }
__device__ __forceinline__ f32x2 rbf2(f32x2 a) {
  unsigned p = pk(a.x, a.y);
  asm("" : "+v"(p));
  return f32x2{lo2f(p), hi2f(p)};
}
__device__ __forceinline__ f32x4 mfma(v4u a, v4u b, f32x4 c) {
  return __builtin_amdgcn_mfma_f32_16x16x32_bf16(__builtin_bit_cast(bf16x8, a), __builtin_bit_cast(bf16x8, b), c, 0, 0, 0);
}
template <int CTRL> __device__ __forceinline__ float dpp(float x) {
  return __builtin_bit_cast(float, __builtin_amdgcn_mov_dpp(__builtin_bit_cast(int, x), CTRL, 0xf, 0xf, true));
}
constexpr int XOR1 = 0xB1, XOR2 = 0x4E, ROR4 = 0x124, ROR8 = 0x128;
__device__ __forceinline__ f32x2 swap32(float a, float b) {
  auto s = __builtin_amdgcn_permlane32_swap(__float_as_uint(a), __float_as_uint(b), false, false);
  return f32x2{__uint_as_float(s[0]), __uint_as_float(s[1])};
}
__device__ __forceinline__ f32x2 swap16(float a, float b) {
  auto s = __builtin_amdgcn_permlane16_swap(__float_as_uint(a), __float_as_uint(b), false, false);
  return f32x2{__uint_as_float(s[0]), __uint_as_float(s[1])};
}
__device__ __forceinline__ float sum16(float x) { const f32x2 s = swap16(x, x); return s.x + s.y; }
__device__ __forceinline__ float sum32(float x) { const f32x2 s = swap32(x, x); return s.x + s.y; }
__device__ __forceinline__ float max16(float x) { const f32x2 s = swap16(x, x); return fmaxf(s.x, s.y); }
__device__ __forceinline__ float max32(float x) { const f32x2 s = swap32(x, x); return fmaxf(s.x, s.y); }
__device__ __forceinline__ float tred(f32x4 v) {
  const f32x2 a = swap32(v.x, v.y), b = swap32(v.z, v.w);
  const f32x2 c = swap16(a.x + a.y, b.x + b.y);
  const float t = c.x + c.y;
  return t + dpp<ROR8>(t);
}
__device__ __forceinline__ int tcomp(int lane) { return ((lane >> 4) & 1) * 2 + (lane >> 5); }
__device__ __forceinline__ f32x4 texp(float t) {
  const f32x2 a = swap16(t, t);
  const f32x2 b = swap32(a.x, a.x), c = swap32(a.y, a.y);
  return f32x4{b.x, b.y, c.x, c.y};
}
__device__ __forceinline__ f32x4 asf(v4u v) { return __builtin_bit_cast(f32x4, v); }
__device__ __forceinline__ v4u asu(f32x4 v) { return __builtin_bit_cast(v4u, v); }
__device__ __forceinline__ unsigned umax4(v4u v) { return max(max(max(v.x, v.y), v.z), v.w); }
__device__ __forceinline__ float exp_(float x) { return __builtin_amdgcn_exp2f(x * 1.4426950408889634f); }
__device__ __forceinline__ float rcp_(float x) { return __builtin_amdgcn_rcpf(x); }

#define LGKM0() asm volatile("s_waitcnt lgkmcnt(0)" ::: "memory")
#define VM0() asm volatile("s_waitcnt vmcnt(0)" ::: "memory")
#define CFENCE() asm volatile("" ::: "memory")
#define PIN(x) asm volatile("" : "+v"(x))
#define WGBAR() do { LGKM0(); __builtin_amdgcn_s_barrier(); CFENCE(); } while (0)

__device__ __forceinline__ void dma16(unsigned voff, const unsigned char* sbase, unsigned ldsaddr) {
  unsigned keep;
  asm volatile("s_mov_b32 %0, m0\n\ts_mov_b32 m0, %3\n\ts_nop 4\n\tglobal_load_lds_dwordx4 %1, %2 nt\n\ts_mov_b32 m0, %0"
               : "=&s"(keep) : "v"(voff), "s"(sbase), "s"(ldsaddr) : "memory");
}
__device__ __forceinline__ void lds_set(lptr L, int off, int lane, unsigned v) {
  if (lane == 0) *(volatile LAS unsigned*)(L + off) = v;
}
__device__ __forceinline__ void lds_wait_ge(lptr L, int off, unsigned need) {
#pragma nounroll
  for (unsigned sp = 0; (unsigned)__builtin_amdgcn_readfirstlane(*(volatile LAS unsigned*)(L + off)) < need && sp < LDS_CAP; ++sp) { }
  CFENCE();
}
__device__ __forceinline__ void wait_landed(lptr L, int n) { lds_wait_ge(L, L_LANDED + 4 * (n & 1), (unsigned)(n >> 1) + 1u); }
__device__ __forceinline__ void release(lptr L, int w, int lane, int n) { LGKM0(); lds_set(L, L_PROG + 4 * w, lane, (unsigned)n); }
__device__ __forceinline__ void lds_inc(lptr L, int off, int lane) {
  LGKM0();
  if (lane == 0) __hip_atomic_fetch_add((LAS unsigned*)(L + off), 1u, __ATOMIC_RELAXED, __HIP_MEMORY_SCOPE_WORKGROUP);
}
__device__ __forceinline__ void cbar(lptr L, int lane, unsigned& target) {
  target += 8;
  lds_inc(L, L_BAR, lane);
  lds_wait_ge(L, L_BAR, target);
}
__device__ __forceinline__ v4u tfrag(lptr L, int slot, int tl) {
  const s16x4 a = __builtin_amdgcn_ds_read_tr16_b64_v4i16((LAS s16x4*)(L + L_RING + slot * 8192 + tl));
  const s16x4 b = __builtin_amdgcn_ds_read_tr16_b64_v4i16((LAS s16x4*)(L + L_RING + slot * 8192 + tl + 1024));
  const v2u x = __builtin_bit_cast(v2u, a), y = __builtin_bit_cast(v2u, b);
  return v4u{x.x, x.y, y.x, y.y};
}
__device__ __forceinline__ void kfrags(lptr L, int slot, int kl, v4u (&f)[8]) {
#pragma unroll
  for (int q = 0; q < 8; ++q) f[q] = *(const LAS v4u*)(L + L_RING + slot * 8192 + (q >> 2) * 1024 + (kl ^ ((q & 3) << 6)));
}
__device__ __forceinline__ v4u sfrag(lptr L, int lane, int k0) {
  return *(const LAS v4u*)(L + L_SVEC + (lane & 1) * 2048 + k0 * 2 + (lane >> 4) * 16);
}
__device__ __forceinline__ v4u vfrag(lptr L, int off, int lane, int s) {
  return *(const LAS v4u*)(L + off + (lane & 1) * VROW + s * 64 + (lane >> 4) * 16);
}
template <int N>
__device__ __forceinline__ void gather_issue(v4u (&v)[N], const int (&off)[N], __amdgpu_buffer_rsrc_t rs) {
#pragma unroll
  for (int q = 0; q < N; ++q) v[q] = __builtin_amdgcn_raw_buffer_load_b128(rs, off[q], 0, AUX_SC1);
}
template <int N, int PAUSE>
__device__ __forceinline__ void gather_finish(v4u (&v)[N], const int (&off)[N], __amdgpu_buffer_rsrc_t rs) {
#pragma nounroll
  for (unsigned sp = 0; sp < SPIN_CAP; ++sp) {
    unsigned mx = umax4(v[0]);
#pragma unroll
    for (int q = 1; q < N; ++q) mx = max(mx, umax4(v[q]));
    if (__builtin_amdgcn_ballot_w64(mx == SENT) == 0) break;
    if (PAUSE) __builtin_amdgcn_s_sleep(PAUSE);
    CFENCE();
    if (mx == SENT) gather_issue<N>(v, off, rs);
  }
}
template <int N, int PAUSE = 0>
__device__ __forceinline__ void gather(v4u (&v)[N], const int (&off)[N], __amdgpu_buffer_rsrc_t rs) {
  gather_issue<N>(v, off, rs);
  gather_finish<N, PAUSE>(v, off, rs);
}
__device__ __forceinline__ f32x4 sum4(const v4u (&g)[4]) { return (asf(g[0]) + asf(g[1])) + (asf(g[2]) + asf(g[3])); }
__device__ __forceinline__ void tile_pair(lptr L, int vec, const v4u* f0, const v4u* f1, __amdgpu_buffer_rsrc_t rs, int off, int lane) {
  f32x4 a0 = {0.f, 0.f, 0.f, 0.f}, a1 = {0.f, 0.f, 0.f, 0.f};
#pragma unroll
  for (int s = 0; s < 4; ++s) { const v4u bv = vfrag(L, vec, lane, s); a0 = mfma(f0[s], bv, a0); a1 = mfma(f1[s], bv, a1); }
  if ((lane & 15) < 2) {
    __builtin_amdgcn_raw_buffer_store_b128(asu(a0), rs, off + (lane & 1) * 64 + (lane >> 4) * 16, 0, AUX_SC1);
    __builtin_amdgcn_raw_buffer_store_b128(asu(a1), rs, off + 1024 + (lane & 1) * 64 + (lane >> 4) * 16, 0, AUX_SC1);
  }
}
__device__ __forceinline__ int box(int j) { return WS_BOX + j * BOX; }
__device__ __forceinline__ void ln_consts(float s, float q, float& m, float& rden) {
  const float mean = s * (1.f / D);
  const float var = fmaxf(q * (1.f / D) - mean * mean, 0.f);
  m = rbf(mean);
  rden = rcp_(rbf(__builtin_amdgcn_sqrtf(rbf(rbf(var) + 1e-5f))));
}
__device__ __forceinline__ f32x2 ln2(f32x2 x, float m, float rden) { return rbf2(rbf2(x - m) * rden); }
__device__ __forceinline__ void stat_put(lptr L, int off, int w, int lane, f32x4 v) {
  const float s = (v.x + v.y) + (v.z + v.w), q = (v.x * v.x + v.y * v.y) + (v.z * v.z + v.w * v.w);
  const f32x2 a = swap32(s, q);
  float r = sum16(a.x + a.y);
  r += dpp<ROR8>(r); r += dpp<XOR2>(r); r += dpp<XOR1>(r);
  if ((lane & 0x1B) == 0) *(LAS float*)(L + off + w * 16 + (lane & 4) * 2 + (lane >> 5) * 4) = r;
}
__device__ __forceinline__ void stat_get(lptr L, int off, int lane, float& m, float& rden) {
  const f32x2 p = *(const LAS f32x2*)(L + off + (lane >> 3) * 16 + (lane & 4) * 2);
  const f32x2 a = swap32(p.x, p.y);
  float r = sum16(a.x + a.y);
  r += dpp<ROR8>(r);
  const f32x2 t = swap32(r, r);
  ln_consts(t.x, t.y, m, rden);
}

__device__ __forceinline__ int read_minprog(lptr L, int lane) {
  unsigned m = *(volatile LAS unsigned*)(L + L_PROG + 4 * (lane & 7));
  m = min(m, (unsigned)__builtin_amdgcn_mov_dpp((int)m, XOR1, 0xf, 0xf, true));
  m = min(m, (unsigned)__builtin_amdgcn_mov_dpp((int)m, XOR2, 0xf, 0xf, true));
  m = min(m, (unsigned)__builtin_amdgcn_mov_dpp((int)m, ROR4, 0xf, 0xf, true));
  return __builtin_amdgcn_readfirstlane((int)m);
}
template <int W>
__device__ __forceinline__ void load_block(lptr L, int ld, int lane, int n, const unsigned char* bp, int ldb, int hi, int& minprog) {
  if (minprog < n - 15) {
    minprog = read_minprog(L, lane);
    if (minprog < n - 15) {
      VM0();
      lds_set(L, L_LANDED + 4 * ld, lane, (unsigned)(n >> 1));
      for (unsigned sp = 0; sp < LDS_CAP; ++sp) {
        minprog = read_minprog(L, lane);
        if (minprog >= n - 15) break;
      }
    }
  }
  const unsigned voff = (unsigned)((lane >> 4) * ldb + (((lane & 15) ^ ((lane >> 4) << 2)) << 4));
  const unsigned dst = (unsigned)(size_t)L + L_RING + (unsigned)(n & 15) * 8192u;
#pragma unroll
  for (int p = 0; p < 8; ++p) {
    dma16(voff, bp + (size_t)((p & 3) * 4) * ldb + (size_t)(p >> 2) * hi, dst + p * 1024);
    asm volatile("s_waitcnt vmcnt(%0)" ::"i"(W - 1) : "memory");
    if (((p + 2 - W) & 7) == 0) {
      const int done = (n >> 1) + (p + 2 - W) / 8;
      if (done > 0) lds_set(L, L_LANDED + 4 * ld, lane, (unsigned)done);
    }
  }
}

__global__ void __launch_bounds__(NTHR) fused_block(const u16* __restrict__ x, const u16* __restrict__ kc, const u16* __restrict__ vc,
                                                     const u16* __restrict__ wq, const u16* __restrict__ wo, const u16* __restrict__ w1,
                                                     const u16* __restrict__ b1, const u16* __restrict__ w2, const u16* __restrict__ b2,
                                                     const u16* __restrict__ cvec, const u16* __restrict__ wmod, u16* __restrict__ out,
                                                     unsigned char* ws) {
  extern __shared__ __attribute__((aligned(16))) unsigned char smem[];
  lptr L = (lptr)smem;
  const int tid = threadIdx.x, lane = tid & 63, w = __builtin_amdgcn_readfirstlane(tid >> 6);
  const int j = blockIdx.x, grp = j & 7, split = j >> 3, batt = grp >> 2, hkv = grp & 3;
  const bool isA = split < 16, isC = split >= 30, isB = !isA && !isC;
  const int kq = split & 7, g32 = grp * 4 + (split >> 3), g16 = grp * 2 + ((split >> 3) & 1);
  const int mu0 = isA ? 2 * (grp * 16 + split) : 256 + 2 * (grp * 16 + split - 16);
  const int offQ = isA ? 0 : 4, offMR = offQ + 4;
  constexpr int offWO = 8, offK = 12, offV = offK + 32, offW = offV + 32;
  const int ai = grp * 16 + split, a_hkv = ai >> 5, a_dt = (ai >> 2) & 7, a_cg = ai & 3;
  const int bi = grp * 14 + (split - 16), ci = grp * 2 + (split - 30);

  if (tid < 64) *(volatile LAS unsigned*)(L + L_CTL + 4 * tid) = 0u;
  WGBAR();
  unsigned long long epoch = 0, tag = 0;
  if (w == 10) {
    const __amdgpu_buffer_rsrc_t rsh = __builtin_amdgcn_make_buffer_rsrc((void*)ws, 0, WS_BYTES, 0x00020000);
    const v2u ep = __builtin_amdgcn_raw_buffer_load_b64(rsh, WS_EPOCH, 0, AUX_SC1);
    const v4u s4 = {SENT, SENT, SENT, SENT};
    const unsigned used = isA ? 0x0FDEu : (isB ? 0x3FDFu : 0x7FDFu);
#pragma unroll 1
    for (int u = 0; u < 15; ++u)
      if ((used >> u) & 1) __builtin_amdgcn_raw_buffer_store_b128(s4, rsh, box(j) + u * 1024 + lane * 16, 0, AUX_SC1);
    VM0();
    lds_set(L, L_FCLR, lane, 1u);
    epoch = ((unsigned long long)(unsigned)__builtin_amdgcn_readfirstlane((int)ep.y) << 32) | (unsigned)__builtin_amdgcn_readfirstlane((int)ep.x);
    tag = (epoch ^ 0x9E3779B97F4A7C15ull) * 0xD6E8FEB86659FD93ull + 1ull;
    if (lane == 0) __hip_atomic_store((GAS unsigned long long*)(ws + WS_ALIVE + j * 16), tag, __ATOMIC_RELAXED, __HIP_MEMORY_SCOPE_AGENT);
  }
  if (__builtin_expect(w == 8 || w == 9, 1)) {
    const int ld = w - 8;
    __builtin_amdgcn_s_setprio(3);
    int minprog = 0;
    const unsigned char* aux = (const unsigned char*)(ld ? cvec : x);
#pragma unroll
    for (int p = 0; p < 4; ++p) dma16((unsigned)lane * 16u, aux + p * 1024, (unsigned)(size_t)L + L_X + ld * 4096 + p * 1024);
    const size_t kvoff = ((size_t)grp * SK + split * 1024) * 256;
    const unsigned char* wo_t = (const unsigned char*)wo + ((size_t)(a_hkv * 8) * 128 + 16 * a_dt) * 2048 + a_cg * 512;
#pragma unroll 1
    for (int n = ld; n < offW; n += 2) {
      if (n < offK) {
        const unsigned char* bp; int ldb, hi;
        if (isA && n >= offWO) { bp = wo_t + (size_t)(n - offWO) * 256 * 2048; ldb = 2048; hi = 128 * 2048; }
        else {
          if (n < offQ) { bp = (const unsigned char*)wmod + (size_t)kq * 128 * 12288 + g16 * 256; ldb = 12288; }
          else if (n < offMR) { bp = (const unsigned char*)wq + (size_t)kq * 128 * 8192 + g32 * 256; ldb = 8192; }
          else { const int u = mu0 + ((n - offMR) >> 1); bp = (const unsigned char*)wmod + (size_t)(u & 15) * 64 * 12288 + 4096 + (u >> 4) * 256; ldb = 12288; }
          bp += (size_t)((n < offMR ? n & 3 : (n - offMR) & 1) * 32) * ldb; hi = 16 * ldb;
        }
        load_block<W_PRO>(L, ld, lane, n, bp, ldb, hi, minprog);
        if (n < 2) {
          asm volatile("s_waitcnt vmcnt(8)" ::: "memory");
          lds_set(L, L_FAUX + 4 * ld, lane, 1u);
        }
      } else {
        const unsigned char* bp = n < offV ? (const unsigned char*)kc + kvoff + (size_t)(n - offK) * 8192 : (const unsigned char*)vc + kvoff + (size_t)(n - offV) * 8192;
        load_block<W_KV>(L, ld, lane, n, bp, 256, 4096, minprog);
      }
    }
    int nend = offW;
    if (isA) {
      nend = offW + 4;
#pragma unroll 1
      for (int n = offW + ld; n < nend; n += 2) load_block<W_WO>(L, ld, lane, n, wo_t + (size_t)(n - offW) * 256 * 2048 + 256, 2048, 128 * 2048, minprog);
    }
    if (isB) {
      nend = offW + 16;
#pragma unroll 1
      for (int n = offW + ld; n < nend; n += 2) {
        const int q = n - offW;
        const unsigned char* bp; int ldb;
        if (q < 8) { bp = (const unsigned char*)w1 + (size_t)(bi & 7) * 128 * 7168 + (bi >> 3) * 512; ldb = 7168; }
        else { bp = (const unsigned char*)w2 + (size_t)(bi >> 2) * 128 * 2048 + (bi & 3) * 512; ldb = 2048; }
        load_block<W_MLP>(L, ld, lane, n, bp + (size_t)((q & 3) * 32) * ldb + ((q >> 2) & 1) * 256, ldb, 16 * ldb, minprog);
      }
    }
    VM0();
    lds_set(L, L_LANDED + 4 * ld, lane, (unsigned)(nend >> 1));
    return;
  }

  const __amdgpu_buffer_rsrc_t rs = __builtin_amdgcn_make_buffer_rsrc((void*)ws, 0, WS_BYTES, 0x00020000);
  const int mybox = box(j);
  const int piece = lane & 7, sg = lane >> 3, comp = tcomp(lane);

  if (w == 10) {
    for (int i = 0; i < 3; ++i) __builtin_amdgcn_s_sleep(24);
    for (unsigned sp = 0; sp < SPIN_CAP; ++sp) {
      bool ok = true;
#pragma unroll
      for (int q = 0; q < 4; ++q) {
        const v2u t = __builtin_amdgcn_raw_buffer_load_b64(rs, WS_ALIVE + (q * 64 + lane) * 16, 0, AUX_SC1);
        ok = ok && t.x == (unsigned)tag && t.y == (unsigned)(tag >> 32);
      }
      if (__all(ok)) break;
      __builtin_amdgcn_s_sleep(4);
      CFENCE();
    }
    lds_set(L, L_FALIVE, lane, 1u);
    if (j == 0 && lane == 0) __hip_atomic_store((GAS unsigned long long*)(ws + WS_EPOCH), epoch + 1ull, __ATOMIC_RELAXED, __HIP_MEMORY_SCOPE_AGENT);
    if (isC) {
      lds_wait_ge(L, L_LANDED, (unsigned)(offK + 8) >> 1);
      auto mr = [&](int cg, int kh, int line) {
        const int u = cg * 16 + kh;
        const int ia = (u >> 1) & 127, jj = ((u < 256 ? 0 : 16) + (ia & 15)) * 8 + (ia >> 4);
        return box(jj) + BX_MR + (u & 1) * 1024 + line * 128 + piece * 16;
      };
#pragma unroll 1
      for (int h4 = 0; h4 < 4; ++h4) {
        const int cg = h4 < 2 ? 8 + ci : (h4 == 2 ? 0 : 24) + (ci >> 1), l0 = h4 < 2 ? 4 * h4 : (ci & 1) * 4;
        const int o0 = mr(cg, sg, l0), o1 = mr(cg, sg + 8, l0);
        v4u g[8];
        const int off[8] = {o0, o1, o0 + 128, o1 + 128, o0 + 256, o1 + 256, o0 + 384, o1 + 384};
        gather<8, 8>(g, off, rs);
#pragma unroll
        for (int q = 0; q < 4; ++q) {
          const float t = rbf(tred(asf(g[2 * q]) + asf(g[2 * q + 1])));
          if (h4 < 2) *(LAS float*)(L + L_SBST + (l0 + q) * 128 + piece * 16 + comp * 4) = t;
          else *(LAS float*)(L + L_GATE + ((h4 - 2) * 4 + q) * 256 + lane * 4) = t;
        }
      }
      LGKM0();
      __builtin_amdgcn_raw_buffer_store_b128(*(const LAS v4u*)(L + L_SBST + lane * 16), rs, mybox + BX_T1 + lane * 16, 0, AUX_SC1);
      lds_set(L, L_FGATE, lane, 1u);
    }
    lds_wait_ge(L, L_MLCNT, 8);
    const f32x2 r = *(const LAS f32x2*)(L + L_MLW + sg * 64 + piece * 8);
    float M = r.x;
    M = fmaxf(M, dpp<ROR8>(M)); M = max32(max16(M));
    float Ls = r.y * exp_(r.x - M);
    Ls += dpp<ROR8>(Ls); Ls = sum32(sum16(Ls));
    const float M1 = dpp<XOR1>(M), L1 = dpp<XOR1>(Ls);
    if (lane < 8 && !(lane & 1)) __builtin_amdgcn_raw_buffer_store_b128(asu(f32x4{M, Ls, M1, L1}), rs, mybox + BX_ML + lane * 8, 0, AUX_SC1);
    for (int i = 0; i < 2; ++i) __builtin_amdgcn_s_sleep(32);
    v4u g[2];
    const int off[2] = {box((lane >> 2) * 8 + grp) + BX_ML + (lane & 3) * 16, box(((lane >> 2) + 16) * 8 + grp) + BX_ML + (lane & 3) * 16};
    gather<2, 16>(g, off, rs);
    const f32x4 u = asf(g[0]), v = asf(g[1]);
    float M0 = fmaxf(u.x, v.x), Mb = fmaxf(u.z, v.z);
    M0 = fmaxf(M0, dpp<ROR4>(M0)); M0 = fmaxf(M0, dpp<ROR8>(M0)); M0 = max32(max16(M0));
    Mb = fmaxf(Mb, dpp<ROR4>(Mb)); Mb = fmaxf(Mb, dpp<ROR8>(Mb)); Mb = max32(max16(Mb));
    float L0 = u.y * exp_(u.x - M0) + v.y * exp_(v.x - M0), Lb = u.w * exp_(u.z - Mb) + v.w * exp_(v.z - Mb);
    L0 += dpp<ROR4>(L0); L0 += dpp<ROR8>(L0); L0 = sum32(sum16(L0));
    Lb += dpp<ROR4>(Lb); Lb += dpp<ROR8>(Lb); Lb = sum32(sum16(Lb));
    if (lane < 4) *(LAS f32x4*)(L + L_MLF + lane * 16) = f32x4{M0, 1.f / L0, Mb, 1.f / Lb};
    LGKM0();
    lds_set(L, L_FML, lane, 1u);
    return;
  }

  unsigned warm = 0;
  if (w == 0) {
    const unsigned char* p = (const unsigned char*)ws; size_t o = 0;
    const size_t kv0 = ((size_t)grp * SK + split * 1024) * 256;
    const int l = lane;
    if (l < 2) { p = (const unsigned char*)kc; o = kv0 + l * 131072; }
    else if (l < 4) { p = (const unsigned char*)vc; o = kv0 + (l - 2) * 131072; }
    else if (l < 8) { p = (const unsigned char*)wq; o = (size_t)kq * 128 * 8192 + (l - 4) * 262144 + g32 * 256; }
    else if (l < 14) { p = (const unsigned char*)wmod; o = (size_t)kq * 128 * 12288 + (l - 8) * 262144; }
    else if (l < 20) { const int u = mu0 + (l - 14) / 3; p = (const unsigned char*)wmod; o = (size_t)(u & 15) * 64 * 12288 + ((l - 14) % 3) * 262144 + 4096; }
    else if (l < 28) {
      if (isA) { p = (const unsigned char*)wo; o = (size_t)(a_hkv * 8) * 128 * 2048 + (l - 20) * 262144; }
      else if (isB && l < 24) { p = (const unsigned char*)w1; o = (size_t)(bi & 7) * 128 * 7168 + (l - 20) * 229376; }
      else if (isB) { p = (const unsigned char*)w2; o = (size_t)(bi >> 2) * 128 * 2048; }
    }
    else if (l < 45) o = (size_t)(l - 28) * 262144;
    else if (l == 45) p = (const unsigned char*)b1;
    else if (l == 46) p = (const unsigned char*)b2;
    asm volatile("global_load_dword %0, %1, off" : "=&v"(warm) : "v"(p + o) : "memory");
  }
  unsigned bar = 0;
  lds_wait_ge(L, L_FAUX, 1); lds_wait_ge(L, L_FAUX + 4, 1);
  asm volatile("s_waitcnt vmcnt(0)" : "+v"(warm) :: "memory");
  const unsigned xh = *(const LAS u16*)(L + L_X + ((piece >> 2) * D + kq * 128 + 16 * w + 4 * (piece & 3) + comp) * 2);
  {
    const v2u cr = *(const LAS v2u*)(L + L_C + tid * 8);
    float s[4] = {lo2f(cr.x), hi2f(cr.x), lo2f(cr.y), hi2f(cr.y)};
#pragma unroll
    for (int i = 0; i < 4; ++i) s[i] = s[i] * rcp_(1.f + exp_(-s[i]));
    *(LAS v2u*)(L + L_SVEC + tid * 8) = v2u{pk(s[0], s[1]), pk(s[2], s[3])};
  }
  {
    const v2u xr = *(const LAS v2u*)(L + L_X + tid * 8);
    const float a0 = lo2f(xr.x), a1 = hi2f(xr.x), a2 = lo2f(xr.y), a3 = hi2f(xr.y);
    const f32x2 sq = swap32((a0 + a1) + (a2 + a3), (a0 * a0 + a1 * a1) + (a2 * a2 + a3 * a3));
    float r = sum16(sq.x + sq.y);
    r += dpp<ROR8>(r); r += dpp<ROR4>(r); r += dpp<XOR2>(r); r += dpp<XOR1>(r);
    if ((lane & 31) == 0) *(LAS float*)(L + L_ST1 + w * 8 + (lane >> 5) * 4) = r;
  }
  cbar(L, lane, bar);
  float lnx;
  {
    const int b = piece >> 2;
    const f32x4 r0 = *(const LAS f32x4*)(L + L_ST1 + b * 32), r1 = *(const LAS f32x4*)(L + L_ST1 + b * 32 + 16);
    float m, rden;
    ln_consts((r0.x + r0.z) + (r1.x + r1.z), (r0.y + r0.w) + (r1.y + r1.w), m, rden);
    lnx = rbf(rbf(lo2f(xh) - m) * rden);
    PIN(lnx);
  }
  const int tl = (8 * (lane >> 4) + ((lane >> 2) & 3)) * 256 + (((2 * w + ((lane & 3) >> 1)) ^ (((lane >> 2) & 3) << 2)) << 4) + (lane & 1) * 8;
  const int pubo = w * 128 + (lane & 15) * 64 + (lane >> 4) * 16;

  if (!isA) {
    f32x4 acc = {0.f, 0.f, 0.f, 0.f};
#pragma unroll
    for (int s = 0; s < 4; ++s) { wait_landed(L, s); acc = mfma(tfrag(L, s, tl), sfrag(L, lane, kq * 128 + 32 * s), acc); }
    release(L, w, lane, 4);
    if ((lane & 15) < 2) *(LAS f32x4*)(L + L_STG + pubo) = acc;
    cbar(L, lane, bar);
    if (w == 0) {
      lds_wait_ge(L, L_FCLR, 1);
      __builtin_amdgcn_raw_buffer_store_b128(*(const LAS v4u*)(L + L_STG + lane * 16), rs, mybox + BX_MC + lane * 16, 0, AUX_SC1);
    }
  }
  v4u qw[4];
#pragma unroll
  for (int s = 0; s < 4; ++s) { wait_landed(L, offQ + s); qw[s] = tfrag(L, (offQ + s) & 15, tl); }
  release(L, w, lane, offMR);
#pragma unroll 1
  for (int u = 0; u < 2; ++u) {
    const int n = offMR + 2 * u, k0 = ((mu0 + u) & 15) * 64;
    wait_landed(L, n); wait_landed(L, n + 1);
    f32x4 acc = mfma(tfrag(L, n & 15, tl), sfrag(L, lane, k0), f32x4{0.f, 0.f, 0.f, 0.f});
    acc = mfma(tfrag(L, (n + 1) & 15, tl), sfrag(L, lane, k0 + 32), acc);
    release(L, w, lane, n + 2 < offK ? n + 2 : offK + w);
    if ((lane & 15) < 2) *(LAS f32x4*)(L + L_STG + 2048 + u * 1024 + pubo) = acc;
  }
  v4u wf[4];
  if (isA) {
#pragma unroll
    for (int q = 0; q < 4; ++q) { wait_landed(L, offWO + q); wf[q] = tfrag(L, offWO + q, tl); }
    release(L, w, lane, offK + w);
  }
  {
    lds_wait_ge(L, L_FALIVE, 1);
    v4u g[2];
    const int js = (16 + (kq & 1) * 8 + sg) * 8 + (kq >> 1);
    const int off[2] = {box(js) + BX_MC + w * 128 + piece * 16, box(js + 4) + BX_MC + w * 128 + piece * 16};
    gather<2>(g, off, rs);
    const float sa = rbf(tred(asf(g[0]))), ca = rbf(tred(asf(g[1])));
    const float h = rbf(lnx * rbf(1.f + ca)) + sa;
    if (!(lane & 8)) *(LAS u16*)(L + L_HVEC + (piece >> 2) * VROW + (16 * w + 4 * (piece & 3) + comp) * 2) = (u16)pk(h, h);
    cbar(L, lane, bar);
    if (w >= 1 && w <= 2)
      __builtin_amdgcn_raw_buffer_store_b128(*(const LAS v4u*)(L + L_STG + 1024 + w * 1024 + lane * 16), rs, mybox + BX_MR + (w - 1) * 1024 + lane * 16, 0, AUX_SC1);
    f32x4 acc = {0.f, 0.f, 0.f, 0.f};
#pragma unroll
    for (int s = 0; s < 4; ++s) acc = mfma(qw[s], vfrag(L, L_HVEC, lane, s), acc);
    if ((lane & 15) < 2) *(LAS f32x4*)(L + L_STG + 1024 + (lane & 15) * 512 + (16 * w + 4 * (lane >> 4)) * 4) = acc;
    cbar(L, lane, bar);
    if (w == 0) __builtin_amdgcn_raw_buffer_store_b128(*(const LAS v4u*)(L + L_STG + 1024 + lane * 16), rs, mybox + BX_Q + lane * 16, 0, AUX_SC1);
  }
  v4u qf[4], tw[8], tx[8];
  const int kl = (8 * ((lane & 15) >> 2) + (lane & 3)) * 256 + ((lane & 3) << 6) + (lane >> 4) * 16;
  {
    wait_landed(L, offK + w);
    kfrags(L, (offK + w) & 15, kl, tw);
    release(L, w, lane, offK + w + 8);
    wait_landed(L, offK + w + 8);
    kfrags(L, (offK + w + 8) & 15, kl, tx);
    release(L, w, lane, offK + w + 16);
    v4u g[4]; int off[4];
    const int head = hkv * 8 + w;
#pragma unroll
    for (int q = 0; q < 4; ++q) {
      const int jq = ((head & 3) * 8 + 2 * q + (lane >> 5)) * 8 + (head >> 2);
      off[q] = box(jq) + BX_Q + batt * 512 + (lane & 31) * 16;
    }
    gather<4, 8>(g, off, rs);
    const f32x4 t = sum4(g);
    const f32x2 a = swap32(t.x, t.y), b = swap32(t.z, t.w);
    const unsigned p = pk(a.x + a.y, b.x + b.y);
    LAS u16* qv = (LAS u16*)(L + L_QVEC + w * 256 + (lane & 31) * 8 + (lane >> 5) * 2);
    qv[0] = (u16)p; qv[2] = (u16)(p >> 16);
    cbar(L, lane, bar);
#pragma unroll
    for (int s = 0; s < 4; ++s) qf[s] = *(const LAS v4u*)(L + L_QVEC + (lane & 7) * 256 + s * 64 + (lane >> 4) * 16);
  }
  float sc[4][8];
#pragma unroll
  for (int i = 0; i < 4; ++i) {
    v4u kf[8];
    if (i < 2) {
#pragma unroll
      for (int q = 0; q < 8; ++q) kf[q] = i == 0 ? tw[q] : tx[q];
    } else {
      const int n = offK + w + 8 * i;
      wait_landed(L, n);
      kfrags(L, n & 15, kl, kf);
      release(L, w, lane, i < 3 ? n + 8 : offV);
    }
#pragma unroll
    for (int T = 0; T < 2; ++T) {
      f32x4 a = {0.f, 0.f, 0.f, 0.f};
#pragma unroll
      for (int s = 0; s < 4; ++s) a = mfma(kf[T * 4 + s], qf[s], a);
      const f32x2 lo = rbf2(rbf2(f32x2{a.x, a.y}) * 0.08838834764831845f), hi = rbf2(rbf2(f32x2{a.z, a.w}) * 0.08838834764831845f);
      sc[i][T * 4] = lo.x; sc[i][T * 4 + 1] = lo.y; sc[i][T * 4 + 2] = hi.x; sc[i][T * 4 + 3] = hi.y;
    }
  }
  float mw = sc[0][0];
  {
#pragma unroll
    for (int i = 0; i < 4; ++i)
#pragma unroll
      for (int r = 0; r < 8; ++r) mw = fmaxf(mw, sc[i][r]);
    mw = max32(max16(mw));
    float lw = 0.f;
#pragma unroll
    for (int i = 0; i < 4; ++i)
#pragma unroll
      for (int r = 0; r < 8; ++r) { sc[i][r] = exp_(sc[i][r] - mw); lw += sc[i][r]; }
    lw = sum32(sum16(lw));
    if (lane < 8) *(LAS f32x2*)(L + L_MLW + w * 64 + lane * 8) = f32x2{mw, lw};
    lds_inc(L, L_MLCNT, lane);
  }
#pragma unroll
  for (int q = 0; q < VPARK; ++q) {
    wait_landed(L, offV + q);
    if (q < 8) tw[q & 7] = tfrag(L, (offV + q) & 15, tl); else tx[q & 7] = tfrag(L, (offV + q) & 15, tl);
    release(L, w, lane, offV + q + 1);
  }
  lds_wait_ge(L, L_FML, 1);
  {
    const f32x2 ml = *(const LAS f32x2*)(L + L_MLF + (lane & 7) * 8);
    const float f = exp_(mw - ml.x) * ml.y;
#pragma unroll
    for (int i = 0; i < 4; ++i) {
      const v4u p = {pk(sc[i][0] * f, sc[i][1] * f), pk(sc[i][2] * f, sc[i][3] * f), pk(sc[i][4] * f, sc[i][5] * f), pk(sc[i][6] * f, sc[i][7] * f)};
      if ((lane & 15) < 8) *(LAS v4u*)(L + L_P + (w + 8 * i) * 512 + ((lane >> 4) * 8 + (lane & 7)) * 16) = p;
    }
    cbar(L, lane, bar);
  }
  f32x4 oacc = {0.f, 0.f, 0.f, 0.f};
  const int pl = L_P + ((lane >> 4) * 8 + (lane & 7)) * 16;
#pragma unroll
  for (int i = 0; i < VPARK; ++i) oacc = mfma(i < 8 ? tw[i & 7] : tx[i & 7], *(const LAS v4u*)(L + pl + i * 512), oacc);
#pragma unroll 1
  for (int i = VPARK; i < 32; i += 2) {
    const int n = offV + i;
    wait_landed(L, n + 1); wait_landed(L, n);
    const v4u a0 = tfrag(L, n & 15, tl), a1 = tfrag(L, (n + 1) & 15, tl);
    const v4u p0 = *(const LAS v4u*)(L + pl + i * 512), p1 = *(const LAS v4u*)(L + pl + (i + 1) * 512);
    release(L, w, lane, n + 2);
    oacc = mfma(a0, p0, oacc);
    oacc = mfma(a1, p1, oacc);
  }
  if ((lane & 15) < 8) __builtin_amdgcn_raw_buffer_store_b128(asu(oacc), rs, mybox + BX_O + w * 512 + ((lane >> 4) * 8 + (lane & 15)) * 16, 0, AUX_SC1);

  if (isA) {
    v4u g[4]; int off[4];
#pragma unroll
    for (int q = 0; q < 4; ++q) off[q] = box((sg + 8 * q) * 8 + (w >> 2) * 4 + a_hkv) + BX_O + a_dt * 512 + (w & 3) * 128 + piece * 16;
#pragma unroll
    for (int q = 0; q < 4; ++q) { wait_landed(L, offW + q); tw[q] = tfrag(L, (offW + q) & 15, tl); }
    __builtin_amdgcn_s_sleep(4);
    gather<4>(g, off, rs);
    const float t = tred(sum4(g));
    *(LAS u16*)(L + L_TVEC + (w >> 2) * VROW + (16 * piece + 4 * (w & 3) + comp) * 2) = (u16)pk(t, t);
    WGBAR();
    tile_pair(L, L_TVEC, wf, tw, rs, mybox + BX_T1 + w * 128, lane);
  } else if (isC) {
    if (w < 4) {
      const int col = 64 * ci + 16 * w + 4 * (piece & 3) + comp;
      const float xres = lo2f(*(const GAS u16*)(x + (piece >> 2) * D + col)), bias = lo2f(*(const GAS u16*)(b2 + col));
      lds_wait_ge(L, L_FGATE, 1);
      const float ga = *(const LAS float*)(L + L_GATE + w * 256 + lane * 4), gb = *(const LAS float*)(L + L_GATE + (4 + w) * 256 + lane * 4);
      const int ct = 4 * ci + w, cg = ct >> 4, ti = ct & 15;
      v4u g[4]; int off[4], off2[4];
#pragma unroll
      for (int q = 0; q < 4; ++q) {
        const int a = (sg + 8 * q) * 4 + cg;
        off[q] = box((a & 15) * 8 + (a >> 4)) + BX_T1 + ti * 128 + piece * 16;
        const int kf = sg + 8 * q, bb = kf * 4 + cg, gg = bb / 14;
        off2[q] = kf < 28 ? box((16 + bb - gg * 14) * 8 + gg) + BX_T2 + ti * 128 + piece * 16 : WS_BYTES;
        PIN(off2[q]);
      }
      gather<4>(g, off, rs);
      const float x1 = rbf(xres + rbf(ga * rbf(tred(sum4(g)))));
      const f32x4 x4 = texp(x1);
      __builtin_amdgcn_raw_buffer_store_b128(asu(x4), rs, mybox + BX_X1 + sg * 512 + w * 128 + piece * 16, 0, AUX_SC1);
      gather<4>(g, off2, rs);
      const float o = x1 + rbf(gb * rbf(rbf(tred(sum4(g))) + bias));
      if (!(lane & 8)) *(GAS u16*)(out + (piece >> 2) * D + col) = (u16)pk(o, o);
    }
  } else {
    f32x4 sbv, cb1;
    float bias;
    {
      v4u g2[2];
      const int off[2] = {box(30 * 8 + (w >> 1) + (w & 1) * 8) + BX_T1 + lane * 16, box(30 * 8 + 4 + (w >> 1) + (w & 1) * 8) + BX_T1 + lane * 16};
      gather<2>(g2, off, rs);
      sbv = asf(g2[0]);
      const f32x4 cb = asf(g2[1]);
      const f32x2 c0 = rbf2(f32x2{cb.x, cb.y} + 1.f), c1 = rbf2(f32x2{cb.z, cb.w} + 1.f);
      cb1 = f32x4{c0.x, c0.y, c1.x, c1.y};
      bias = lo2f(*(const GAS u16*)(b1 + (bi >> 2) * 128 + 16 * w + 4 * (piece & 3) + comp));
      PIN(bias); PIN(sbv); PIN(cb1);
    }
#pragma unroll
    for (int q = 0; q < 8; ++q) { wait_landed(L, offW + q); tw[q] = tfrag(L, (offW + q) & 15, tl); }
    const int cc = 2 * w + (sg >> 2);
    const int offx[1] = {box((30 + (cc & 1)) * 8 + (cc >> 1)) + BX_X1 + grp * 512 + (sg & 3) * 128 + piece * 16};
    const int kf = bi >> 2, bb = (kf >> 1) * 8 + sg, gg = bb / 14;
    int offh[1] = {box((16 + bb - gg * 14) * 8 + gg) + BX_T1 + ((kf & 1) * 8 + w) * 128 + piece * 16};
    PIN(offh[0]);
    wait_landed(L, offW + 14); wait_landed(L, offW + 15);
    v4u g[1];
    gather<1>(g, offx, rs);
    const f32x4 x1 = asf(g[0]);
    float m, rden;
    stat_put(L, L_ST1, w, lane, x1);
    WGBAR();
    stat_get(L, L_ST1, lane, m, rden);
    const f32x2 ha = rbf2(rbf2(ln2(f32x2{x1.x, x1.y}, m, rden) * f32x2{cb1.x, cb1.y}) + f32x2{sbv.x, sbv.y});
    const f32x2 hb = rbf2(rbf2(ln2(f32x2{x1.z, x1.w}, m, rden) * f32x2{cb1.z, cb1.w}) + f32x2{sbv.z, sbv.w});
    stat_put(L, L_ST2, w, lane, f32x4{ha.x, ha.y, hb.x, hb.y});
    WGBAR();
    if (w == (bi & 7)) {
      stat_get(L, L_ST2, lane, m, rden);
      const f32x2 ya = rbf2(ha - m) * rden, yb = rbf2(hb - m) * rden;
      *(LAS v2u*)(L + L_TVEC + (piece >> 2) * VROW + (16 * sg + 4 * (piece & 3)) * 2) = v2u{pk(ya.x, ya.y), pk(yb.x, yb.y)};
    }
    WGBAR();
    tile_pair(L, L_TVEC, tw, tw + 4, rs, mybox + BX_T1 + w * 128, lane);
#pragma unroll
    for (int q = 0; q < 8; ++q) { wait_landed(L, offW + 8 + q); tw[q] = tfrag(L, (offW + 8 + q) & 15, tl); }
    __builtin_amdgcn_s_sleep(5);
    gather<1>(g, offh, rs);
    const float u = rbf(rbf(tred(asf(g[0]))) + bias);
    const float z = u * (0.7978845608028654f + 0.035677408136300125f * u * u);
    const float hh = u * rcp_(1.f + __builtin_amdgcn_exp2f(-2.8853900817779268f * z));
    *(LAS u16*)(L + L_UVEC + (piece >> 2) * VROW + (16 * w + 4 * (piece & 3) + comp) * 2) = (u16)pk(hh, hh);
    WGBAR();
    tile_pair(L, L_UVEC, tw, tw + 4, rs, mybox + BX_T2 + w * 128, lane);
  }
}

}

extern "C" void kernel_launch(void* const* d_in, const int* in_sizes, int n_in, void* d_out, int out_size, void* d_ws, size_t ws_size,
                              hipStream_t stream) {
  (void)in_sizes; (void)n_in; (void)out_size; (void)ws_size;
  (void)hipFuncSetAttribute((const void*)fused_block, hipFuncAttributeMaxDynamicSharedMemorySize, LDS_BYTES);
  hipLaunchKernelGGL(fused_block, dim3(256), dim3(NTHR), LDS_BYTES, stream, (const u16*)d_in[0], (const u16*)d_in[1], (const u16*)d_in[2],
                     (const u16*)d_in[3], (const u16*)d_in[4], (const u16*)d_in[6], (const u16*)d_in[7], (const u16*)d_in[8],
                     (const u16*)d_in[9], (const u16*)d_in[10], (const u16*)d_in[11], (u16*)d_out, (unsigned char*)d_ws);
}
```

```cpp
#include <hip/hip_runtime.h>
#include <hip/hip_bf16.h>
#include <stdint.h>

#define LAS __attribute__((address_space(3)))
#define GAS __attribute__((address_space(1)))
typedef unsigned short u16;
typedef unsigned v4u __attribute__((ext_vector_type(4)));
typedef unsigned v2u __attribute__((ext_vector_type(2)));
typedef float f32x4 __attribute__((ext_vector_type(4)));
typedef float f32x2 __attribute__((ext_vector_type(2)));
typedef short s16x4 __attribute__((ext_vector_type(4)));
typedef short bf16x8 __attribute__((ext_vector_type(8)));
typedef __bf16 bf16x2 __attribute__((ext_vector_type(2)));

namespace {

constexpr int D = 1024, SK = 32768;
constexpr int NTHR = 704;
constexpr int W_PRO = 18, W_KV = 12, W_WO = 16, W_MLP = 10;
constexpr int VPARK = 12;

constexpr int WS_EPOCH = 0, WS_ALIVE = 4096, WS_BOX = 65536, BOX = 16384;
constexpr int BX_MC = 0;
constexpr int BX_Q = 1024;
constexpr int BX_ML = 2048;
constexpr int BX_MR = 3072;
constexpr int BX_O = 6144;
constexpr int BX_T1 = 10240;
constexpr int BX_X1 = BX_T1 + 1024;
constexpr int BX_T2 = 12288;
constexpr int WS_BYTES = WS_BOX + 256 * BOX;
constexpr unsigned SENT = 0xFFFFFFFFu;
constexpr unsigned SPIN_CAP = 1u << 15, LDS_CAP = 1u << 20;
constexpr int AUX_SC1 = 16;

constexpr int L_RING = 0, L_P = 131072, L_CTL = L_P + 16384;
constexpr int L_X = L_P, L_C = L_P + 4096, L_STG = L_P + 8192;
constexpr int L_LANDED = L_CTL;
constexpr int L_PROG = L_CTL + 32;
constexpr int L_BAR = L_CTL + 64, L_MLCNT = L_CTL + 68, L_FALIVE = L_CTL + 72, L_FML = L_CTL + 76, L_FAUX = L_CTL + 80, L_FGATE = L_CTL + 88, L_FCLR = L_CTL + 92;
constexpr int L_SVEC = L_CTL + 256;
constexpr int VROW = 320;
constexpr int L_HVEC = L_SVEC + 4096;
constexpr int L_QVEC = L_HVEC + 640;
constexpr int L_ST1 = L_QVEC + 2048;
constexpr int L_ST2 = L_ST1 + 128;
constexpr int L_MLW = L_ST2 + 128;
constexpr int L_MLF = L_MLW + 512;
constexpr int L_TVEC = L_MLF + 64;
constexpr int L_UVEC = L_TVEC + 640;
constexpr int L_SBST = L_TVEC;
constexpr int L_GATE = L_UVEC + 640;
constexpr int LDS_BYTES = L_GATE + 2048;
static_assert(LDS_BYTES <= 160 * 1024 && LDS_BYTES > 80 * 1024, "one workgroup per CU");

typedef LAS unsigned char* lptr;

__device__ __forceinline__ float lo2f(unsigned w) { return __uint_as_float(w << 16); }
__device__ __forceinline__ float hi2f(unsigned w) { return __uint_as_float(w & 0xffff0000u); }
__device__ __forceinline__ unsigned pk(float a, float b) {
  bf16x2 v = {(__bf16)a, (__bf16)b};
  return __builtin_bit_cast(unsigned, v);
}
__device__ __forceinline__ float rbf(float a) { return hi2f(pk(a, a)); }
__device__ __forceinline__ f32x2 rbf2(f32x2 a) {
  unsigned p = pk(a.x, a.y);
  asm("" : "+v"(p));
  return f32x2{lo2f(p), hi2f(p)};
}
__device__ __forceinline__ f32x4 mfma(v4u a, v4u b, f32x4 c) {
  return __builtin_amdgcn_mfma_f32_16x16x32_bf16(__builtin_bit_cast(bf16x8, a), __builtin_bit_cast(bf16x8, b), c, 0, 0, 0);
}
template <int CTRL> __device__ __forceinline__ float dpp(float x) {
  return __builtin_bit_cast(float, __builtin_amdgcn_mov_dpp(__builtin_bit_cast(int, x), CTRL, 0xf, 0xf, true));
}
constexpr int XOR1 = 0xB1, XOR2 = 0x4E, ROR4 = 0x124, ROR8 = 0x128;
__device__ __forceinline__ f32x2 swap32(float a, float b) {
  auto s = __builtin_amdgcn_permlane32_swap(__float_as_uint(a), __float_as_uint(b), false, false);
  return f32x2{__uint_as_float(s[0]), __uint_as_float(s[1])};
}
__device__ __forceinline__ f32x2 swap16(float a, float b) {
  auto s = __builtin_amdgcn_permlane16_swap(__float_as_uint(a), __float_as_uint(b), false, false);
  return f32x2{__uint_as_float(s[0]), __uint_as_float(s[1])};
}
__device__ __forceinline__ float sum16(float x) { const f32x2 s = swap16(x, x); return s.x + s.y; }
__device__ __forceinline__ float sum32(float x) { const f32x2 s = swap32(x, x); return s.x + s.y; }
__device__ __forceinline__ float max16(float x) { const f32x2 s = swap16(x, x); return fmaxf(s.x, s.y); }
__device__ __forceinline__ float max32(float x) { const f32x2 s = swap32(x, x); return fmaxf(s.x, s.y); }
__device__ __forceinline__ float tred(f32x4 v) {
  const f32x2 a = swap32(v.x, v.y), b = swap32(v.z, v.w);
  const f32x2 c = swap16(a.x + a.y, b.x + b.y);
  const float t = c.x + c.y;
  return t + dpp<ROR8>(t);
}
__device__ __forceinline__ int tcomp(int lane) { return ((lane >> 4) & 1) * 2 + (lane >> 5); }
__device__ __forceinline__ f32x4 texp(float t) {
  const f32x2 a = swap16(t, t);
  const f32x2 b = swap32(a.x, a.x), c = swap32(a.y, a.y);
  return f32x4{b.x, b.y, c.x, c.y};
}
__device__ __forceinline__ f32x4 asf(v4u v) { return __builtin_bit_cast(f32x4, v); }
__device__ __forceinline__ v4u asu(f32x4 v) { return __builtin_bit_cast(v4u, v); }
__device__ __forceinline__ unsigned umax4(v4u v) { return max(max(max(v.x, v.y), v.z), v.w); }
__device__ __forceinline__ float exp_(float x) { return __builtin_amdgcn_exp2f(x * 1.4426950408889634f); }
__device__ __forceinline__ float rcp_(float x) { return __builtin_amdgcn_rcpf(x); }

#define LGKM0() asm volatile("s_waitcnt lgkmcnt(0)" ::: "memory")
#define VM0() asm volatile("s_waitcnt vmcnt(0)" ::: "memory")
#define CFENCE() asm volatile("" ::: "memory")
#define PIN(x) asm volatile("" : "+v"(x))
#define WGBAR() do { LGKM0(); __builtin_amdgcn_s_barrier(); CFENCE(); } while (0)

__device__ __forceinline__ void dma16(unsigned voff, const unsigned char* sbase, unsigned ldsaddr) {
  unsigned keep;
  asm volatile("s_mov_b32 %0, m0\n\ts_mov_b32 m0, %3\n\ts_nop 4\n\tglobal_load_lds_dwordx4 %1, %2 sc0 nt\n\ts_mov_b32 m0, %0"
               : "=&s"(keep) : "v"(voff), "s"(sbase), "s"(ldsaddr) : "memory");
}
__device__ __forceinline__ void lds_set(lptr L, int off, int lane, unsigned v) {
  if (lane == 0) *(volatile LAS unsigned*)(L + off) = v;
}
__device__ __forceinline__ void lds_wait_ge(lptr L, int off, unsigned need) {
#pragma nounroll
  for (unsigned sp = 0; (unsigned)__builtin_amdgcn_readfirstlane(*(volatile LAS unsigned*)(L + off)) < need && sp < LDS_CAP; ++sp) { }
  CFENCE();
}
__device__ __forceinline__ void wait_landed(lptr L, int n) { lds_wait_ge(L, L_LANDED + 4 * (n & 1), (unsigned)(n >> 1) + 1u); }
__device__ __forceinline__ void release(lptr L, int w, int lane, int n) { LGKM0(); lds_set(L, L_PROG + 4 * w, lane, (unsigned)n); }
__device__ __forceinline__ void lds_inc(lptr L, int off, int lane) {
  LGKM0();
  if (lane == 0) __hip_atomic_fetch_add((LAS unsigned*)(L + off), 1u, __ATOMIC_RELAXED, __HIP_MEMORY_SCOPE_WORKGROUP);
}
__device__ __forceinline__ void cbar(lptr L, int lane, unsigned& target) {
  target += 8;
  lds_inc(L, L_BAR, lane);
  lds_wait_ge(L, L_BAR, target);
}
__device__ __forceinline__ v4u tfrag(lptr L, int slot, int tl) {
  const s16x4 a = __builtin_amdgcn_ds_read_tr16_b64_v4i16((LAS s16x4*)(L + L_RING + slot * 8192 + tl));
  const s16x4 b = __builtin_amdgcn_ds_read_tr16_b64_v4i16((LAS s16x4*)(L + L_RING + slot * 8192 + tl + 1024));
  const v2u x = __builtin_bit_cast(v2u, a), y = __builtin_bit_cast(v2u, b);
  return v4u{x.x, x.y, y.x, y.y};
}
__device__ __forceinline__ void kfrags(lptr L, int slot, int kl, v4u (&f)[8]) {
#pragma unroll
  for (int q = 0; q < 8; ++q) f[q] = *(const LAS v4u*)(L + L_RING + slot * 8192 + (q >> 2) * 1024 + (kl ^ ((q & 3) << 6)));
}
__device__ __forceinline__ v4u sfrag(lptr L, int lane, int k0) {
  return *(const LAS v4u*)(L + L_SVEC + (lane & 1) * 2048 + k0 * 2 + (lane >> 4) * 16);
}
__device__ __forceinline__ v4u vfrag(lptr L, int off, int lane, int s) {
  return *(const LAS v4u*)(L + off + (lane & 1) * VROW + s * 64 + (lane >> 4) * 16);
}
template <int N>
__device__ __forceinline__ void gather_issue(v4u (&v)[N], const int (&off)[N], __amdgpu_buffer_rsrc_t rs) {
#pragma unroll
  for (int q = 0; q < N; ++q) v[q] = __builtin_amdgcn_raw_buffer_load_b128(rs, off[q], 0, AUX_SC1);
}
template <int N, int PAUSE>
__device__ __forceinline__ void gather_finish(v4u (&v)[N], const int (&off)[N], __amdgpu_buffer_rsrc_t rs) {
#pragma nounroll
  for (unsigned sp = 0; sp < SPIN_CAP; ++sp) {
    unsigned mx = umax4(v[0]);
#pragma unroll
    for (int q = 1; q < N; ++q) mx = max(mx, umax4(v[q]));
    if (__builtin_amdgcn_ballot_w64(mx == SENT) == 0) break;
    if (PAUSE) __builtin_amdgcn_s_sleep(PAUSE);
    CFENCE();
    if (mx == SENT) gather_issue<N>(v, off, rs);
  }
}
template <int N, int PAUSE = 0>
__device__ __forceinline__ void gather(v4u (&v)[N], const int (&off)[N], __amdgpu_buffer_rsrc_t rs) {
  gather_issue<N>(v, off, rs);
  gather_finish<N, PAUSE>(v, off, rs);
}
__device__ __forceinline__ f32x4 sum4(const v4u (&g)[4]) { return (asf(g[0]) + asf(g[1])) + (asf(g[2]) + asf(g[3])); }
__device__ __forceinline__ void tile_pair(lptr L, int vec, const v4u* f0, const v4u* f1, __amdgpu_buffer_rsrc_t rs, int off, int lane) {
  f32x4 a0 = {0.f, 0.f, 0.f, 0.f}, a1 = {0.f, 0.f, 0.f, 0.f};
#pragma unroll
  for (int s = 0; s < 4; ++s) { const v4u bv = vfrag(L, vec, lane, s); a0 = mfma(f0[s], bv, a0); a1 = mfma(f1[s], bv, a1); }
  if ((lane & 15) < 2) {
    __builtin_amdgcn_raw_buffer_store_b128(asu(a0), rs, off + (lane & 1) * 64 + (lane >> 4) * 16, 0, AUX_SC1);
    __builtin_amdgcn_raw_buffer_store_b128(asu(a1), rs, off + 1024 + (lane & 1) * 64 + (lane >> 4) * 16, 0, AUX_SC1);
  }
}
__device__ __forceinline__ int box(int j) { return WS_BOX + j * BOX; }
__device__ __forceinline__ void ln_consts(float s, float q, float& m, float& rden) {
  const float mean = s * (1.f / D);
  const float var = fmaxf(q * (1.f / D) - mean * mean, 0.f);
  m = rbf(mean);
  rden = rcp_(rbf(__builtin_amdgcn_sqrtf(rbf(rbf(var) + 1e-5f))));
}
__device__ __forceinline__ f32x2 ln2(f32x2 x, float m, float rden) { return rbf2(rbf2(x - m) * rden); }
__device__ __forceinline__ void stat_put(lptr L, int off, int w, int lane, f32x4 v) {
  const float s = (v.x + v.y) + (v.z + v.w), q = (v.x * v.x + v.y * v.y) + (v.z * v.z + v.w * v.w);
  const f32x2 a = swap32(s, q);
  float r = sum16(a.x + a.y);
  r += dpp<ROR8>(r); r += dpp<XOR2>(r); r += dpp<XOR1>(r);
  if ((lane & 0x1B) == 0) *(LAS float*)(L + off + w * 16 + (lane & 4) * 2 + (lane >> 5) * 4) = r;
}
__device__ __forceinline__ void stat_get(lptr L, int off, int lane, float& m, float& rden) {
  const f32x2 p = *(const LAS f32x2*)(L + off + (lane >> 3) * 16 + (lane & 4) * 2);
  const f32x2 a = swap32(p.x, p.y);
  float r = sum16(a.x + a.y);
  r += dpp<ROR8>(r);
  const f32x2 t = swap32(r, r);
  ln_consts(t.x, t.y, m, rden);
}

__device__ __forceinline__ int read_minprog(lptr L, int lane) {
  unsigned m = *(volatile LAS unsigned*)(L + L_PROG + 4 * (lane & 7));
  m = min(m, (unsigned)__builtin_amdgcn_mov_dpp((int)m, XOR1, 0xf, 0xf, true));
  m = min(m, (unsigned)__builtin_amdgcn_mov_dpp((int)m, XOR2, 0xf, 0xf, true));
  m = min(m, (unsigned)__builtin_amdgcn_mov_dpp((int)m, ROR4, 0xf, 0xf, true));
  return __builtin_amdgcn_readfirstlane((int)m);
}
template <int W>
__device__ __forceinline__ void load_block(lptr L, int ld, int lane, int n, const unsigned char* bp, int ldb, int hi, int& minprog) {
  if (minprog < n - 15) {
    minprog = read_minprog(L, lane);
    if (minprog < n - 15) {
      VM0();
      lds_set(L, L_LANDED + 4 * ld, lane, (unsigned)(n >> 1));
      for (unsigned sp = 0; sp < LDS_CAP; ++sp) {
        minprog = read_minprog(L, lane);
        if (minprog >= n - 15) break;
      }
    }
  }
  const unsigned voff = (unsigned)((lane >> 4) * ldb + (((lane & 15) ^ ((lane >> 4) << 2)) << 4));
  const unsigned dst = (unsigned)(size_t)L + L_RING + (unsigned)(n & 15) * 8192u;
#pragma unroll
  for (int p = 0; p < 8; ++p) {
    dma16(voff, bp + (size_t)((p & 3) * 4) * ldb + (size_t)(p >> 2) * hi, dst + p * 1024);
    asm volatile("s_waitcnt vmcnt(%0)" ::"i"(W - 1) : "memory");
    if (((p + 2 - W) & 7) == 0) {
      const int done = (n >> 1) + (p + 2 - W) / 8;
      if (done > 0) lds_set(L, L_LANDED + 4 * ld, lane, (unsigned)done);
    }
  }
}

__global__ void __launch_bounds__(NTHR) fused_block(const u16* __restrict__ x, const u16* __restrict__ kc, const u16* __restrict__ vc,
                                                     const u16* __restrict__ wq, const u16* __restrict__ wo, const u16* __restrict__ w1,
                                                     const u16* __restrict__ b1, const u16* __restrict__ w2, const u16* __restrict__ b2,
                                                     const u16* __restrict__ cvec, const u16* __restrict__ wmod, u16* __restrict__ out,
                                                     unsigned char* ws) {
  extern __shared__ __attribute__((aligned(16))) unsigned char smem[];
  lptr L = (lptr)smem;
  const int tid = threadIdx.x, lane = tid & 63, w = __builtin_amdgcn_readfirstlane(tid >> 6);
  const int j = blockIdx.x, grp = j & 7, split = j >> 3, batt = grp >> 2, hkv = grp & 3;
  const bool isA = split < 16, isC = split >= 30, isB = !isA && !isC;
  const int kq = split & 7, g32 = grp * 4 + (split >> 3), g16 = grp * 2 + ((split >> 3) & 1);
  const int mu0 = isA ? 2 * (grp * 16 + split) : 256 + 2 * (grp * 16 + split - 16);
  const int offQ = isA ? 0 : 4, offMR = offQ + 4;
  constexpr int offWO = 8, offK = 12, offV = offK + 32, offW = offV + 32;
  const int ai = grp * 16 + split, a_hkv = ai >> 5, a_dt = (ai >> 2) & 7, a_cg = ai & 3;
  const int bi = grp * 14 + (split - 16), ci = grp * 2 + (split - 30);

  if (tid < 64) *(volatile LAS unsigned*)(L + L_CTL + 4 * tid) = 0u;
  WGBAR();
  unsigned long long epoch = 0, tag = 0;
  if (w == 10) {
    const __amdgpu_buffer_rsrc_t rsh = __builtin_amdgcn_make_buffer_rsrc((void*)ws, 0, WS_BYTES, 0x00020000);
    const v2u ep = __builtin_amdgcn_raw_buffer_load_b64(rsh, WS_EPOCH, 0, AUX_SC1);
    const v4u s4 = {SENT, SENT, SENT, SENT};
    const unsigned used = isA ? 0x0FDEu : (isB ? 0x3FDFu : 0x7FDFu);
#pragma unroll 1
    for (int u = 0; u < 15; ++u)
      if ((used >> u) & 1) __builtin_amdgcn_raw_buffer_store_b128(s4, rsh, box(j) + u * 1024 + lane * 16, 0, AUX_SC1);
    VM0();
    lds_set(L, L_FCLR, lane, 1u);
    epoch = ((unsigned long long)(unsigned)__builtin_amdgcn_readfirstlane((int)ep.y) << 32) | (unsigned)__builtin_amdgcn_readfirstlane((int)ep.x);
    tag = (epoch ^ 0x9E3779B97F4A7C15ull) * 0xD6E8FEB86659FD93ull + 1ull;
    if (lane == 0) __hip_atomic_store((GAS unsigned long long*)(ws + WS_ALIVE + j * 16), tag, __ATOMIC_RELAXED, __HIP_MEMORY_SCOPE_AGENT);
  }
  if (__builtin_expect(w == 8 || w == 9, 1)) {
    const int ld = w - 8;
    __builtin_amdgcn_s_setprio(3);
    int minprog = 0;
    const unsigned char* aux = (const unsigned char*)(ld ? cvec : x);
#pragma unroll
    for (int p = 0; p < 4; ++p) dma16((unsigned)lane * 16u, aux + p * 1024, (unsigned)(size_t)L + L_X + ld * 4096 + p * 1024);
    const size_t kvoff = ((size_t)grp * SK + split * 1024) * 256;
    const unsigned char* wo_t = (const unsigned char*)wo + ((size_t)(a_hkv * 8) * 128 + 16 * a_dt) * 2048 + a_cg * 512;
#pragma unroll 1
    for (int n = ld; n < offW; n += 2) {
      if (n < offK) {
        const unsigned char* bp; int ldb, hi;
        if (isA && n >= offWO) { bp = wo_t + (size_t)(n - offWO) * 256 * 2048; ldb = 2048; hi = 128 * 2048; }
        else {
          if (n < offQ) { bp = (const unsigned char*)wmod + (size_t)kq * 128 * 12288 + g16 * 256; ldb = 12288; }
          else if (n < offMR) { bp = (const unsigned char*)wq + (size_t)kq * 128 * 8192 + g32 * 256; ldb = 8192; }
          else { const int u = mu0 + ((n - offMR) >> 1); bp = (const unsigned char*)wmod + (size_t)(u & 15) * 64 * 12288 + 4096 + (u >> 4) * 256; ldb = 12288; }
          bp += (size_t)((n < offMR ? n & 3 : (n - offMR) & 1) * 32) * ldb; hi = 16 * ldb;
        }
        load_block<W_PRO>(L, ld, lane, n, bp, ldb, hi, minprog);
        if (n < 2) {
          asm volatile("s_waitcnt vmcnt(8)" ::: "memory");
          lds_set(L, L_FAUX + 4 * ld, lane, 1u);
        }
      } else {
        const unsigned char* bp = n < offV ? (const unsigned char*)kc + kvoff + (size_t)(n - offK) * 8192 : (const unsigned char*)vc + kvoff + (size_t)(n - offV) * 8192;
        load_block<W_KV>(L, ld, lane, n, bp, 256, 4096, minprog);
      }
    }
    int nend = offW;
    if (isA) {
      nend = offW + 4;
#pragma unroll 1
      for (int n = offW + ld; n < nend; n += 2) load_block<W_WO>(L, ld, lane, n, wo_t + (size_t)(n - offW) * 256 * 2048 + 256, 2048, 128 * 2048, minprog);
    }
    if (isB) {
      nend = offW + 16;
#pragma unroll 1
      for (int n = offW + ld; n < nend; n += 2) {
        const int q = n - offW;
        const unsigned char* bp; int ldb;
        if (q < 8) { bp = (const unsigned char*)w1 + (size_t)(bi & 7) * 128 * 7168 + (bi >> 3) * 512; ldb = 7168; }
        else { bp = (const unsigned char*)w2 + (size_t)(bi >> 2) * 128 * 2048 + (bi & 3) * 512; ldb = 2048; }
        load_block<W_MLP>(L, ld, lane, n, bp + (size_t)((q & 3) * 32) * ldb + ((q >> 2) & 1) * 256, ldb, 16 * ldb, minprog);
      }
    }
    VM0();
    lds_set(L, L_LANDED + 4 * ld, lane, (unsigned)(nend >> 1));
    return;
  }

  const __amdgpu_buffer_rsrc_t rs = __builtin_amdgcn_make_buffer_rsrc((void*)ws, 0, WS_BYTES, 0x00020000);
  const int mybox = box(j);
  const int piece = lane & 7, sg = lane >> 3, comp = tcomp(lane);

  if (w == 10) {
    for (int i = 0; i < 3; ++i) __builtin_amdgcn_s_sleep(24);
    for (unsigned sp = 0; sp < SPIN_CAP; ++sp) {
      bool ok = true;
#pragma unroll
      for (int q = 0; q < 4; ++q) {
        const v2u t = __builtin_amdgcn_raw_buffer_load_b64(rs, WS_ALIVE + (q * 64 + lane) * 16, 0, AUX_SC1);
        ok = ok && t.x == (unsigned)tag && t.y == (unsigned)(tag >> 32);
      }
      if (__all(ok)) break;
      __builtin_amdgcn_s_sleep(4);
      CFENCE();
    }
    lds_set(L, L_FALIVE, lane, 1u);
    if (j == 0 && lane == 0) __hip_atomic_store((GAS unsigned long long*)(ws + WS_EPOCH), epoch + 1ull, __ATOMIC_RELAXED, __HIP_MEMORY_SCOPE_AGENT);
    if (isC) {
      lds_wait_ge(L, L_LANDED, (unsigned)(offK + 8) >> 1);
      auto mr = [&](int cg, int kh, int line) {
        const int u = cg * 16 + kh;
        const int ia = (u >> 1) & 127, jj = ((u < 256 ? 0 : 16) + (ia & 15)) * 8 + (ia >> 4);
        return box(jj) + BX_MR + (u & 1) * 1024 + line * 128 + piece * 16;
      };
#pragma unroll 1
      for (int h4 = 0; h4 < 4; ++h4) {
        const int cg = h4 < 2 ? 8 + ci : (h4 == 2 ? 0 : 24) + (ci >> 1), l0 = h4 < 2 ? 4 * h4 : (ci & 1) * 4;
        const int o0 = mr(cg, sg, l0), o1 = mr(cg, sg + 8, l0);
        v4u g[8];
        const int off[8] = {o0, o1, o0 + 128, o1 + 128, o0 + 256, o1 + 256, o0 + 384, o1 + 384};
        gather<8, 8>(g, off, rs);
#pragma unroll
        for (int q = 0; q < 4; ++q) {
          const float t = rbf(tred(asf(g[2 * q]) + asf(g[2 * q + 1])));
          if (h4 < 2) *(LAS float*)(L + L_SBST + (l0 + q) * 128 + piece * 16 + comp * 4) = t;
          else *(LAS float*)(L + L_GATE + ((h4 - 2) * 4 + q) * 256 + lane * 4) = t;
        }
      }
      LGKM0();
      __builtin_amdgcn_raw_buffer_store_b128(*(const LAS v4u*)(L + L_SBST + lane * 16), rs, mybox + BX_T1 + lane * 16, 0, AUX_SC1);
      lds_set(L, L_FGATE, lane, 1u);
    }
    lds_wait_ge(L, L_MLCNT, 8);
    const f32x2 r = *(const LAS f32x2*)(L + L_MLW + sg * 64 + piece * 8);
    float M = r.x;
    M = fmaxf(M, dpp<ROR8>(M)); M = max32(max16(M));
    float Ls = r.y * exp_(r.x - M);
    Ls += dpp<ROR8>(Ls); Ls = sum32(sum16(Ls));
    const float M1 = dpp<XOR1>(M), L1 = dpp<XOR1>(Ls);
    if (lane < 8 && !(lane & 1)) __builtin_amdgcn_raw_buffer_store_b128(asu(f32x4{M, Ls, M1, L1}), rs, mybox + BX_ML + lane * 8, 0, AUX_SC1);
    for (int i = 0; i < 2; ++i) __builtin_amdgcn_s_sleep(32);
    v4u g[2];
    const int off[2] = {box((lane >> 2) * 8 + grp) + BX_ML + (lane & 3) * 16, box(((lane >> 2) + 16) * 8 + grp) + BX_ML + (lane & 3) * 16};
    gather<2, 16>(g, off, rs);
    const f32x4 u = asf(g[0]), v = asf(g[1]);
    float M0 = fmaxf(u.x, v.x), Mb = fmaxf(u.z, v.z);
    M0 = fmaxf(M0, dpp<ROR4>(M0)); M0 = fmaxf(M0, dpp<ROR8>(M0)); M0 = max32(max16(M0));
    Mb = fmaxf(Mb, dpp<ROR4>(Mb)); Mb = fmaxf(Mb, dpp<ROR8>(Mb)); Mb = max32(max16(Mb));
    float L0 = u.y * exp_(u.x - M0) + v.y * exp_(v.x - M0), Lb = u.w * exp_(u.z - Mb) + v.w * exp_(v.z - Mb);
    L0 += dpp<ROR4>(L0); L0 += dpp<ROR8>(L0); L0 = sum32(sum16(L0));
    Lb += dpp<ROR4>(Lb); Lb += dpp<ROR8>(Lb); Lb = sum32(sum16(Lb));
    if (lane < 4) *(LAS f32x4*)(L + L_MLF + lane * 16) = f32x4{M0, 1.f / L0, Mb, 1.f / Lb};
    LGKM0();
    lds_set(L, L_FML, lane, 1u);
    return;
  }

  unsigned warm = 0;
  if (w < 3) {
    const unsigned char* p = (const unsigned char*)ws; size_t o = 0;
    const int l = lane;
    if (w == 0) {
      const size_t kv0 = ((size_t)grp * SK + split * 1024) * 256;
      if (l < 4) { p = (const unsigned char*)kc; o = kv0 + l * 65536; }
      else if (l < 8) { p = (const unsigned char*)vc; o = kv0 + (l - 4) * 65536; }
      else if (l < 24) { p = (const unsigned char*)wq; o = (size_t)kq * 128 * 8192 + (l - 8) * 65536 + g32 * 256; }
      else if (l < 48) { p = (const unsigned char*)wmod; o = (size_t)kq * 128 * 12288 + (l - 24) * 65536; }
      else if (l < 52) { if (isB) { p = (const unsigned char*)w2; o = (size_t)(bi >> 2) * 128 * 2048 + (l - 48) * 65536; } }
      else if (l == 52) p = (const unsigned char*)b1;
      else if (l == 53) p = (const unsigned char*)b2;
    } else if (w == 1) {
      if (l < 24) { const int u = mu0 + l / 12; p = (const unsigned char*)wmod; o = (size_t)(u & 15) * 64 * 12288 + (l % 12) * 65536 + 4096; }
      else if (l < 56) {
        if (isA) { p = (const unsigned char*)wo; o = (size_t)(a_hkv * 8) * 128 * 2048 + (l - 24) * 65536; }
        else if (isB && l < 38) { p = (const unsigned char*)w1; o = (size_t)(bi & 7) * 128 * 7168 + (l - 24) * 65536; }
      }
    } else o = (size_t)l * 65536 + 65536;
    asm volatile("global_load_dword %0, %1, off" : "=&v"(warm) : "v"(p + o) : "memory");
  }
  unsigned bar = 0;
  lds_wait_ge(L, L_FAUX, 1); lds_wait_ge(L, L_FAUX + 4, 1);
  asm volatile("s_waitcnt vmcnt(0)" : "+v"(warm) :: "memory");
  const unsigned xh = *(const LAS u16*)(L + L_X + ((piece >> 2) * D + kq * 128 + 16 * w + 4 * (piece & 3) + comp) * 2);
  {
    const v2u cr = *(const LAS v2u*)(L + L_C + tid * 8);
    float s[4] = {lo2f(cr.x), hi2f(cr.x), lo2f(cr.y), hi2f(cr.y)};
#pragma unroll
    for (int i = 0; i < 4; ++i) s[i] = s[i] * rcp_(1.f + exp_(-s[i]));
    *(LAS v2u*)(L + L_SVEC + tid * 8) = v2u{pk(s[0], s[1]), pk(s[2], s[3])};
  }
  {
    const v2u xr = *(const LAS v2u*)(L + L_X + tid * 8);
    const float a0 = lo2f(xr.x), a1 = hi2f(xr.x), a2 = lo2f(xr.y), a3 = hi2f(xr.y);
    const f32x2 sq = swap32((a0 + a1) + (a2 + a3), (a0 * a0 + a1 * a1) + (a2 * a2 + a3 * a3));
    float r = sum16(sq.x + sq.y);
    r += dpp<ROR8>(r); r += dpp<ROR4>(r); r += dpp<XOR2>(r); r += dpp<XOR1>(r);
    if ((lane & 31) == 0) *(LAS float*)(L + L_ST1 + w * 8 + (lane >> 5) * 4) = r;
  }
  cbar(L, lane, bar);
  float lnx;
  {
    const int b = piece >> 2;
    const f32x4 r0 = *(const LAS f32x4*)(L + L_ST1 + b * 32), r1 = *(const LAS f32x4*)(L + L_ST1 + b * 32 + 16);
    float m, rden;
    ln_consts((r0.x + r0.z) + (r1.x + r1.z), (r0.y + r0.w) + (r1.y + r1.w), m, rden);
    lnx = rbf(rbf(lo2f(xh) - m) * rden);
    PIN(lnx);
  }
  const int tl = (8 * (lane >> 4) + ((lane >> 2) & 3)) * 256 + (((2 * w + ((lane & 3) >> 1)) ^ (((lane >> 2) & 3) << 2)) << 4) + (lane & 1) * 8;
  const int pubo = w * 128 + (lane & 15) * 64 + (lane >> 4) * 16;

  if (!isA) {
    f32x4 acc = {0.f, 0.f, 0.f, 0.f};
#pragma unroll
    for (int s = 0; s < 4; ++s) { wait_landed(L, s); acc = mfma(tfrag(L, s, tl), sfrag(L, lane, kq * 128 + 32 * s), acc); }
    release(L, w, lane, 4);
    if ((lane & 15) < 2) *(LAS f32x4*)(L + L_STG + pubo) = acc;
    cbar(L, lane, bar);
    if (w == 0) {
      lds_wait_ge(L, L_FCLR, 1);
      __builtin_amdgcn_raw_buffer_store_b128(*(const LAS v4u*)(L + L_STG + lane * 16), rs, mybox + BX_MC + lane * 16, 0, AUX_SC1);
    }
  }
  v4u qw[4];
#pragma unroll
  for (int s = 0; s < 4; ++s) { wait_landed(L, offQ + s); qw[s] = tfrag(L, (offQ + s) & 15, tl); }
  release(L, w, lane, offMR);
#pragma unroll 1
  for (int u = 0; u < 2; ++u) {
    const int n = offMR + 2 * u, k0 = ((mu0 + u) & 15) * 64;
    wait_landed(L, n); wait_landed(L, n + 1);
    f32x4 acc = mfma(tfrag(L, n & 15, tl), sfrag(L, lane, k0), f32x4{0.f, 0.f, 0.f, 0.f});
    acc = mfma(tfrag(L, (n + 1) & 15, tl), sfrag(L, lane, k0 + 32), acc);
    release(L, w, lane, n + 2 < offK ? n + 2 : offK + w);
    if ((lane & 15) < 2) *(LAS f32x4*)(L + L_STG + 2048 + u * 1024 + pubo) = acc;
  }
  v4u wf[4];
  if (isA) {
#pragma unroll
    for (int q = 0; q < 4; ++q) { wait_landed(L, offWO + q); wf[q] = tfrag(L, offWO + q, tl); }
    release(L, w, lane, offK + w);
  }
  {
    lds_wait_ge(L, L_FALIVE, 1);
    v4u g[2];
    const int js = (16 + (kq & 1) * 8 + sg) * 8 + (kq >> 1);
    const int off[2] = {box(js) + BX_MC + w * 128 + piece * 16, box(js + 4) + BX_MC + w * 128 + piece * 16};
    gather<2>(g, off, rs);
    const float sa = rbf(tred(asf(g[0]))), ca = rbf(tred(asf(g[1])));
    const float h = rbf(lnx * rbf(1.f + ca)) + sa;
    if (!(lane & 8)) *(LAS u16*)(L + L_HVEC + (piece >> 2) * VROW + (16 * w + 4 * (piece & 3) + comp) * 2) = (u16)pk(h, h);
    cbar(L, lane, bar);
    if (w >= 1 && w <= 2)
      __builtin_amdgcn_raw_buffer_store_b128(*(const LAS v4u*)(L + L_STG + 1024 + w * 1024 + lane * 16), rs, mybox + BX_MR + (w - 1) * 1024 + lane * 16, 0, AUX_SC1);
    f32x4 acc = {0.f, 0.f, 0.f, 0.f};
#pragma unroll
    for (int s = 0; s < 4; ++s) acc = mfma(qw[s], vfrag(L, L_HVEC, lane, s), acc);
    if ((lane & 15) < 2) *(LAS f32x4*)(L + L_STG + 1024 + (lane & 15) * 512 + (16 * w + 4 * (lane >> 4)) * 4) = acc;
    cbar(L, lane, bar);
    if (w == 0) __builtin_amdgcn_raw_buffer_store_b128(*(const LAS v4u*)(L + L_STG + 1024 + lane * 16), rs, mybox + BX_Q + lane * 16, 0, AUX_SC1);
  }
  v4u qf[4], tw[8], tx[8];
  const int kl = (8 * ((lane & 15) >> 2) + (lane & 3)) * 256 + ((lane & 3) << 6) + (lane >> 4) * 16;
  {
    wait_landed(L, offK + w);
    kfrags(L, (offK + w) & 15, kl, tw);
    release(L, w, lane, offK + w + 8);
    wait_landed(L, offK + w + 8);
    kfrags(L, (offK + w + 8) & 15, kl, tx);
    release(L, w, lane, offK + w + 16);
    v4u g[4]; int off[4];
    const int head = hkv * 8 + w;
#pragma unroll
    for (int q = 0; q < 4; ++q) {
      const int jq = ((head & 3) * 8 + 2 * q + (lane >> 5)) * 8 + (head >> 2);
      off[q] = box(jq) + BX_Q + batt * 512 + (lane & 31) * 16;
    }
    gather<4, 8>(g, off, rs);
    const f32x4 t = sum4(g);
    const f32x2 a = swap32(t.x, t.y), b = swap32(t.z, t.w);
    const unsigned p = pk(a.x + a.y, b.x + b.y);
    LAS u16* qv = (LAS u16*)(L + L_QVEC + w * 256 + (lane & 31) * 8 + (lane >> 5) * 2);
    qv[0] = (u16)p; qv[2] = (u16)(p >> 16);
    cbar(L, lane, bar);
#pragma unroll
    for (int s = 0; s < 4; ++s) qf[s] = *(const LAS v4u*)(L + L_QVEC + (lane & 7) * 256 + s * 64 + (lane >> 4) * 16);
  }
  float sc[4][8];
#pragma unroll
  for (int i = 0; i < 4; ++i) {
    v4u kf[8];
    if (i < 2) {
#pragma unroll
      for (int q = 0; q < 8; ++q) kf[q] = i == 0 ? tw[q] : tx[q];
    } else {
      const int n = offK + w + 8 * i;
      wait_landed(L, n);
      kfrags(L, n & 15, kl, kf);
      release(L, w, lane, i < 3 ? n + 8 : offV);
    }
#pragma unroll
    for (int T = 0; T < 2; ++T) {
      f32x4 a = {0.f, 0.f, 0.f, 0.f};
#pragma unroll
      for (int s = 0; s < 4; ++s) a = mfma(kf[T * 4 + s], qf[s], a);
      const f32x2 lo = rbf2(rbf2(f32x2{a.x, a.y}) * 0.08838834764831845f), hi = rbf2(rbf2(f32x2{a.z, a.w}) * 0.08838834764831845f);
      sc[i][T * 4] = lo.x; sc[i][T * 4 + 1] = lo.y; sc[i][T * 4 + 2] = hi.x; sc[i][T * 4 + 3] = hi.y;
    }
  }
  float mw = sc[0][0];
  {
#pragma unroll
    for (int i = 0; i < 4; ++i)
#pragma unroll
      for (int r = 0; r < 8; ++r) mw = fmaxf(mw, sc[i][r]);
    mw = max32(max16(mw));
    float lw = 0.f;
#pragma unroll
    for (int i = 0; i < 4; ++i)
#pragma unroll
      for (int r = 0; r < 8; ++r) { sc[i][r] = exp_(sc[i][r] - mw); lw += sc[i][r]; }
    lw = sum32(sum16(lw));
    if (lane < 8) *(LAS f32x2*)(L + L_MLW + w * 64 + lane * 8) = f32x2{mw, lw};
    lds_inc(L, L_MLCNT, lane);
  }
#pragma unroll
  for (int q = 0; q < VPARK; ++q) {
    wait_landed(L, offV + q);
    if (q < 8) tw[q & 7] = tfrag(L, (offV + q) & 15, tl); else tx[q & 7] = tfrag(L, (offV + q) & 15, tl);
    release(L, w, lane, offV + q + 1);
  }
  lds_wait_ge(L, L_FML, 1);
  {
    const f32x2 ml = *(const LAS f32x2*)(L + L_MLF + (lane & 7) * 8);
    const float f = exp_(mw - ml.x) * ml.y;
#pragma unroll
    for (int i = 0; i < 4; ++i) {
      const v4u p = {pk(sc[i][0] * f, sc[i][1] * f), pk(sc[i][2] * f, sc[i][3] * f), pk(sc[i][4] * f, sc[i][5] * f), pk(sc[i][6] * f, sc[i][7] * f)};
      if ((lane & 15) < 8) *(LAS v4u*)(L + L_P + (w + 8 * i) * 512 + ((lane >> 4) * 8 + (lane & 7)) * 16) = p;
    }
    cbar(L, lane, bar);
  }
  f32x4 oacc = {0.f, 0.f, 0.f, 0.f};
  const int pl = L_P + ((lane >> 4) * 8 + (lane & 7)) * 16;
#pragma unroll
  for (int i = 0; i < VPARK; ++i) oacc = mfma(i < 8 ? tw[i & 7] : tx[i & 7], *(const LAS v4u*)(L + pl + i * 512), oacc);
#pragma unroll 1
  for (int i = VPARK; i < 32; i += 2) {
    const int n = offV + i;
    wait_landed(L, n + 1); wait_landed(L, n);
    const v4u a0 = tfrag(L, n & 15, tl), a1 = tfrag(L, (n + 1) & 15, tl);
    const v4u p0 = *(const LAS v4u*)(L + pl + i * 512), p1 = *(const LAS v4u*)(L + pl + (i + 1) * 512);
    release(L, w, lane, n + 2);
    oacc = mfma(a0, p0, oacc);
    oacc = mfma(a1, p1, oacc);
  }
  if ((lane & 15) < 8) __builtin_amdgcn_raw_buffer_store_b128(asu(oacc), rs, mybox + BX_O + w * 512 + ((lane >> 4) * 8 + (lane & 15)) * 16, 0, AUX_SC1);

  if (isA) {
    v4u g[4]; int off[4];
#pragma unroll
    for (int q = 0; q < 4; ++q) off[q] = box((sg + 8 * q) * 8 + (w >> 2) * 4 + a_hkv) + BX_O + a_dt * 512 + (w & 3) * 128 + piece * 16;
#pragma unroll
    for (int q = 0; q < 4; ++q) { wait_landed(L, offW + q); tw[q] = tfrag(L, (offW + q) & 15, tl); }
    __builtin_amdgcn_s_sleep(4);
    gather<4>(g, off, rs);
    const float t = tred(sum4(g));
    *(LAS u16*)(L + L_TVEC + (w >> 2) * VROW + (16 * piece + 4 * (w & 3) + comp) * 2) = (u16)pk(t, t);
    WGBAR();
    tile_pair(L, L_TVEC, wf, tw, rs, mybox + BX_T1 + w * 128, lane);
  } else if (isC) {
    if (w < 4) {
      const int col = 64 * ci + 16 * w + 4 * (piece & 3) + comp;
      const float xres = lo2f(*(const GAS u16*)(x + (piece >> 2) * D + col)), bias = lo2f(*(const GAS u16*)(b2 + col));
      lds_wait_ge(L, L_FGATE, 1);
      const float ga = *(const LAS float*)(L + L_GATE + w * 256 + lane * 4), gb = *(const LAS float*)(L + L_GATE + (4 + w) * 256 + lane * 4);
      const int ct = 4 * ci + w, cg = ct >> 4, ti = ct & 15;
      v4u g[4]; int off[4], off2[4];
#pragma unroll
      for (int q = 0; q < 4; ++q) {
        const int a = (sg + 8 * q) * 4 + cg;
        off[q] = box((a & 15) * 8 + (a >> 4)) + BX_T1 + ti * 128 + piece * 16;
        const int kf = sg + 8 * q, bb = kf * 4 + cg, gg = bb / 14;
        off2[q] = kf < 28 ? box((16 + bb - gg * 14) * 8 + gg) + BX_T2 + ti * 128 + piece * 16 : WS_BYTES;
        PIN(off2[q]);
      }
      gather<4>(g, off, rs);
      const float x1 = rbf(xres + rbf(ga * rbf(tred(sum4(g)))));
      const f32x4 x4 = texp(x1);
      __builtin_amdgcn_raw_buffer_store_b128(asu(x4), rs, mybox + BX_X1 + sg * 512 + w * 128 + piece * 16, 0, AUX_SC1);
      gather<4>(g, off2, rs);
      const float o = x1 + rbf(gb * rbf(rbf(tred(sum4(g))) + bias));
      if (!(lane & 8)) *(GAS u16*)(out + (piece >> 2) * D + col) = (u16)pk(o, o);
    }
  } else {
    f32x4 sbv, cb1;
    float bias;
    {
      v4u g2[2];
      const int off[2] = {box(30 * 8 + (w >> 1) + (w & 1) * 8) + BX_T1 + lane * 16, box(30 * 8 + 4 + (w >> 1) + (w & 1) * 8) + BX_T1 + lane * 16};
      gather<2>(g2, off, rs);
      sbv = asf(g2[0]);
      const f32x4 cb = asf(g2[1]);
      const f32x2 c0 = rbf2(f32x2{cb.x, cb.y} + 1.f), c1 = rbf2(f32x2{cb.z, cb.w} + 1.f);
      cb1 = f32x4{c0.x, c0.y, c1.x, c1.y};
      bias = lo2f(*(const GAS u16*)(b1 + (bi >> 2) * 128 + 16 * w + 4 * (piece & 3) + comp));
      PIN(bias); PIN(sbv); PIN(cb1);
    }
#pragma unroll
    for (int q = 0; q < 8; ++q) { wait_landed(L, offW + q); tw[q] = tfrag(L, (offW + q) & 15, tl); }
    const int cc = 2 * w + (sg >> 2);
    const int offx[1] = {box((30 + (cc & 1)) * 8 + (cc >> 1)) + BX_X1 + grp * 512 + (sg & 3) * 128 + piece * 16};
    const int kf = bi >> 2, bb = (kf >> 1) * 8 + sg, gg = bb / 14;
    int offh[1] = {box((16 + bb - gg * 14) * 8 + gg) + BX_T1 + ((kf & 1) * 8 + w) * 128 + piece * 16};
    PIN(offh[0]);
    wait_landed(L, offW + 14); wait_landed(L, offW + 15);
    v4u g[1];
    gather<1>(g, offx, rs);
    const f32x4 x1 = asf(g[0]);
    float m, rden;
    stat_put(L, L_ST1, w, lane, x1);
    WGBAR();
    stat_get(L, L_ST1, lane, m, rden);
    const f32x2 ha = rbf2(rbf2(ln2(f32x2{x1.x, x1.y}, m, rden) * f32x2{cb1.x, cb1.y}) + f32x2{sbv.x, sbv.y});
    const f32x2 hb = rbf2(rbf2(ln2(f32x2{x1.z, x1.w}, m, rden) * f32x2{cb1.z, cb1.w}) + f32x2{sbv.z, sbv.w});
    stat_put(L, L_ST2, w, lane, f32x4{ha.x, ha.y, hb.x, hb.y});
    WGBAR();
    if (w == (bi & 7)) {
      stat_get(L, L_ST2, lane, m, rden);
      const f32x2 ya = rbf2(ha - m) * rden, yb = rbf2(hb - m) * rden;
      *(LAS v2u*)(L + L_TVEC + (piece >> 2) * VROW + (16 * sg + 4 * (piece & 3)) * 2) = v2u{pk(ya.x, ya.y), pk(yb.x, yb.y)};
    }
    WGBAR();
    tile_pair(L, L_TVEC, tw, tw + 4, rs, mybox + BX_T1 + w * 128, lane);
#pragma unroll
    for (int q = 0; q < 8; ++q) { wait_landed(L, offW + 8 + q); tw[q] = tfrag(L, (offW + 8 + q) & 15, tl); }
    __builtin_amdgcn_s_sleep(5);
    gather<1>(g, offh, rs);
    const float u = rbf(rbf(tred(asf(g[0]))) + bias);
    const float z = u * (0.7978845608028654f + 0.035677408136300125f * u * u);
    const float hh = u * rcp_(1.f + __builtin_amdgcn_exp2f(-2.8853900817779268f * z));
    *(LAS u16*)(L + L_UVEC + (piece >> 2) * VROW + (16 * w + 4 * (piece & 3) + comp) * 2) = (u16)pk(hh, hh);
    WGBAR();
    tile_pair(L, L_UVEC, tw, tw + 4, rs, mybox + BX_T2 + w * 128, lane);
  }
}

}

extern "C" void kernel_launch(void* const* d_in, const int* in_sizes, int n_in, void* d_out, int out_size, void* d_ws, size_t ws_size,
                              hipStream_t stream) {
  (void)in_sizes; (void)n_in; (void)out_size; (void)ws_size;
  (void)hipFuncSetAttribute((const void*)fused_block, hipFuncAttributeMaxDynamicSharedMemorySize, LDS_BYTES);
  hipLaunchKernelGGL(fused_block, dim3(256), dim3(NTHR), LDS_BYTES, stream, (const u16*)d_in[0], (const u16*)d_in[1], (const u16*)d_in[2],
                     (const u16*)d_in[3], (const u16*)d_in[4], (const u16*)d_in[6], (const u16*)d_in[7], (const u16*)d_in[8],
                     (const u16*)d_in[9], (const u16*)d_in[10], (const u16*)d_in[11], (u16*)d_out, (unsigned char*)d_ws);
}
```

```cpp
#include <hip/hip_runtime.h>
#include <hip/hip_bf16.h>
#include <stdint.h>

#define LAS __attribute__((address_space(3)))
#define GAS __attribute__((address_space(1)))
typedef unsigned short u16;
typedef unsigned v4u __attribute__((ext_vector_type(4)));
typedef unsigned v2u __attribute__((ext_vector_type(2)));
typedef float f32x4 __attribute__((ext_vector_type(4)));
typedef float f32x2 __attribute__((ext_vector_type(2)));
typedef short s16x4 __attribute__((ext_vector_type(4)));
typedef short bf16x8 __attribute__((ext_vector_type(8)));
typedef __bf16 bf16x2 __attribute__((ext_vector_type(2)));

namespace {

constexpr int D = 1024, SK = 32768;
constexpr int NTHR = 704;
constexpr int W_PRO = 18, W_KV = 12, W_WO = 16, W_MLP = 10;
constexpr int VPARK = 12;

constexpr int WS_EPOCH = 0, WS_ALIVE = 4096, WS_BOX = 65536, BOX = 16384;
constexpr int BX_MC = 0;
constexpr int BX_Q = 1024;
constexpr int BX_ML = 2048;
constexpr int BX_MR = 3072;
constexpr int BX_O = 6144;
constexpr int BX_T1 = 10240;
constexpr int BX_X1 = BX_T1 + 1024;
constexpr int BX_T2 = 12288;
constexpr int WS_BYTES = WS_BOX + 256 * BOX;
constexpr unsigned SENT = 0xFFFFFFFFu;
constexpr unsigned SPIN_CAP = 1u << 15, LDS_CAP = 1u << 20;
constexpr int AUX_SC1 = 16;

constexpr int L_RING = 0, L_P = 131072, L_CTL = L_P + 16384;
constexpr int L_X = L_P, L_C = L_P + 4096, L_STG = L_P + 8192;
constexpr int L_LANDED = L_CTL;
constexpr int L_PROG = L_CTL + 32;
constexpr int L_BAR = L_CTL + 64, L_MLCNT = L_CTL + 68, L_FALIVE = L_CTL + 72, L_FML = L_CTL + 76, L_FAUX = L_CTL + 80, L_FGATE = L_CTL + 88, L_FCLR = L_CTL + 92;
constexpr int L_SVEC = L_CTL + 256;
constexpr int VROW = 320;
constexpr int L_HVEC = L_SVEC + 4096;
constexpr int L_QVEC = L_HVEC + 640;
constexpr int L_ST1 = L_QVEC + 2048;
constexpr int L_ST2 = L_ST1 + 128;
constexpr int L_MLW = L_ST2 + 128;
constexpr int L_MLF = L_MLW + 512;
constexpr int L_TVEC = L_MLF + 64;
constexpr int L_UVEC = L_TVEC + 640;
constexpr int L_SBST = L_TVEC;
constexpr int L_GATE = L_UVEC + 640;
constexpr int LDS_BYTES = L_GATE + 2048;
static_assert(LDS_BYTES <= 160 * 1024 && LDS_BYTES > 80 * 1024, "one workgroup per CU");

typedef LAS unsigned char* lptr;

__device__ __forceinline__ float lo2f(unsigned w) { return __uint_as_float(w << 16); }
__device__ __forceinline__ float hi2f(unsigned w) { return __uint_as_float(w & 0xffff0000u); }
__device__ __forceinline__ unsigned pk(float a, float b) {
  bf16x2 v = {(__bf16)a, (__bf16)b};
  return __builtin_bit_cast(unsigned, v);
}
__device__ __forceinline__ float rbf(float a) { return hi2f(pk(a, a)); }
__device__ __forceinline__ f32x2 rbf2(f32x2 a) {
  unsigned p = pk(a.x, a.y);
  asm("" : "+v"(p));
  return f32x2{lo2f(p), hi2f(p)};
}
__device__ __forceinline__ f32x4 mfma(v4u a, v4u b, f32x4 c) {
  return __builtin_amdgcn_mfma_f32_16x16x32_bf16(__builtin_bit_cast(bf16x8, a), __builtin_bit_cast(bf16x8, b), c, 0, 0, 0);
}
template <int CTRL> __device__ __forceinline__ float dpp(float x) {
  return __builtin_bit_cast(float, __builtin_amdgcn_mov_dpp(__builtin_bit_cast(int, x), CTRL, 0xf, 0xf, true));
}
constexpr int XOR1 = 0xB1, XOR2 = 0x4E, ROR4 = 0x124, ROR8 = 0x128;
__device__ __forceinline__ f32x2 swap32(float a, float b) {
  auto s = __builtin_amdgcn_permlane32_swap(__float_as_uint(a), __float_as_uint(b), false, false);
  return f32x2{__uint_as_float(s[0]), __uint_as_float(s[1])};
}
__device__ __forceinline__ f32x2 swap16(float a, float b) {
  auto s = __builtin_amdgcn_permlane16_swap(__float_as_uint(a), __float_as_uint(b), false, false);
  return f32x2{__uint_as_float(s[0]), __uint_as_float(s[1])};
}
__device__ __forceinline__ float sum16(float x) { const f32x2 s = swap16(x, x); return s.x + s.y; }
__device__ __forceinline__ float sum32(float x) { const f32x2 s = swap32(x, x); return s.x + s.y; }
__device__ __forceinline__ float max16(float x) { const f32x2 s = swap16(x, x); return fmaxf(s.x, s.y); }
__device__ __forceinline__ float max32(float x) { const f32x2 s = swap32(x, x); return fmaxf(s.x, s.y); }
__device__ __forceinline__ float tred(f32x4 v) {
  const f32x2 a = swap32(v.x, v.y), b = swap32(v.z, v.w);
  const f32x2 c = swap16(a.x + a.y, b.x + b.y);
  const float t = c.x + c.y;
  return t + dpp<ROR8>(t);
}
__device__ __forceinline__ int tcomp(int lane) { return ((lane >> 4) & 1) * 2 + (lane >> 5); }
__device__ __forceinline__ f32x4 texp(float t) {
  const f32x2 a = swap16(t, t);
  const f32x2 b = swap32(a.x, a.x), c = swap32(a.y, a.y);
  return f32x4{b.x, b.y, c.x, c.y};
}
__device__ __forceinline__ f32x4 asf(v4u v) { return __builtin_bit_cast(f32x4, v); }
__device__ __forceinline__ v4u asu(f32x4 v) { return __builtin_bit_cast(v4u, v); }
__device__ __forceinline__ unsigned umax4(v4u v) { return max(max(max(v.x, v.y), v.z), v.w); }
__device__ __forceinline__ float exp_(float x) { return __builtin_amdgcn_exp2f(x * 1.4426950408889634f); }
__device__ __forceinline__ float rcp_(float x) { return __builtin_amdgcn_rcpf(x); }

#define LGKM0() asm volatile("s_waitcnt lgkmcnt(0)" ::: "memory")
#define VM0() asm volatile("s_waitcnt vmcnt(0)" ::: "memory")
#define CFENCE() asm volatile("" ::: "memory")
#define PIN(x) asm volatile("" : "+v"(x))
#define WGBAR() do { LGKM0(); __builtin_amdgcn_s_barrier(); CFENCE(); } while (0)

__device__ __forceinline__ void dma16(unsigned voff, const unsigned char* sbase, unsigned ldsaddr) {
  unsigned keep;
  asm volatile("s_mov_b32 %0, m0\n\ts_mov_b32 m0, %3\n\ts_nop 4\n\tglobal_load_lds_dwordx4 %1, %2 sc0 nt\n\ts_mov_b32 m0, %0"
               : "=&s"(keep) : "v"(voff), "s"(sbase), "s"(ldsaddr) : "memory");
}
__device__ __forceinline__ void lds_set(lptr L, int off, int lane, unsigned v) {
  if (lane == 0) *(volatile LAS unsigned*)(L + off) = v;
}
__device__ __forceinline__ void lds_wait_ge(lptr L, int off, unsigned need) {
#pragma nounroll
  for (unsigned sp = 0; (unsigned)__builtin_amdgcn_readfirstlane(*(volatile LAS unsigned*)(L + off)) < need && sp < LDS_CAP; ++sp) { }
  CFENCE();
}
__device__ __forceinline__ void wait_landed(lptr L, int n) { lds_wait_ge(L, L_LANDED + 4 * (n & 1), (unsigned)(n >> 1) + 1u); }
__device__ __forceinline__ void release(lptr L, int w, int lane, int n) { LGKM0(); lds_set(L, L_PROG + 4 * w, lane, (unsigned)n); }
__device__ __forceinline__ void lds_inc(lptr L, int off, int lane) {
  LGKM0();
  if (lane == 0) __hip_atomic_fetch_add((LAS unsigned*)(L + off), 1u, __ATOMIC_RELAXED, __HIP_MEMORY_SCOPE_WORKGROUP);
}
__device__ __forceinline__ void cbar(lptr L, int lane, unsigned& target) {
  target += 8;
  lds_inc(L, L_BAR, lane);
  lds_wait_ge(L, L_BAR, target);
}
__device__ __forceinline__ v4u tfrag(lptr L, int slot, int tl) {
  const s16x4 a = __builtin_amdgcn_ds_read_tr16_b64_v4i16((LAS s16x4*)(L + L_RING + slot * 8192 + tl));
  const s16x4 b = __builtin_amdgcn_ds_read_tr16_b64_v4i16((LAS s16x4*)(L + L_RING + slot * 8192 + tl + 1024));
  const v2u x = __builtin_bit_cast(v2u, a), y = __builtin_bit_cast(v2u, b);
  return v4u{x.x, x.y, y.x, y.y};
}
__device__ __forceinline__ void kfrags(lptr L, int slot, int kl, v4u (&f)[8]) {
#pragma unroll
  for (int q = 0; q < 8; ++q) f[q] = *(const LAS v4u*)(L + L_RING + slot * 8192 + (q >> 2) * 1024 + (kl ^ ((q & 3) << 6)));
}
__device__ __forceinline__ v4u sfrag(lptr L, int lane, int k0) {
  return *(const LAS v4u*)(L + L_SVEC + (lane & 1) * 2048 + k0 * 2 + (lane >> 4) * 16);
}
__device__ __forceinline__ v4u vfrag(lptr L, int off, int lane, int s) {
  return *(const LAS v4u*)(L + off + (lane & 1) * VROW + s * 64 + (lane >> 4) * 16);
}
template <int N>
__device__ __forceinline__ void gather_issue(v4u (&v)[N], const int (&off)[N], __amdgpu_buffer_rsrc_t rs) {
#pragma unroll
  for (int q = 0; q < N; ++q) v[q] = __builtin_amdgcn_raw_buffer_load_b128(rs, off[q], 0, AUX_SC1);
}
template <int N, int PAUSE>
__device__ __forceinline__ void gather_finish(v4u (&v)[N], const int (&off)[N], __amdgpu_buffer_rsrc_t rs) {
#pragma nounroll
  for (unsigned sp = 0; sp < SPIN_CAP; ++sp) {
    unsigned mx = umax4(v[0]);
#pragma unroll
    for (int q = 1; q < N; ++q) mx = max(mx, umax4(v[q]));
    if (__builtin_amdgcn_ballot_w64(mx == SENT) == 0) break;
    if (PAUSE) __builtin_amdgcn_s_sleep(PAUSE);
    CFENCE();
    if (mx == SENT) gather_issue<N>(v, off, rs);
  }
}
template <int N, int PAUSE = 0>
__device__ __forceinline__ void gather(v4u (&v)[N], const int (&off)[N], __amdgpu_buffer_rsrc_t rs) {
  gather_issue<N>(v, off, rs);
  gather_finish<N, PAUSE>(v, off, rs);
}
__device__ __forceinline__ f32x4 sum4(const v4u (&g)[4]) { return (asf(g[0]) + asf(g[1])) + (asf(g[2]) + asf(g[3])); }
__device__ __forceinline__ void tile_pair(lptr L, int vec, const v4u* f0, const v4u* f1, __amdgpu_buffer_rsrc_t rs, int off, int lane) {
  f32x4 a0 = {0.f, 0.f, 0.f, 0.f}, a1 = {0.f, 0.f, 0.f, 0.f};
#pragma unroll
  for (int s = 0; s < 4; ++s) { const v4u bv = vfrag(L, vec, lane, s); a0 = mfma(f0[s], bv, a0); a1 = mfma(f1[s], bv, a1); }
  if ((lane & 15) < 2) {
    __builtin_amdgcn_raw_buffer_store_b128(asu(a0), rs, off + (lane & 1) * 64 + (lane >> 4) * 16, 0, AUX_SC1);
    __builtin_amdgcn_raw_buffer_store_b128(asu(a1), rs, off + 1024 + (lane & 1) * 64 + (lane >> 4) * 16, 0, AUX_SC1);
  }
}
__device__ __forceinline__ int box(int j) { return WS_BOX + j * BOX; }
__device__ __forceinline__ void ln_consts(float s, float q, float& m, float& rden) {
  const float mean = s * (1.f / D);
  const float var = fmaxf(q * (1.f / D) - mean * mean, 0.f);
  m = rbf(mean);
  rden = rcp_(rbf(__builtin_amdgcn_sqrtf(rbf(rbf(var) + 1e-5f))));
}
__device__ __forceinline__ f32x2 ln2(f32x2 x, float m, float rden) { return rbf2(rbf2(x - m) * rden); }
__device__ __forceinline__ void stat_put(lptr L, int off, int w, int lane, f32x4 v) {
  const float s = (v.x + v.y) + (v.z + v.w), q = (v.x * v.x + v.y * v.y) + (v.z * v.z + v.w * v.w);
  const f32x2 a = swap32(s, q);
  float r = sum16(a.x + a.y);
  r += dpp<ROR8>(r); r += dpp<XOR2>(r); r += dpp<XOR1>(r);
  if ((lane & 0x1B) == 0) *(LAS float*)(L + off + w * 16 + (lane & 4) * 2 + (lane >> 5) * 4) = r;
}
__device__ __forceinline__ void stat_get(lptr L, int off, int lane, float& m, float& rden) {
  const f32x2 p = *(const LAS f32x2*)(L + off + (lane >> 3) * 16 + (lane & 4) * 2);
  const f32x2 a = swap32(p.x, p.y);
  float r = sum16(a.x + a.y);
  r += dpp<ROR8>(r);
  const f32x2 t = swap32(r, r);
  ln_consts(t.x, t.y, m, rden);
}

__device__ __forceinline__ int read_minprog(lptr L, int lane) {
  unsigned m = *(volatile LAS unsigned*)(L + L_PROG + 4 * (lane & 7));
  m = min(m, (unsigned)__builtin_amdgcn_mov_dpp((int)m, XOR1, 0xf, 0xf, true));
  m = min(m, (unsigned)__builtin_amdgcn_mov_dpp((int)m, XOR2, 0xf, 0xf, true));
  m = min(m, (unsigned)__builtin_amdgcn_mov_dpp((int)m, ROR4, 0xf, 0xf, true));
  return __builtin_amdgcn_readfirstlane((int)m);
}
template <int W>
__device__ __forceinline__ void load_block(lptr L, int ld, int lane, int n, const unsigned char* bp, int ldb, int hi, int& minprog) {
  if (minprog < n - 15) {
    minprog = read_minprog(L, lane);
    if (minprog < n - 15) {
      VM0();
      lds_set(L, L_LANDED + 4 * ld, lane, (unsigned)(n >> 1));
      for (unsigned sp = 0; sp < LDS_CAP; ++sp) {
        minprog = read_minprog(L, lane);
        if (minprog >= n - 15) break;
      }
    }
  }
  const unsigned voff = (unsigned)((lane >> 4) * ldb + (((lane & 15) ^ ((lane >> 4) << 2)) << 4));
  const unsigned dst = (unsigned)(size_t)L + L_RING + (unsigned)(n & 15) * 8192u;
#pragma unroll
  for (int p = 0; p < 8; ++p) {
    dma16(voff, bp + (size_t)((p & 3) * 4) * ldb + (size_t)(p >> 2) * hi, dst + p * 1024);
    asm volatile("s_waitcnt vmcnt(%0)" ::"i"(W - 1) : "memory");
    if (((p + 2 - W) & 7) == 0) {
      const int done = (n >> 1) + (p + 2 - W) / 8;
      if (done > 0) lds_set(L, L_LANDED + 4 * ld, lane, (unsigned)done);
    }
  }
}

__global__ void __launch_bounds__(NTHR) fused_block(const u16* __restrict__ x, const u16* __restrict__ kc, const u16* __restrict__ vc,
                                                     const u16* __restrict__ wq, const u16* __restrict__ wo, const u16* __restrict__ w1,
                                                     const u16* __restrict__ b1, const u16* __restrict__ w2, const u16* __restrict__ b2,
                                                     const u16* __restrict__ cvec, const u16* __restrict__ wmod, u16* __restrict__ out,
                                                     unsigned char* ws) {
  extern __shared__ __attribute__((aligned(16))) unsigned char smem[];
  lptr L = (lptr)smem;
  const int tid = threadIdx.x, lane = tid & 63, w = __builtin_amdgcn_readfirstlane(tid >> 6);
  const int j = blockIdx.x, grp = j & 7, split = j >> 3, batt = grp >> 2, hkv = grp & 3;
  const bool isA = split < 16, isC = split >= 30, isB = !isA && !isC;
  const int kq = split & 7, g32 = grp * 4 + (split >> 3), g16 = grp * 2 + ((split >> 3) & 1);
  const int mu0 = isA ? 2 * (grp * 16 + split) : 256 + 2 * (grp * 16 + split - 16);
  const int offQ = isA ? 0 : 4, offMR = offQ + 4;
  constexpr int offWO = 8, offK = 12, offV = offK + 32, offW = offV + 32;
  const int ai = grp * 16 + split, a_hkv = ai >> 5, a_dt = (ai >> 2) & 7, a_cg = ai & 3;
  const int bi = grp * 14 + (split - 16), ci = grp * 2 + (split - 30);

  if (tid < 64) *(volatile LAS unsigned*)(L + L_CTL + 4 * tid) = 0u;
  WGBAR();
  unsigned long long epoch = 0, tag = 0;
  if (w == 10) {
    const __amdgpu_buffer_rsrc_t rsh = __builtin_amdgcn_make_buffer_rsrc((void*)ws, 0, WS_BYTES, 0x00020000);
    const v2u ep = __builtin_amdgcn_raw_buffer_load_b64(rsh, WS_EPOCH, 0, AUX_SC1);
    const v4u s4 = {SENT, SENT, SENT, SENT};
    const unsigned used = isA ? 0x0FDEu : (isB ? 0x3FDFu : 0x7FDFu);
#pragma unroll 1
    for (int u = 0; u < 15; ++u)
      if ((used >> u) & 1) __builtin_amdgcn_raw_buffer_store_b128(s4, rsh, box(j) + u * 1024 + lane * 16, 0, AUX_SC1);
    VM0();
    lds_set(L, L_FCLR, lane, 1u);
    epoch = ((unsigned long long)(unsigned)__builtin_amdgcn_readfirstlane((int)ep.y) << 32) | (unsigned)__builtin_amdgcn_readfirstlane((int)ep.x);
    tag = (epoch ^ 0x9E3779B97F4A7C15ull) * 0xD6E8FEB86659FD93ull + 1ull;
    if (lane == 0) __hip_atomic_store((GAS unsigned long long*)(ws + WS_ALIVE + j * 16), tag, __ATOMIC_RELAXED, __HIP_MEMORY_SCOPE_AGENT);
  }
  if (__builtin_expect(w == 8 || w == 9, 1)) {
    const int ld = w - 8;
    __builtin_amdgcn_s_setprio(3);
    int minprog = 0;
    const unsigned char* aux = (const unsigned char*)(ld ? cvec : x);
#pragma unroll
    for (int p = 0; p < 4; ++p) dma16((unsigned)lane * 16u, aux + p * 1024, (unsigned)(size_t)L + L_X + ld * 4096 + p * 1024);
    const size_t kvoff = ((size_t)grp * SK + split * 1024) * 256;
    const unsigned char* wo_t = (const unsigned char*)wo + ((size_t)(a_hkv * 8) * 128 + 16 * a_dt) * 2048 + a_cg * 512;
#pragma unroll 1
    for (int n = ld; n < offW; n += 2) {
      if (n < offK) {
        const unsigned char* bp; int ldb, hi;
        if (isA && n >= offWO) { bp = wo_t + (size_t)(n - offWO) * 256 * 2048; ldb = 2048; hi = 128 * 2048; }
        else {
          if (n < offQ) { bp = (const unsigned char*)wmod + (size_t)kq * 128 * 12288 + g16 * 256; ldb = 12288; }
          else if (n < offMR) { bp = (const unsigned char*)wq + (size_t)kq * 128 * 8192 + g32 * 256; ldb = 8192; }
          else { const int u = mu0 + ((n - offMR) >> 1); bp = (const unsigned char*)wmod + (size_t)(u & 15) * 64 * 12288 + 4096 + (u >> 4) * 256; ldb = 12288; }
          bp += (size_t)((n < offMR ? n & 3 : (n - offMR) & 1) * 32) * ldb; hi = 16 * ldb;
        }
        load_block<W_PRO>(L, ld, lane, n, bp, ldb, hi, minprog);
        if (n < 2) {
          asm volatile("s_waitcnt vmcnt(8)" ::: "memory");
          lds_set(L, L_FAUX + 4 * ld, lane, 1u);
        }
      } else {
        const unsigned char* bp = n < offV ? (const unsigned char*)kc + kvoff + (size_t)(n - offK) * 8192 : (const unsigned char*)vc + kvoff + (size_t)(n - offV) * 8192;
        load_block<W_KV>(L, ld, lane, n, bp, 256, 4096, minprog);
      }
    }
    int nend = offW;
    if (isA) {
      nend = offW + 4;
#pragma unroll 1
      for (int n = offW + ld; n < nend; n += 2) load_block<W_WO>(L, ld, lane, n, wo_t + (size_t)(n - offW) * 256 * 2048 + 256, 2048, 128 * 2048, minprog);
    }
    if (isB) {
      nend = offW + 16;
#pragma unroll 1
      for (int n = offW + ld; n < nend; n += 2) {
        const int q = n - offW;
        const unsigned char* bp; int ldb;
        if (q < 8) { bp = (const unsigned char*)w1 + (size_t)(bi & 7) * 128 * 7168 + (bi >> 3) * 512; ldb = 7168; }
        else { bp = (const unsigned char*)w2 + (size_t)(bi >> 2) * 128 * 2048 + (bi & 3) * 512; ldb = 2048; }
        load_block<W_MLP>(L, ld, lane, n, bp + (size_t)((q & 3) * 32) * ldb + ((q >> 2) & 1) * 256, ldb, 16 * ldb, minprog);
      }
    }
    VM0();
    lds_set(L, L_LANDED + 4 * ld, lane, (unsigned)(nend >> 1));
    return;
  }

  const __amdgpu_buffer_rsrc_t rs = __builtin_amdgcn_make_buffer_rsrc((void*)ws, 0, WS_BYTES, 0x00020000);
  const int mybox = box(j);
  const int piece = lane & 7, sg = lane >> 3, comp = tcomp(lane);

  if (w == 10) {
    for (int i = 0; i < 3; ++i) __builtin_amdgcn_s_sleep(24);
    for (unsigned sp = 0; sp < SPIN_CAP; ++sp) {
      bool ok = true;
#pragma unroll
      for (int q = 0; q < 4; ++q) {
        const v2u t = __builtin_amdgcn_raw_buffer_load_b64(rs, WS_ALIVE + (q * 64 + lane) * 16, 0, AUX_SC1);
        ok = ok && t.x == (unsigned)tag && t.y == (unsigned)(tag >> 32);
      }
      if (__all(ok)) break;
      __builtin_amdgcn_s_sleep(4);
      CFENCE();
    }
    lds_set(L, L_FALIVE, lane, 1u);
    if (j == 0 && lane == 0) __hip_atomic_store((GAS unsigned long long*)(ws + WS_EPOCH), epoch + 1ull, __ATOMIC_RELAXED, __HIP_MEMORY_SCOPE_AGENT);
    if (isC) {
      lds_wait_ge(L, L_LANDED, (unsigned)(offK + 8) >> 1);
      auto mr = [&](int cg, int kh, int line) {
        const int u = cg * 16 + kh;
        const int ia = (u >> 1) & 127, jj = ((u < 256 ? 0 : 16) + (ia & 15)) * 8 + (ia >> 4);
        return box(jj) + BX_MR + (u & 1) * 1024 + line * 128 + piece * 16;
      };
#pragma unroll 1
      for (int h4 = 0; h4 < 4; ++h4) {
        const int cg = h4 < 2 ? 8 + ci : (h4 == 2 ? 0 : 24) + (ci >> 1), l0 = h4 < 2 ? 4 * h4 : (ci & 1) * 4;
        const int o0 = mr(cg, sg, l0), o1 = mr(cg, sg + 8, l0);
        v4u g[8];
        const int off[8] = {o0, o1, o0 + 128, o1 + 128, o0 + 256, o1 + 256, o0 + 384, o1 + 384};
        gather<8, 8>(g, off, rs);
#pragma unroll
        for (int q = 0; q < 4; ++q) {
          const float t = rbf(tred(asf(g[2 * q]) + asf(g[2 * q + 1])));
          if (h4 < 2) *(LAS float*)(L + L_SBST + (l0 + q) * 128 + piece * 16 + comp * 4) = t;
          else *(LAS float*)(L + L_GATE + ((h4 - 2) * 4 + q) * 256 + lane * 4) = t;
        }
      }
      LGKM0();
      __builtin_amdgcn_raw_buffer_store_b128(*(const LAS v4u*)(L + L_SBST + lane * 16), rs, mybox + BX_T1 + lane * 16, 0, AUX_SC1);
      lds_set(L, L_FGATE, lane, 1u);
    }
    lds_wait_ge(L, L_MLCNT, 8);
    const f32x2 r = *(const LAS f32x2*)(L + L_MLW + sg * 64 + piece * 8);
    float M = r.x;
    M = fmaxf(M, dpp<ROR8>(M)); M = max32(max16(M));
    float Ls = r.y * exp_(r.x - M);
    Ls += dpp<ROR8>(Ls); Ls = sum32(sum16(Ls));
    const float M1 = dpp<XOR1>(M), L1 = dpp<XOR1>(Ls);
    if (lane < 8 && !(lane & 1)) __builtin_amdgcn_raw_buffer_store_b128(asu(f32x4{M, Ls, M1, L1}), rs, mybox + BX_ML + lane * 8, 0, AUX_SC1);
    for (int i = 0; i < 2; ++i) __builtin_amdgcn_s_sleep(32);
    v4u g[2];
    const int off[2] = {box((lane >> 2) * 8 + grp) + BX_ML + (lane & 3) * 16, box(((lane >> 2) + 16) * 8 + grp) + BX_ML + (lane & 3) * 16};
    gather<2, 16>(g, off, rs);
    const f32x4 u = asf(g[0]), v = asf(g[1]);
    float M0 = fmaxf(u.x, v.x), Mb = fmaxf(u.z, v.z);
    M0 = fmaxf(M0, dpp<ROR4>(M0)); M0 = fmaxf(M0, dpp<ROR8>(M0)); M0 = max32(max16(M0));
    Mb = fmaxf(Mb, dpp<ROR4>(Mb)); Mb = fmaxf(Mb, dpp<ROR8>(Mb)); Mb = max32(max16(Mb));
    float L0 = u.y * exp_(u.x - M0) + v.y * exp_(v.x - M0), Lb = u.w * exp_(u.z - Mb) + v.w * exp_(v.z - Mb);
    L0 += dpp<ROR4>(L0); L0 += dpp<ROR8>(L0); L0 = sum32(sum16(L0));
    Lb += dpp<ROR4>(Lb); Lb += dpp<ROR8>(Lb); Lb = sum32(sum16(Lb));
    if (lane < 4) *(LAS f32x4*)(L + L_MLF + lane * 16) = f32x4{M0, 1.f / L0, Mb, 1.f / Lb};
    LGKM0();
    lds_set(L, L_FML, lane, 1u);
    return;
  }

  unsigned warm = 0;
  if (w == 0) {
    const unsigned char* p = (const unsigned char*)ws; size_t o = 0;
    const size_t kv0 = ((size_t)grp * SK + split * 1024) * 256;
    const int l = lane;
    if (l < 2) { p = (const unsigned char*)kc; o = kv0 + l * 131072; }
    else if (l < 4) { p = (const unsigned char*)vc; o = kv0 + (l - 2) * 131072; }
    else if (l < 8) { p = (const unsigned char*)wq; o = (size_t)kq * 128 * 8192 + (l - 4) * 262144 + g32 * 256; }
    else if (l < 14) { p = (const unsigned char*)wmod; o = (size_t)kq * 128 * 12288 + (l - 8) * 262144; }
    else if (l < 20) { const int u = mu0 + (l - 14) / 3; p = (const unsigned char*)wmod; o = (size_t)(u & 15) * 64 * 12288 + ((l - 14) % 3) * 262144 + 4096; }
    else if (l < 28) {
      if (isA) { p = (const unsigned char*)wo; o = (size_t)(a_hkv * 8) * 128 * 2048 + (l - 20) * 262144; }
      else if (isB && l < 24) { p = (const unsigned char*)w1; o = (size_t)(bi & 7) * 128 * 7168 + (l - 20) * 229376; }
      else if (isB) { p = (const unsigned char*)w2; o = (size_t)(bi >> 2) * 128 * 2048; }
    }
    else if (l < 45) o = (size_t)(l - 28) * 262144;
    else if (l == 45) p = (const unsigned char*)b1;
    else if (l == 46) p = (const unsigned char*)b2;
    asm volatile("global_load_dword %0, %1, off" : "=&v"(warm) : "v"(p + o) : "memory");
  }
  unsigned bar = 0;
  lds_wait_ge(L, L_FAUX, 1); lds_wait_ge(L, L_FAUX + 4, 1);
  asm volatile("s_waitcnt vmcnt(0)" : "+v"(warm) :: "memory");
  const unsigned xh = *(const LAS u16*)(L + L_X + ((piece >> 2) * D + kq * 128 + 16 * w + 4 * (piece & 3) + comp) * 2);
  {
    const v2u cr = *(const LAS v2u*)(L + L_C + tid * 8);
    float s[4] = {lo2f(cr.x), hi2f(cr.x), lo2f(cr.y), hi2f(cr.y)};
#pragma unroll
    for (int i = 0; i < 4; ++i) s[i] = s[i] * rcp_(1.f + exp_(-s[i]));
    *(LAS v2u*)(L + L_SVEC + tid * 8) = v2u{pk(s[0], s[1]), pk(s[2], s[3])};
  }
  {
    const v2u xr = *(const LAS v2u*)(L + L_X + tid * 8);
    const float a0 = lo2f(xr.x), a1 = hi2f(xr.x), a2 = lo2f(xr.y), a3 = hi2f(xr.y);
    const f32x2 sq = swap32((a0 + a1) + (a2 + a3), (a0 * a0 + a1 * a1) + (a2 * a2 + a3 * a3));
    float r = sum16(sq.x + sq.y);
    r += dpp<ROR8>(r); r += dpp<ROR4>(r); r += dpp<XOR2>(r); r += dpp<XOR1>(r);
    if ((lane & 31) == 0) *(LAS float*)(L + L_ST1 + w * 8 + (lane >> 5) * 4) = r;
  }
  cbar(L, lane, bar);
  float lnx;
  {
    const int b = piece >> 2;
    const f32x4 r0 = *(const LAS f32x4*)(L + L_ST1 + b * 32), r1 = *(const LAS f32x4*)(L + L_ST1 + b * 32 + 16);
    float m, rden;
    ln_consts((r0.x + r0.z) + (r1.x + r1.z), (r0.y + r0.w) + (r1.y + r1.w), m, rden);
    lnx = rbf(rbf(lo2f(xh) - m) * rden);
    PIN(lnx);
  }
  const int tl = (8 * (lane >> 4) + ((lane >> 2) & 3)) * 256 + (((2 * w + ((lane & 3) >> 1)) ^ (((lane >> 2) & 3) << 2)) << 4) + (lane & 1) * 8;
  const int pubo = w * 128 + (lane & 15) * 64 + (lane >> 4) * 16;

  if (!isA) {
    f32x4 acc = {0.f, 0.f, 0.f, 0.f};
#pragma unroll
    for (int s = 0; s < 4; ++s) { wait_landed(L, s); acc = mfma(tfrag(L, s, tl), sfrag(L, lane, kq * 128 + 32 * s), acc); }
    release(L, w, lane, 4);
    if ((lane & 15) < 2) *(LAS f32x4*)(L + L_STG + pubo) = acc;
    cbar(L, lane, bar);
    if (w == 0) {
      lds_wait_ge(L, L_FCLR, 1);
      __builtin_amdgcn_raw_buffer_store_b128(*(const LAS v4u*)(L + L_STG + lane * 16), rs, mybox + BX_MC + lane * 16, 0, AUX_SC1);
    }
  }
  v4u qw[4];
#pragma unroll
  for (int s = 0; s < 4; ++s) { wait_landed(L, offQ + s); qw[s] = tfrag(L, (offQ + s) & 15, tl); }
  release(L, w, lane, offMR);
#pragma unroll 1
  for (int u = 0; u < 2; ++u) {
    const int n = offMR + 2 * u, k0 = ((mu0 + u) & 15) * 64;
    wait_landed(L, n); wait_landed(L, n + 1);
    f32x4 acc = mfma(tfrag(L, n & 15, tl), sfrag(L, lane, k0), f32x4{0.f, 0.f, 0.f, 0.f});
    acc = mfma(tfrag(L, (n + 1) & 15, tl), sfrag(L, lane, k0 + 32), acc);
    release(L, w, lane, n + 2 < offK ? n + 2 : offK + w);
    if ((lane & 15) < 2) *(LAS f32x4*)(L + L_STG + 2048 + u * 1024 + pubo) = acc;
  }
  v4u wf[4];
  if (isA) {
#pragma unroll
    for (int q = 0; q < 4; ++q) { wait_landed(L, offWO + q); wf[q] = tfrag(L, offWO + q, tl); }
    release(L, w, lane, offK + w);
  }
  {
    lds_wait_ge(L, L_FALIVE, 1);
    v4u g[2];
    const int js = (16 + (kq & 1) * 8 + sg) * 8 + (kq >> 1);
    const int off[2] = {box(js) + BX_MC + w * 128 + piece * 16, box(js + 4) + BX_MC + w * 128 + piece * 16};
    gather<2>(g, off, rs);
    const float sa = rbf(tred(asf(g[0]))), ca = rbf(tred(asf(g[1])));
    const float h = rbf(lnx * rbf(1.f + ca)) + sa;
    if (!(lane & 8)) *(LAS u16*)(L + L_HVEC + (piece >> 2) * VROW + (16 * w + 4 * (piece & 3) + comp) * 2) = (u16)pk(h, h);
    cbar(L, lane, bar);
    if (w >= 1 && w <= 2)
      __builtin_amdgcn_raw_buffer_store_b128(*(const LAS v4u*)(L + L_STG + 1024 + w * 1024 + lane * 16), rs, mybox + BX_MR + (w - 1) * 1024 + lane * 16, 0, AUX_SC1);
    f32x4 acc = {0.f, 0.f, 0.f, 0.f};
#pragma unroll
    for (int s = 0; s < 4; ++s) acc = mfma(qw[s], vfrag(L, L_HVEC, lane, s), acc);
    if ((lane & 15) < 2) *(LAS f32x4*)(L + L_STG + 1024 + (lane & 15) * 512 + (16 * w + 4 * (lane >> 4)) * 4) = acc;
    cbar(L, lane, bar);
    if (w == 0) __builtin_amdgcn_raw_buffer_store_b128(*(const LAS v4u*)(L + L_STG + 1024 + lane * 16), rs, mybox + BX_Q + lane * 16, 0, AUX_SC1);
  }
  v4u qf[4], tw[8], tx[8];
  const int kl = (8 * ((lane & 15) >> 2) + (lane & 3)) * 256 + ((lane & 3) << 6) + (lane >> 4) * 16;
  {
    wait_landed(L, offK + w);
    kfrags(L, (offK + w) & 15, kl, tw);
    release(L, w, lane, offK + w + 8);
    wait_landed(L, offK + w + 8);
    kfrags(L, (offK + w + 8) & 15, kl, tx);
    release(L, w, lane, offK + w + 16);
    v4u g[4]; int off[4];
    const int head = hkv * 8 + w;
#pragma unroll
    for (int q = 0; q < 4; ++q) {
      const int jq = ((head & 3) * 8 + 2 * q + (lane >> 5)) * 8 + (head >> 2);
      off[q] = box(jq) + BX_Q + batt * 512 + (lane & 31) * 16;
    }
    gather<4, 8>(g, off, rs);
    const f32x4 t = sum4(g);
    const f32x2 a = swap32(t.x, t.y), b = swap32(t.z, t.w);
    const unsigned p = pk(a.x + a.y, b.x + b.y);
    LAS u16* qv = (LAS u16*)(L + L_QVEC + w * 256 + (lane & 31) * 8 + (lane >> 5) * 2);
    qv[0] = (u16)p; qv[2] = (u16)(p >> 16);
    cbar(L, lane, bar);
#pragma unroll
    for (int s = 0; s < 4; ++s) qf[s] = *(const LAS v4u*)(L + L_QVEC + (lane & 7) * 256 + s * 64 + (lane >> 4) * 16);
  }
  float sc[4][8];
#pragma unroll
  for (int i = 0; i < 4; ++i) {
    v4u kf[8];
    if (i < 2) {
#pragma unroll
      for (int q = 0; q < 8; ++q) kf[q] = i == 0 ? tw[q] : tx[q];
    } else {
      const int n = offK + w + 8 * i;
      wait_landed(L, n);
      kfrags(L, n & 15, kl, kf);
      release(L, w, lane, i < 3 ? n + 8 : offV);
    }
#pragma unroll
    for (int T = 0; T < 2; ++T) {
      f32x4 a = {0.f, 0.f, 0.f, 0.f};
#pragma unroll
      for (int s = 0; s < 4; ++s) a = mfma(kf[T * 4 + s], qf[s], a);
      const f32x2 lo = rbf2(rbf2(f32x2{a.x, a.y}) * 0.08838834764831845f), hi = rbf2(rbf2(f32x2{a.z, a.w}) * 0.08838834764831845f);
      sc[i][T * 4] = lo.x; sc[i][T * 4 + 1] = lo.y; sc[i][T * 4 + 2] = hi.x; sc[i][T * 4 + 3] = hi.y;
    }
  }
  float mw = sc[0][0];
  {
#pragma unroll
    for (int i = 0; i < 4; ++i)
#pragma unroll
      for (int r = 0; r < 8; ++r) mw = fmaxf(mw, sc[i][r]);
    mw = max32(max16(mw));
    float lw = 0.f;
#pragma unroll
    for (int i = 0; i < 4; ++i)
#pragma unroll
      for (int r = 0; r < 8; ++r) { sc[i][r] = exp_(sc[i][r] - mw); lw += sc[i][r]; }
    lw = sum32(sum16(lw));
    if (lane < 8) *(LAS f32x2*)(L + L_MLW + w * 64 + lane * 8) = f32x2{mw, lw};
    lds_inc(L, L_MLCNT, lane);
  }
#pragma unroll
  for (int q = 0; q < VPARK; ++q) {
    wait_landed(L, offV + q);
    if (q < 8) tw[q & 7] = tfrag(L, (offV + q) & 15, tl); else tx[q & 7] = tfrag(L, (offV + q) & 15, tl);
    release(L, w, lane, offV + q + 1);
  }
  lds_wait_ge(L, L_FML, 1);
  {
    const f32x2 ml = *(const LAS f32x2*)(L + L_MLF + (lane & 7) * 8);
    const float f = exp_(mw - ml.x) * ml.y;
#pragma unroll
    for (int i = 0; i < 4; ++i) {
      const v4u p = {pk(sc[i][0] * f, sc[i][1] * f), pk(sc[i][2] * f, sc[i][3] * f), pk(sc[i][4] * f, sc[i][5] * f), pk(sc[i][6] * f, sc[i][7] * f)};
      if ((lane & 15) < 8) *(LAS v4u*)(L + L_P + (w + 8 * i) * 512 + ((lane >> 4) * 8 + (lane & 7)) * 16) = p;
    }
    cbar(L, lane, bar);
  }
  f32x4 oacc = {0.f, 0.f, 0.f, 0.f};
  const int pl = L_P + ((lane >> 4) * 8 + (lane & 7)) * 16;
#pragma unroll
  for (int i = 0; i < VPARK; ++i) oacc = mfma(i < 8 ? tw[i & 7] : tx[i & 7], *(const LAS v4u*)(L + pl + i * 512), oacc);
#pragma unroll 1
  for (int i = VPARK; i < 32; i += 2) {
    const int n = offV + i;
    wait_landed(L, n + 1); wait_landed(L, n);
    const v4u a0 = tfrag(L, n & 15, tl), a1 = tfrag(L, (n + 1) & 15, tl);
    const v4u p0 = *(const LAS v4u*)(L + pl + i * 512), p1 = *(const LAS v4u*)(L + pl + (i + 1) * 512);
    release(L, w, lane, n + 2);
    oacc = mfma(a0, p0, oacc);
    oacc = mfma(a1, p1, oacc);
  }
  if ((lane & 15) < 8) __builtin_amdgcn_raw_buffer_store_b128(asu(oacc), rs, mybox + BX_O + w * 512 + ((lane >> 4) * 8 + (lane & 15)) * 16, 0, AUX_SC1);

  if (isA) {
    v4u g[4]; int off[4];
#pragma unroll
    for (int q = 0; q < 4; ++q) off[q] = box((sg + 8 * q) * 8 + (w >> 2) * 4 + a_hkv) + BX_O + a_dt * 512 + (w & 3) * 128 + piece * 16;
#pragma unroll
    for (int q = 0; q < 4; ++q) { wait_landed(L, offW + q); tw[q] = tfrag(L, (offW + q) & 15, tl); }
    __builtin_amdgcn_s_sleep(4);
    gather<4>(g, off, rs);
    const float t = tred(sum4(g));
    *(LAS u16*)(L + L_TVEC + (w >> 2) * VROW + (16 * piece + 4 * (w & 3) + comp) * 2) = (u16)pk(t, t);
    WGBAR();
    tile_pair(L, L_TVEC, wf, tw, rs, mybox + BX_T1 + w * 128, lane);
  } else if (isC) {
    if (w < 4) {
      const int col = 64 * ci + 16 * w + 4 * (piece & 3) + comp;
      const float xres = lo2f(*(const GAS u16*)(x + (piece >> 2) * D + col)), bias = lo2f(*(const GAS u16*)(b2 + col));
      lds_wait_ge(L, L_FGATE, 1);
      const float ga = *(const LAS float*)(L + L_GATE + w * 256 + lane * 4), gb = *(const LAS float*)(L + L_GATE + (4 + w) * 256 + lane * 4);
      const int ct = 4 * ci + w, cg = ct >> 4, ti = ct & 15;
      v4u g[4]; int off[4], off2[4];
#pragma unroll
      for (int q = 0; q < 4; ++q) {
        const int a = (sg + 8 * q) * 4 + cg;
        off[q] = box((a & 15) * 8 + (a >> 4)) + BX_T1 + ti * 128 + piece * 16;
        const int kf = sg + 8 * q, bb = kf * 4 + cg, gg = bb / 14;
        off2[q] = kf < 28 ? box((16 + bb - gg * 14) * 8 + gg) + BX_T2 + ti * 128 + piece * 16 : WS_BYTES;
        PIN(off2[q]);
      }
      gather<4>(g, off, rs);
      const float x1 = rbf(xres + rbf(ga * rbf(tred(sum4(g)))));
      const f32x4 x4 = texp(x1);
      __builtin_amdgcn_raw_buffer_store_b128(asu(x4), rs, mybox + BX_X1 + sg * 512 + w * 128 + piece * 16, 0, AUX_SC1);
      gather<4>(g, off2, rs);
      const float o = x1 + rbf(gb * rbf(rbf(tred(sum4(g))) + bias));
      if (!(lane & 8)) *(GAS u16*)(out + (piece >> 2) * D + col) = (u16)pk(o, o);
    }
  } else {
    f32x4 sbv, cb1;
    float bias;
    {
      v4u g2[2];
      const int off[2] = {box(30 * 8 + (w >> 1) + (w & 1) * 8) + BX_T1 + lane * 16, box(30 * 8 + 4 + (w >> 1) + (w & 1) * 8) + BX_T1 + lane * 16};
      gather<2>(g2, off, rs);
      sbv = asf(g2[0]);
      const f32x4 cb = asf(g2[1]);
      const f32x2 c0 = rbf2(f32x2{cb.x, cb.y} + 1.f), c1 = rbf2(f32x2{cb.z, cb.w} + 1.f);
      cb1 = f32x4{c0.x, c0.y, c1.x, c1.y};
      bias = lo2f(*(const GAS u16*)(b1 + (bi >> 2) * 128 + 16 * w + 4 * (piece & 3) + comp));
      PIN(bias); PIN(sbv); PIN(cb1);
    }
#pragma unroll
    for (int q = 0; q < 8; ++q) { wait_landed(L, offW + q); tw[q] = tfrag(L, (offW + q) & 15, tl); }
    const int cc = 2 * w + (sg >> 2);
    const int offx[1] = {box((30 + (cc & 1)) * 8 + (cc >> 1)) + BX_X1 + grp * 512 + (sg & 3) * 128 + piece * 16};
    const int kf = bi >> 2, bb = (kf >> 1) * 8 + sg, gg = bb / 14;
    int offh[1] = {box((16 + bb - gg * 14) * 8 + gg) + BX_T1 + ((kf & 1) * 8 + w) * 128 + piece * 16};
    PIN(offh[0]);
    wait_landed(L, offW + 14); wait_landed(L, offW + 15);
    v4u g[1];
    gather<1>(g, offx, rs);
    const f32x4 x1 = asf(g[0]);
    float m, rden;
    stat_put(L, L_ST1, w, lane, x1);
    WGBAR();
    stat_get(L, L_ST1, lane, m, rden);
    const f32x2 ha = rbf2(rbf2(ln2(f32x2{x1.x, x1.y}, m, rden) * f32x2{cb1.x, cb1.y}) + f32x2{sbv.x, sbv.y});
    const f32x2 hb = rbf2(rbf2(ln2(f32x2{x1.z, x1.w}, m, rden) * f32x2{cb1.z, cb1.w}) + f32x2{sbv.z, sbv.w});
    stat_put(L, L_ST2, w, lane, f32x4{ha.x, ha.y, hb.x, hb.y});
    WGBAR();
    if (w == (bi & 7)) {
      stat_get(L, L_ST2, lane, m, rden);
      const f32x2 ya = rbf2(ha - m) * rden, yb = rbf2(hb - m) * rden;
      *(LAS v2u*)(L + L_TVEC + (piece >> 2) * VROW + (16 * sg + 4 * (piece & 3)) * 2) = v2u{pk(ya.x, ya.y), pk(yb.x, yb.y)};
    }
    WGBAR();
    tile_pair(L, L_TVEC, tw, tw + 4, rs, mybox + BX_T1 + w * 128, lane);
#pragma unroll
    for (int q = 0; q < 8; ++q) { wait_landed(L, offW + 8 + q); tw[q] = tfrag(L, (offW + 8 + q) & 15, tl); }
    __builtin_amdgcn_s_sleep(5);
    gather<1>(g, offh, rs);
    const float u = rbf(rbf(tred(asf(g[0]))) + bias);
    const float z = u * (0.7978845608028654f + 0.035677408136300125f * u * u);
    const float hh = u * rcp_(1.f + __builtin_amdgcn_exp2f(-2.8853900817779268f * z));
    *(LAS u16*)(L + L_UVEC + (piece >> 2) * VROW + (16 * w + 4 * (piece & 3) + comp) * 2) = (u16)pk(hh, hh);
    WGBAR();
    tile_pair(L, L_UVEC, tw, tw + 4, rs, mybox + BX_T2 + w * 128, lane);
  }
}

}

extern "C" void kernel_launch(void* const* d_in, const int* in_sizes, int n_in, void* d_out, int out_size, void* d_ws, size_t ws_size,
                              hipStream_t stream) {
  (void)in_sizes; (void)n_in; (void)out_size; (void)ws_size;
  (void)hipFuncSetAttribute((const void*)fused_block, hipFuncAttributeMaxDynamicSharedMemorySize, LDS_BYTES);
  hipLaunchKernelGGL(fused_block, dim3(256), dim3(NTHR), LDS_BYTES, stream, (const u16*)d_in[0], (const u16*)d_in[1], (const u16*)d_in[2],
                     (const u16*)d_in[3], (const u16*)d_in[4], (const u16*)d_in[6], (const u16*)d_in[7], (const u16*)d_in[8],
                     (const u16*)d_in[9], (const u16*)d_in[10], (const u16*)d_in[11], (u16*)d_out, (unsigned char*)d_ws);
}
```
